```python
import torch

def _softmax(x, axis=-1):
    return torch.softmax(x, dim=axis)
def _gelu(x):
    return torch.nn.functional.gelu(x, approximate="tanh")
def _silu(x):
    return torch.nn.functional.silu(x)
def _sigmoid(x):
    return torch.sigmoid(x)
def _layernorm(x, axis=-1, eps=1e-5):
    m = x.mean(dim=axis, keepdim=True)
    v = x.var(dim=axis, keepdim=True, unbiased=False)
    return (x - m) / torch.sqrt(v + eps)
def _scatter_add(tgt, idx, src, axis=0):
    return tgt.clone().index_add_(axis, idx, src)
def _segment_sum(src, seg, n):
    out = torch.zeros((n,) + src.shape[1:], dtype=src.dtype)
    return out.index_add_(0, seg, src)
def _pad(x, pw):
    flat = tuple(v for pair in reversed(pw) for v in pair)
    return torch.nn.functional.pad(x, flat)

kernel_name = 'interleave_layers__ssm_conv_s5__ssm_conv_h3__prompt'

def setup_inputs():

    _w = (lambda a: (lambda t: t.to(torch.bfloat16) if t.is_floating_point() else t)(torch.as_tensor(a)))
    def _A_setup():
        B, S, D = 1, 2048, 576
        P = D
        Lambda_re = -0.5 * (lambda _x: torch.exp(torch.as_tensor(_x)))((lambda *s: torch.randn(*s, dtype=torch.bfloat16) * 0.2)(P))
        Lambda_im = (lambda *s: torch.randn(*s, dtype=torch.bfloat16) * 0.4)(P) * 1.0
        B_re = (lambda *s: torch.randn(*s, dtype=torch.bfloat16) * 0.2)(P, D) * (D ** -0.5)
        B_im = (lambda *s: torch.randn(*s, dtype=torch.bfloat16) * 0.2)(P, D) * (D ** -0.5)
        C_re = (lambda *s: torch.randn(*s, dtype=torch.bfloat16) * 0.2)(D, P) * (P ** -0.5)
        C_im = (lambda *s: torch.randn(*s, dtype=torch.bfloat16) * 0.2)(D, P) * (P ** -0.5)
        log_dt = (lambda *s: torch.randn(*s, dtype=torch.bfloat16) * 0.2)(P) * 0.3 - 2.0
        Dskip = (lambda *s: torch.randn(*s, dtype=torch.bfloat16) * 0.05)(D)
        return {
            "x": _w((lambda *s: torch.randn(*s, dtype=torch.bfloat16) * 0.05)(B, S, D)),
            "Lambda_re": _w(Lambda_re),
            "Lambda_im": _w(Lambda_im),
            "B_re": _w(B_re),
            "B_im": _w(B_im),
            "C_re": _w(C_re),
            "C_im": _w(C_im),
            "log_dt": _w(log_dt),
            "Dskip": _w(Dskip),
        }
    _A_in = _A_setup()

    def _B_setup():
        B, S, D = 1, 2048, 576
        ks = 4
        return {
            "q": _w((lambda *s: torch.randn(*s, dtype=torch.bfloat16) * 0.05)(B, S, D)),
            "k": _w((lambda *s: torch.randn(*s, dtype=torch.bfloat16) * 0.4)(B, S, D)),
            "v": _w((lambda *s: torch.randn(*s, dtype=torch.bfloat16) * 0.4)(B, S, D)),
            "shift_k": _w((lambda *s: torch.randn(*s, dtype=torch.bfloat16) * 0.4)(D, ks) * 0.5),
            "ssm_k": _w((lambda *s: torch.randn(*s, dtype=torch.bfloat16) * 0.4)(D, S) * (1.0 / S ** 0.5)),
            "ssm_D": _w((lambda *s: torch.randn(*s, dtype=torch.bfloat16) * 0.4)(D)),
        }
    _B_in = _B_setup()

    return {
        "A_x": _A_in["x"],
        "A_Lambda_re": _A_in["Lambda_re"],
        "A_Lambda_im": _A_in["Lambda_im"],
        "A_B_re": _A_in["B_re"],
        "A_B_im": _A_in["B_im"],
        "A_C_re": _A_in["C_re"],
        "A_C_im": _A_in["C_im"],
        "A_log_dt": _A_in["log_dt"],
        "A_Dskip": _A_in["Dskip"],
        "B_q": _B_in["q"],
        "B_k": _B_in["k"],
        "B_v": _B_in["v"],
        "B_shift_k": _B_in["shift_k"],
        "B_ssm_k": _B_in["ssm_k"],
        "B_ssm_D": _B_in["ssm_D"],
    }

def reference(A_x, A_Lambda_re, A_Lambda_im, A_B_re, A_B_im, A_C_re, A_C_im, A_log_dt, A_Dskip, B_q, B_k, B_v, B_shift_k, B_ssm_k, B_ssm_D):
    def _A_ref(x, Lambda_re, Lambda_im, B_re, B_im, C_re, C_im, log_dt, Dskip):
        Bsz, L, H = x.shape
        Lam = Lambda_re + 1j * Lambda_im
        Bmat = B_re + 1j * B_im
        Cmat = C_re + 1j * C_im
        dt = torch.exp(log_dt)
        Lam_bar = torch.exp(dt * Lam)
        B_bar = ((Lam_bar - 1.0) / Lam)[:, None] * Bmat
        Bu = torch.einsum("bld,pd->blp", x + 0j, B_bar)
        h = torch.zeros_like(Bu[:, 0, :])
        hs = []
        for t in range(L):
            h = Lam_bar[None, :] * h + Bu[:, t, :]
            hs.append(h)
        hseq = torch.stack(hs, 1)
        y = 2.0 * torch.einsum("blp,dp->bld", hseq, Cmat).real + x * Dskip[None, None, :]
        return y

    def _B_ref(q, k, v, shift_k, ssm_k, ssm_D):
        B, S, D = q.shape
        ks = shift_k.shape[-1]
        k_pad = _pad(k, ((0, 0), (ks - 1, 0), (0, 0)))
        k_shift = torch.zeros_like(k)
        for t in range(ks):
            k_shift = k_shift + k_pad[:, t:t + S, :] * shift_k[:, ks - 1 - t][None, None, :]
        kv = k_shift * v
        L = 2 * S
        kv_pad = _pad(kv, ((0, 0), (0, S), (0, 0)))
        kf_pad = _pad(ssm_k, ((0, 0), (0, S)))
        kv_f = (lambda x, axes=(-2,-1): torch.fft.rfft2(x.float(), dim=axes))(kv_pad[:, None, :, :], axes=(1, 2))
        kf_f = (lambda x, axes=(-2,-1): torch.fft.rfft2(x.float(), dim=axes))(kf_pad[:, None, :], axes=(1, 2))
        kf_f = (lambda x, *d: x.permute(*d))(kf_f, 1, 2, 0)
        y_f = kv_f * kf_f[None, :, :, :]
        y = (lambda x, s, axes=(-2,-1): torch.fft.irfft2(x, s=s, dim=axes).to(torch.bfloat16))(y_f, (1, L), axes=(1, 2))[:, 0, :S, :]
        y = y + kv * ssm_D[None, None, :]
        return q * y

    x = A_x
    x = x + _A_ref(_layernorm(x, axis=-1), A_Lambda_re, A_Lambda_im, A_B_re, A_B_im, A_C_re, A_C_im, A_log_dt, A_Dskip)
    x = x + _B_ref(_layernorm(x, axis=-1), B_k, B_v, B_shift_k, B_ssm_k, B_ssm_D)
    x = x + _A_ref(_layernorm(x, axis=-1), A_Lambda_re, A_Lambda_im, A_B_re, A_B_im, A_C_re, A_C_im, A_log_dt, A_Dskip)
    x = x + _B_ref(_layernorm(x, axis=-1), B_k, B_v, B_shift_k, B_ssm_k, B_ssm_D)
    return x
```

```cpp
#include <hip/hip_runtime.h>
#include <stdint.h>

typedef unsigned short u16;
typedef unsigned int u32;
typedef unsigned long long u64;
typedef __attribute__((ext_vector_type(4))) unsigned int u32x4;
typedef __attribute__((ext_vector_type(2))) unsigned int u32x2;
typedef __attribute__((ext_vector_type(4))) float f32x4;
typedef __attribute__((ext_vector_type(2))) float f32x2;
typedef __attribute__((ext_vector_type(8))) __bf16 bf16x8;
typedef __attribute__((ext_vector_type(2))) __bf16 bf16x2;
typedef __attribute__((ext_vector_type(4))) short s16x4;
typedef __attribute__((address_space(1))) u64 gu64;
typedef __attribute__((address_space(1))) u32 gu32;
typedef __attribute__((address_space(3))) s16x4 lds_s16x4;
typedef __amdgpu_buffer_rsrc_t rsrc_t;

#define DI __device__ __forceinline__

constexpr int S = 2048, D = 576, NC = 26;
constexpr u32 W_FLG = 4096;
constexpr u32 W_E = 32768;
constexpr u32 W_ST0 = W_E + 2 * NC * D * 16;
constexpr u32 W_ST1 = W_ST0 + NC * 9 * 320 * 16;
constexpr u32 W_KVT = 4u << 20;
constexpr u32 W_GT = W_KVT + D * 8192;
constexpr u32 W_H = W_GT + D * 8192;
constexpr u32 W_U3 = W_H + 2 * NC * 36 * 5120;
constexpr u32 W_END = W_U3 + NC * 18 * 5120;
constexpr u32 W_DBG = 64u << 20;
constexpr int L_A = 0;
constexpr int CVSZ = 13056, CV_K1 = 4224, CV_R = 8448;
constexpr int L_CV = 92160;
constexpr int L_HI = L_CV + 2 * CVSZ;
constexpr int L_GI = L_HI + 4 * 5376;
constexpr int L_SP = L_GI + 64 * 168;
constexpr int L_PAR = L_SP + 2560;
constexpr int L_P1 = L_PAR + 1280;
constexpr int L_P2 = L_P1 + 640;
constexpr int L_TOT = L_P2 + 640;

struct Params {
  const u16 *x, *lre, *lim, *bre, *bim, *cre, *cim, *ldt, *dsk, *k, *v, *shk, *sk, *sd;
  float* out;
  unsigned char* ws;
};

DI float lo16(u32 w) { return __uint_as_float(w << 16); }
DI float hi16(u32 w) { return __uint_as_float(w & 0xffff0000u); }
DI float b2f(u16 h) { return __uint_as_float(((u32)h) << 16); }
DI u32 pk(float a, float b) {
  f32x2 v = {a, b};
  return __builtin_bit_cast(u32, __builtin_convertvector(v, bf16x2));
}
DI float rb(float a) { return lo16(pk(a, a)); }
DI f32x4 mma(u32x4 a, u32x4 b, f32x4 c) {
  return __builtin_amdgcn_mfma_f32_16x16x32_bf16(__builtin_bit_cast(bf16x8, a), __builtin_bit_cast(bf16x8, b), c, 0, 0, 0);
}
DI float dot2(u32 a, u32 b, float c) {
  return __builtin_amdgcn_fdot2_f32_bf16(__builtin_bit_cast(bf16x2, a), __builtin_bit_cast(bf16x2, b), c, false);
}
DI void bar() { asm volatile("s_waitcnt lgkmcnt(0)\n\ts_barrier" ::: "memory"); }
DI void drain() { asm volatile("s_waitcnt vmcnt(0)" ::: "memory"); }
DI void cfence() { asm volatile("" ::: "memory"); }
DI u32x4 ldc(rsrc_t r, u32 off) { return __builtin_amdgcn_raw_buffer_load_b128(r, off, 0, 16); }
DI u32x4 ldcs(rsrc_t r, u32 off, u32 soff) { return __builtin_amdgcn_raw_buffer_load_b128(r, off, soff, 16); }
DI void stc(rsrc_t r, u32 off, u32x4 v) { __builtin_amdgcn_raw_buffer_store_b128(v, r, off, 0, 16); }
DI void stc2(rsrc_t r, u32 off, u32x2 v) { __builtin_amdgcn_raw_buffer_store_b64(v, r, off, 0, 16); }
DI u32x4 ldg16(const u16* p) { return *(const u32x4*)p; }
DI u32x2 ldg8(const u16* p) { return *(const u32x2*)p; }
DI u32x2 trread(unsigned char* lds, int off) {
  s16x4 r = __builtin_amdgcn_ds_read_tr16_b64_v4i16((lds_s16x4*)(lds + off));
  return __builtin_bit_cast(u32x2, r);
}
DI void cmul(float ar, float ai, float br, float bi, float& cr, float& ci) {
  float r = ar * br - ai * bi, i = ar * bi + ai * br;
  cr = r; ci = i;
}
DI float red32(float a, float b) {
  auto r = __builtin_amdgcn_permlane32_swap(__float_as_uint(a), __float_as_uint(b), false, false);
  return __uint_as_float(r[0]) + __uint_as_float(r[1]);
}
DI float red16(float a, float b) {
  auto r = __builtin_amdgcn_permlane16_swap(__float_as_uint(a), __float_as_uint(b), false, false);
  return __uint_as_float(r[0]) + __uint_as_float(r[1]);
}

DI void pair16(float a, float& e, float& o) {
  auto r = __builtin_amdgcn_permlane16_swap(__float_as_uint(a), __float_as_uint(a), false, false);
  e = __uint_as_float(r[0]); o = __uint_as_float(r[1]);
}
DI void pair32(float a, float& e, float& o) {
  auto r = __builtin_amdgcn_permlane32_swap(__float_as_uint(a), __float_as_uint(a), false, false);
  e = __uint_as_float(r[0]); o = __uint_as_float(r[1]);
}
DI float sum3(float a, int lane) {
  return a + __int_as_float(__builtin_amdgcn_ds_bpermute(lane * 4 + 80, __float_as_int(a))) +
         __int_as_float(__builtin_amdgcn_ds_bpermute(lane * 4 + 160, __float_as_int(a)));
}
constexpr unsigned SPIN_MAX = 1u << 20;
DI bool spin_next(unsigned& budget) {
  if (budget == 0) return false;
  --budget;
  __builtin_amdgcn_s_sleep(1);
  return true;
}
template <int N>
DI void gather(rsrc_t ws, const u32 (&off)[N], const bool (&need)[N], u32x4 (&v)[N], u32 tA, u32 tB, unsigned& budget) {
  for (;;) {
    cfence();
    bool ok = true;
#pragma unroll
    for (int i = 0; i < N; ++i) v[i] = ldc(ws, off[i]);
#pragma unroll
    for (int i = 0; i < N; ++i) ok &= (!need[i]) | ((v[i].y == tA) & (v[i].w == tB));
    if (__all(ok)) break;
    if (!spin_next(budget)) break;
  }
  cfence();
}
DI void wait_flags(unsigned char* wsp, u32 off, u64 tag, int lane, unsigned& budget) {
  gu64* f = (gu64*)(wsp + off) + (lane < 9 ? lane : 0);
  for (;;) {
    u64 x = __hip_atomic_load(f, __ATOMIC_RELAXED, __HIP_MEMORY_SCOPE_AGENT);
    if (__all(x == tag)) break;
    if (!spin_next(budget)) break;
  }
  __builtin_amdgcn_fence(__ATOMIC_ACQUIRE, "wavefront");
  cfence();
}

#define STAMP(i) do {} while (0)

DI void conv_stage_k(unsigned char* cv, u32x4 kx, u32 knext, int tid) {
  *(u32x4*)(cv + 32 + 16 * tid) = kx;
  u32x4 s;
  s.x = __builtin_amdgcn_alignbit(kx.y, kx.x, 16);
  s.y = __builtin_amdgcn_alignbit(kx.z, kx.y, 16);
  s.z = __builtin_amdgcn_alignbit(kx.w, kx.z, 16);
  s.w = __builtin_amdgcn_alignbit(knext, kx.w, 16);
  *(u32x4*)(cv + CV_K1 + 32 + 16 * tid) = s;
  u32x4 z = {0, 0, 0, 0};
  if (tid == 0) {
    *(u32x4*)(cv) = z; *(u32x4*)(cv + 16) = z; *(u32x4*)(cv + CV_K1) = z;
    u32x4 f = {0, 0, 0, kx.x << 16};
    *(u32x4*)(cv + CV_K1 + 16) = f;
  }
  if (tid >= 64 && tid < 96) *(u32x4*)(cv + CV_R + 4096 + 16 * (tid - 64)) = z;
}
DI void conv_gather(unsigned char* cv, rsrc_t ws, int ch0, int tid, u32 tA, u32 tB, unsigned& budget) {
  u32 off[4]; bool need[4]; u32x4 v[4];
#pragma unroll
  for (int r = 0; r < 4; ++r) { off[r] = W_KVT + ch0 * 8192 + (tid + 256 * r) * 16; need[r] = true; }
  gather<4>(ws, off, need, v, tA, tB, budget);
#pragma unroll
  for (int r = 0; r < 4; ++r) {
    u32x2 w = {v[r].x, v[r].z};
    *(u32x2*)(cv + (r >> 1) * CVSZ + CV_R + ((r & 1) * 256 + tid) * 8) = w;
  }
}
template <int R0, int R1>
DI void conv_mma(unsigned char* cv, int w, int lane, f32x4 (&acc)[8]) {
  const int m = lane & 15, kg = lane >> 4;
  unsigned char* cb = cv + (w >> 1) * CVSZ;
  const int rho0 = (w & 1) * 4;
  const u32* ka = (const u32*)(cb + ((m & 1) ? 0 : CV_K1) + 64 * rho0 + 16 * kg + 4 * ((m + 1) >> 1));
  const unsigned char* rbp = cb + CV_R + 480 + 64 * rho0 - 32 * m + 16 * kg;
#pragma unroll
  for (int r = R0; r < R1; ++r) {
    u32x4 A[8], B[8];
#pragma unroll
    for (int a = 0; a < 8; ++a) {
      const u32* q = ka + 16 * r + 128 * a;
      A[a].x = q[0]; A[a].y = q[1]; A[a].z = q[2]; A[a].w = q[3];
      B[a] = *(const u32x4*)(rbp + 64 * r + 512 * (7 - a));
    }
#pragma unroll
    for (int b = 0; b < 8; ++b)
#pragma unroll
      for (int a = 0; a + b < 8; ++a) acc[a + b] = mma(A[a], B[b], acc[a + b]);
  }
}
DI void conv_finish(unsigned char* lds, unsigned char* cv, rsrc_t ws, int ch0, float sdv, int w, int lane,
                    f32x4 (&acc)[8], u32 tA, u32 tB) {
  const int n = lane & 15, kg = lane >> 4, odd = w & 1;
  unsigned char* box = lds + L_HI + (w ^ 1) * 5376;
#pragma unroll
  for (int j = 0; j < 4; ++j) {
    f32x4 lo = acc[j], hi = acc[4 + j], snd, kp;
#pragma unroll
    for (int e = 0; e < 4; ++e) { snd[e] = odd ? lo[e] : hi[e]; kp[e] = odd ? hi[e] : lo[e]; }
    *(f32x4*)(box + j * 1024 + lane * 16) = snd;
    acc[j] = kp;
  }
  bar();
  const unsigned char* mine = lds + L_HI + w * 5376;
  const unsigned char* cb = cv + (w >> 1) * CVSZ + CV_R;
#pragma unroll
  for (int j = 0; j < 4; ++j) {
    f32x4 y = acc[j] + *(const f32x4*)(mine + j * 1024 + lane * 16);
    const int X = 256 * (4 * odd + j) + 16 * n + 4 * kg;
    u32x2 rv = *(const u32x2*)(cb + (2044 - X) * 2);
    u32 y01 = pk(y.x, y.y), y23 = pk(y.z, y.w);
    u32 s01 = pk(hi16(rv.y) * sdv, lo16(rv.y) * sdv), s23 = pk(hi16(rv.x) * sdv, lo16(rv.x) * sdv);
    u32x4 o;
    o.x = pk(lo16(y01) + lo16(s01), hi16(y01) + hi16(s01)); o.y = tA;
    o.z = pk(lo16(y23) + lo16(s23), hi16(y23) + hi16(s23)); o.w = tB;
    stc(ws, W_GT + (ch0 + (w >> 1)) * 8192 + X * 4, o);
  }
}

struct Scan {
  float ar, ai, cr, ci, a20r, a20i, a40r, a40i, aqr, aqi, bqr, bqi, a320r, a320i;
};

DI void gemm_bu(const unsigned char* lds, u32x4 (&Bw)[18][2], int lane, f32x4 (&acc)[5][2]) {
#pragma unroll
  for (int i = 0; i < 5; ++i) { acc[i][0] = f32x4{0, 0, 0, 0}; acc[i][1] = f32x4{0, 0, 0, 0}; }
  int b0 = L_A + lane * 16, b1 = b0 + 46080;
  asm volatile("" : "+v"(b0), "+v"(b1));
#pragma unroll
  for (int ks = 0; ks < 18; ++ks) {
    asm("" : "+a"(Bw[ks][0]), "+a"(Bw[ks][1]));
#pragma unroll
    for (int i = 0; i < 5; ++i) {
      u32x4 a = *(const u32x4*)(lds + (ks < 9 ? b0 : b1) + ((ks % 9) * 5 + i) * 1024);
      acc[i][0] = mma(a, Bw[ks][0], acc[i][0]);
      acc[i][1] = mma(a, Bw[ks][1], acc[i][1]);
    }
  }
}
DI void scan_a(const Scan& sc, const f32x4 (&acc)[5][2], rsrc_t ws, u32 eoff, int lane, u32 tA, u32 tB, float& inr, float& ini) {
  float er = 0.f, ei = 0.f;
#pragma unroll
  for (int i = 0; i < 5; ++i)
#pragma unroll
    for (int r = 0; r < 4; ++r) {
      float nr = sc.ar * er + (acc[i][0][r] - sc.ai * ei), ni = sc.ar * ei + (acc[i][1][r] + sc.ai * er);
      er = nr; ei = ni;
    }
  const int q = lane >> 4;
  float e0r, e1r, e0i, e1i, pr, pi, p0r, p1r, p0i, p1i, tr, ti;
  pair16(er, e0r, e1r); pair16(ei, e0i, e1i);
  cmul(sc.a20r, sc.a20i, e0r, e0i, pr, pi);
  pr += e1r; pi += e1i;
  pair32(pr, p0r, p1r); pair32(pi, p0i, p1i);
  cmul(sc.a40r, sc.a40i, p0r, p0i, tr, ti);
  if (q == 0) { u32x4 o = {__float_as_uint(tr + p1r), tA, __float_as_uint(ti + p1i), tB}; stc(ws, eoff, o); }
  cmul(sc.a20r, sc.a20i, p0r, p0i, tr, ti);
  inr = q == 0 ? 0.f : q == 1 ? e0r : q == 2 ? p0r : tr + e0r;
  ini = q == 0 ? 0.f : q == 1 ? e0i : q == 2 ? p0i : ti + e0i;
}
DI void scan_b(const Scan& sc, const f32x4 (&acc)[5][2], unsigned char* lds, rsrc_t ws, u32 ebase, u32 hoff, int c, int w,
               int lane, u32 tA, u32 tB, float inr, float ini, unsigned& budget) {
  const int n = lane & 15, q = lane >> 4;
  u32 off[7]; bool need[7]; u32x4 v[7];
#pragma unroll
  for (int j = 0; j < 7; ++j) {
    int cp = c - 1 - q - 4 * j;
    need[j] = cp >= 0;
    off[j] = ebase + (cp >= 0 ? cp : 0) * (D * 16);
  }
  gather<7>(ws, off, need, v, tA, tB, budget);
  float hr = 0.f, hi = 0.f;
#pragma unroll
  for (int j = 6; j >= 0; --j) {
    cmul(sc.a320r, sc.a320i, hr, hi, hr, hi);
    hr += need[j] ? __uint_as_float(v[j].x) : 0.f;
    hi += need[j] ? __uint_as_float(v[j].z) : 0.f;
  }
  cmul(sc.bqr, sc.bqi, hr, hi, hr, hi);
  hr = red16(hr, hr); hi = red16(hi, hi);
  hr = red32(hr, hr); hi = red32(hi, hi);
  cmul(sc.aqr, sc.aqi, hr, hi, hr, hi);
  hr += inr; hi += ini;
  unsigned char* img = lds + L_HI + w * 5376;
#pragma unroll
  for (int i = 0; i < 5; ++i) {
    float o_r[4], o_i[4];
#pragma unroll
    for (int r = 0; r < 4; ++r) {
      float nr = sc.ar * hr + (acc[i][0][r] - sc.ai * hi), ni = sc.ar * hi + (acc[i][1][r] + sc.ai * hr);
      hr = nr; hi = ni;
      o_r[r] = sc.cr * hr - sc.ci * hi;
      o_i[r] = -(sc.cr * hi) - sc.ci * hr;
    }
    *(u32x2*)(img + n * 168 + 40 * q + 8 * i) = u32x2{pk(o_r[0], o_r[1]), pk(o_r[2], o_r[3])};
    *(u32x2*)(img + (16 + n) * 168 + 40 * q + 8 * i) = u32x2{pk(o_i[0], o_i[1]), pk(o_i[2], o_i[3])};
  }
  asm volatile("s_waitcnt lgkmcnt(0)" ::: "memory");
  const int tro = L_HI + w * 5376 + (8 * q + (n >> 2)) * 168 + 8 * (n & 3);
#pragma unroll
  for (int i = 0; i < 5; ++i) {
    u32x2 a = trread(lds, tro + 32 * i), b = trread(lds, tro + 4 * 168 + 32 * i);
    stc(ws, hoff + i * 1024 + lane * 16, u32x4{a.x, a.y, b.x, b.y});
  }
}
DI void gemm_y(unsigned char* lds, rsrc_t ws, u32 hbase, u32x4 (&Cw)[9][4], int w, int g, int lane, f32x4 (&y)[5]) {
  f32x4 acc[4][5];
#pragma unroll
  for (int d = 0; d < 4; ++d)
#pragma unroll
    for (int i = 0; i < 5; ++i) acc[d][i] = d ? f32x4{0, 0, 0, 0} : y[i];
  constexpr int PF = 3;
  u32x4 hf[PF][5];
  const u32 ho = hbase + w * 9 * 5120 + lane * 16;
#pragma unroll
  for (int j = 0; j < PF; ++j)
#pragma unroll
    for (int i = 0; i < 5; ++i) hf[j][i] = ldcs(ws, ho + i * 1024, ((j + g) % 9) * 5120);
#pragma unroll
  for (int j = 0; j < 9; ++j) {
    if (j < 8) asm("" : "+a"(Cw[j][0]));
#pragma unroll
    for (int i = 0; i < 5; ++i) {
      u32x4 b = hf[j % PF][i];
#pragma unroll
      for (int d = 3; d >= 0; --d) acc[d][i] = mma(Cw[j][d], b, acc[d][i]);
      if (j + PF < 9) hf[j % PF][i] = ldcs(ws, ho + i * 1024, ((j + PF + g) % 9) * 5120);
      __builtin_amdgcn_sched_barrier(0);
    }
  }
#pragma unroll
  for (int d = 1; d < 4; ++d)
#pragma unroll
    for (int i = 0; i < 5; ++i)
      *(f32x4*)(lds + L_A + ((((w + d) & 3) * 3 + d - 1) * 5 + i) * 1024 + lane * 16) = acc[d][i];
  bar();
#pragma unroll
  for (int i = 0; i < 5; ++i) {
    const unsigned char* b = lds + L_A + (w * 15 + i) * 1024 + lane * 16;
    y[i] = (acc[0][i] + *(const f32x4*)(b)) + (*(const f32x4*)(b + 5120) + *(const f32x4*)(b + 10240));
  }
}
__global__ void __launch_bounds__(256, 1) fused_kernel(Params p) {
  extern __shared__ __attribute__((aligned(16))) unsigned char lds[];
  int tid = threadIdx.x, lane = tid & 63;
  const int w = __builtin_amdgcn_readfirstlane(tid >> 6);
  const int xcd = blockIdx.x & 7, slot = blockIdx.x >> 3;
  int c, g, spare = -1;
  if (slot < 27) { c = xcd * 3 + slot / 9; g = slot % 9; }
  else {
    int e = (xcd & 3) * 5 + (slot - 27);
    if (xcd < 4) { int e2 = (xcd & 1) * 5 + (slot - 27); if (e2 >= 9) return; c = 24 + (xcd >> 1); g = e2; }
    else { if (e >= 18) return; spare = e; c = 0; g = e >> 1; }
  }
  STAMP(0);
  const rsrc_t ws = __builtin_amdgcn_make_buffer_rsrc(p.ws, 0, W_END, 0x00020000);
  const u32 tA = __hip_atomic_load((gu32*)p.ws, __ATOMIC_RELAXED, __HIP_MEMORY_SCOPE_AGENT) + 1u;
  const u32 tB = tA * 0x9E3779B1u + 0x7F4A7C15u;
  const u64 tF = ((u64)tB << 32) | tA;
  unsigned budget = SPIN_MAX;
  int n = lane & 15, kg = lane >> 4;
#define PHASE() do { asm volatile("" : "+v"(tid)); lane = tid & 63; n = lane & 15; kg = lane >> 4; pch = 64 * g + 16 * w + n; } while (0)

  if (spare >= 0) {
    const int ch0 = 64 * g + 52 + 6 * (spare & 1);
#pragma unroll
    for (int i = 0; i < 6; ++i) {
      const u16* kp = p.sk + (ch0 + i) * S + 8 * tid;
      u32x4 kx = ldg16(kp);
      u32 kn = tid < 255 ? kp[8] : 0;
      conv_stage_k(lds + i * CVSZ, kx, kn, tid);
    }
#pragma unroll
    for (int r = 0; r < 3; ++r) conv_gather(lds + 2 * r * CVSZ, ws, ch0 + 2 * r, tid, tA, tB, budget);
#pragma unroll 1
    for (int r = 0; r < 3; ++r) {
      float sdv = b2f(p.sd[ch0 + 2 * r + (w >> 1)]);
      bar();
      f32x4 acc[8];
#pragma unroll
      for (int j = 0; j < 8; ++j) acc[j] = f32x4{0, 0, 0, 0};
      conv_mma<0, 4>(lds + 2 * r * CVSZ, w, lane, acc);
      conv_finish(lds, lds + 2 * r * CVSZ, ws, ch0 + 2 * r, sdv, w, lane, acc, tA, tB);
    }
    STAMP(15);
    return;
  }

  const int t0 = 80 * c;
  u32x2 kk[8], vv[5];
  u32x4 shw[2];
  {
    const int col = 64 * g + 4 * (tid & 15), r0 = t0 + 5 * (tid >> 4);
#pragma unroll
    for (int j = 0; j < 8; ++j) { int t = r0 + j - 3; t = t < 0 ? 0 : (t > S - 1 ? S - 1 : t); kk[j] = ldg8(p.k + t * D + col); }
#pragma unroll
    for (int j = 0; j < 5; ++j) { int t = r0 + j; t = t > S - 1 ? S - 1 : t; vv[j] = ldg8(p.v + t * D + col); }
    shw[0] = ldg16(p.shk + col * 4); shw[1] = ldg16(p.shk + col * 4 + 8);
  }
  const int ks0 = w < 2 ? 5 * w : 4 * w + 2;
  u32x4 xs[5][5];
  {
#pragma unroll
    for (int i = 0; i < 5; ++i) {
      int t = t0 + 20 * (n >> 2) + 4 * i + (n & 3); t = t > S - 1 ? S - 1 : t;
      const u16* xp = p.x + t * D + 32 * ks0 + 8 * kg;
#pragma unroll
      for (int j = 0; j < 4; ++j) xs[j][i] = ldg16(xp + 32 * j);
      xs[4][i] = ldg16(xp + (w < 2 ? 128 : 0));
    }
  }
  const int ch0 = 64 * g + 2 * c;
  u32x4 kx0 = ldg16(p.sk + ch0 * S + 8 * tid), kx1 = ldg16(p.sk + (ch0 + 1) * S + 8 * tid);
  u32 kn0 = tid < 255 ? p.sk[ch0 * S + 8 * tid + 8] : 0, kn1 = tid < 255 ? p.sk[(ch0 + 1) * S + 8 * tid + 8] : 0;
  int pch = 64 * g + 16 * w + n;
  const u16 lre_h = p.lre[pch], lim_h = p.lim[pch], ldt_h = p.ldt[pch], sd_h = p.sd[ch0 + (w >> 1)];
  u32x2 dsk_h = ldg8(p.dsk + 64 * g + 16 * w + 4 * kg);
  __builtin_amdgcn_sched_barrier(0);
  u32x4 Bw[18][2];
#pragma unroll
  for (int ks = 0; ks < 18; ++ks) {
    Bw[ks][0] = ldg16(p.bre + pch * D + 32 * ks + 8 * kg);
    Bw[ks][1] = ldg16(p.bim + pch * D + 32 * ks + 8 * kg);
  }
  __builtin_amdgcn_sched_barrier(0);
  {
    const int r0 = 5 * (tid >> 4);
    const bool first = (t0 + r0 == 0);
    u16* img = (u16*)(lds + L_GI) + 4 * (tid & 15) * 84 + r0;
#pragma unroll
    for (int h = 0; h < 2; ++h) {
      float sa[4], sb[4], ka[8], kb[8];
      sa[0] = lo16(shw[h].x); sa[1] = hi16(shw[h].x); sa[2] = lo16(shw[h].y); sa[3] = hi16(shw[h].y);
      sb[0] = lo16(shw[h].z); sb[1] = hi16(shw[h].z); sb[2] = lo16(shw[h].w); sb[3] = hi16(shw[h].w);
#pragma unroll
      for (int j = 0; j < 8; ++j) {
        u32 q = kk[j][h];
        if (j < 3) q = first ? 0u : q;
        ka[j] = lo16(q); kb[j] = hi16(q);
      }
#pragma unroll
      for (int r = 0; r < 5; ++r) {
        u32 a = pk(ka[r] * sa[3], kb[r] * sb[3]);
#pragma unroll
        for (int j = 1; j < 4; ++j) {
          u32 m2 = pk(ka[r + j] * sa[3 - j], kb[r + j] * sb[3 - j]);
          a = pk(lo16(a) + lo16(m2), hi16(a) + hi16(m2));
        }
        a = pk(lo16(a) * lo16(vv[r][h]), hi16(a) * hi16(vv[r][h]));
        img[(2 * h) * 84 + r] = (u16)a;
        img[(2 * h + 1) * 84 + r] = (u16)(a >> 16);
      }
    }
  }
  {
    const u32 ones = 0x3f803f80u;
#pragma unroll
    for (int i = 0; i < 5; ++i) {
      float s1 = 0.f, s2 = 0.f;
#pragma unroll
      for (int e = 0; e < 4; ++e) { s1 = dot2(xs[4][i][e], ones, s1); s2 = dot2(xs[4][i][e], xs[4][i][e], s2); }
      if (w >= 2) { s1 = 0.f; s2 = 0.f; }
#pragma unroll
      for (int j = 0; j < 4; ++j) {
        *(u32x4*)(lds + L_A + ((ks0 + j) * 5 + i) * 1024 + lane * 16) = xs[j][i];
#pragma unroll
        for (int e = 0; e < 4; ++e) { s1 = dot2(xs[j][i][e], ones, s1); s2 = dot2(xs[j][i][e], xs[j][i][e], s2); }
      }
      *(u32x4*)(lds + L_A + ((ks0 + (w < 2 ? 4 : 0)) * 5 + i) * 1024 + lane * 16) = xs[4][i];
      float s = red32(s1, s2);
      s = red16(s, s);
      if (!(kg & 1)) *(float*)(lds + L_SP + (w * 80 + i * 16 + n) * 8 + (kg >> 1) * 4) = s;
    }
  }
  conv_stage_k(lds + L_CV, kx0, kn0, tid);
  conv_stage_k(lds + L_CV + CVSZ, kx1, kn1, tid);
  bar();
  STAMP(1);
#pragma unroll
  for (int r = 0; r < 5; ++r) {
    const int it = tid + 256 * r, ch = it / 20, tq = it - 20 * ch;
    u32x2 q = *(const u32x2*)(lds + L_GI + ch * 168 + tq * 8);
    u32x4 o = {__builtin_amdgcn_alignbit(q.y, q.y, 16), tA, __builtin_amdgcn_alignbit(q.x, q.x, 16), tB};
    const int gq = 20 * c + tq;
    if (gq < 512) stc(ws, W_KVT + (64 * g + ch) * 8192 + (511 - gq) * 16, o);
  }
  if (tid < 80) {
    float s1 = 0.f, s2 = 0.f;
#pragma unroll
    for (int q = 0; q < 4; ++q) { f32x2 v = *(const f32x2*)(lds + L_SP + (q * 80 + tid) * 8); s1 += v.x; s2 += v.y; }
    float m = s1 * (1.f / D), var = fmaxf(s2 * (1.f / D) - m * m, 0.f);
    float den = rb(sqrtf(rb(rb(var) + 1e-5f)));
    const int mm = tid & 15, tl = 20 * (mm >> 2) + 4 * (tid >> 4) + (mm & 3);
    *(f32x2*)(lds + L_P1 + tl * 8) = f32x2{1.f / den, rb(m)};
  }
  Scan sc;
  {
    float lr = b2f(lre_h), li = b2f(lim_h), dt = rb(expf(b2f(ldt_h)));
    float e = expf(dt * lr), sn, cs;
    sincosf(dt * li, &sn, &cs);
    sc.ar = e * cs; sc.ai = e * sn;
    float nr = sc.ar - 1.f, ni = sc.ai, dn = lr * lr + li * li;
    sc.cr = 2.f * ((nr * lr + ni * li) / dn); sc.ci = 2.f * ((ni * lr - nr * li) / dn);
    float r2, i2, r4, i4, r5, i5, r80, i80, r160, i160, r60, i60, r240, i240;
    cmul(sc.ar, sc.ai, sc.ar, sc.ai, r2, i2); cmul(r2, i2, r2, i2, r4, i4); cmul(r4, i4, sc.ar, sc.ai, r5, i5);
    cmul(r5, i5, r5, i5, r2, i2); cmul(r2, i2, r2, i2, sc.a20r, sc.a20i);
    cmul(sc.a20r, sc.a20i, sc.a20r, sc.a20i, sc.a40r, sc.a40i);
    cmul(sc.a40r, sc.a40i, sc.a20r, sc.a20i, r60, i60);
    cmul(sc.a40r, sc.a40i, sc.a40r, sc.a40i, r80, i80);
    cmul(r80, i80, r80, i80, r160, i160); cmul(r160, i160, r80, i80, r240, i240);
    cmul(r160, i160, r160, i160, sc.a320r, sc.a320i);
    sc.aqr = kg == 0 ? 1.f : kg == 1 ? sc.a20r : kg == 2 ? sc.a40r : r60;
    sc.aqi = kg == 0 ? 0.f : kg == 1 ? sc.a20i : kg == 2 ? sc.a40i : i60;
    sc.bqr = kg == 0 ? 1.f : kg == 1 ? r80 : kg == 2 ? r160 : r240;
    sc.bqi = kg == 0 ? 0.f : kg == 1 ? i80 : kg == 2 ? i160 : i240;
  }
  f32x4 acc[5][2];
  gemm_bu(lds, Bw, lane, acc);
  float rsr, rsi;
  {
    f32x4 a0 = {0, 0, 0, 0}, a1 = {0, 0, 0, 0};
    const u32x4 ones = {0x3f803f80u, 0x3f803f80u, 0x3f803f80u, 0x3f803f80u};
#pragma unroll
    for (int ks = 0; ks < 18; ++ks) { a0 = mma(ones, Bw[ks][0], a0); a1 = mma(ones, Bw[ks][1], a1); }
    rsr = a0.x; rsi = a1.x;
  }
  __builtin_amdgcn_sched_barrier(0);
  u32x4 Cw[9][4];
  {
    const u16* cp = (kg < 2 ? p.cre : p.cim) + (64 * g + n) * D + 144 * w + 8 * (kg & 1);
#pragma unroll
    for (int d = 0; d < 4; ++d)
#pragma unroll
      for (int j = 0; j < 9; ++j) Cw[j][d] = ldg16(cp + (((w + d) & 3) * 16) * D + 16 * ((j + g) % 9));
  }
  __builtin_amdgcn_sched_barrier(0);
  bar();
  STAMP(2);
#pragma unroll
  for (int i = 0; i < 5; ++i) {
    f32x4 pa = *(const f32x4*)(lds + L_P1 + (20 * kg + 4 * i) * 8), pb = *(const f32x4*)(lds + L_P1 + (20 * kg + 4 * i) * 8 + 16);
    acc[i][0].x = (acc[i][0].x - pa.y * rsr) * pa.x; acc[i][1].x = (acc[i][1].x - pa.y * rsi) * pa.x;
    acc[i][0].y = (acc[i][0].y - pa.w * rsr) * pa.z; acc[i][1].y = (acc[i][1].y - pa.w * rsi) * pa.z;
    acc[i][0].z = (acc[i][0].z - pb.y * rsr) * pb.x; acc[i][1].z = (acc[i][1].z - pb.y * rsi) * pb.x;
    acc[i][0].w = (acc[i][0].w - pb.w * rsr) * pb.z; acc[i][1].w = (acc[i][1].w - pb.w * rsi) * pb.z;
  }
  float inr, ini;
  scan_a(sc, acc, ws, W_E + (c * D + pch) * 16, lane, tA, tB, inr, ini);
  STAMP(3);
  conv_gather(lds + L_CV, ws, ch0, tid, tA, tB, budget);
  bar();
  STAMP(4);
  f32x4 cacc[8];
  {
#pragma unroll
    for (int j = 0; j < 8; ++j) cacc[j] = f32x4{0, 0, 0, 0};
    conv_mma<0, 2>(lds + L_CV, w, lane, cacc);
    STAMP(5);
    scan_b(sc, acc, lds, ws, W_E + pch * 16, W_H + (c * 36 + 4 * g + w) * 5120, c, w, lane, tA, tB, inr, ini, budget);
    drain();
    bar();
    if (tid == 0) __hip_atomic_store((gu64*)(p.ws + W_FLG + c * 128) + g, tF, __ATOMIC_RELAXED, __HIP_MEMORY_SCOPE_AGENT);
    STAMP(6);
    conv_mma<2, 4>(lds + L_CV, w, lane, cacc);
  }
  float dk0 = lo16(dsk_h.x), dk1 = hi16(dsk_h.x), dk2 = lo16(dsk_h.y), dk3 = hi16(dsk_h.y);
  f32x4 z[5];
  auto frag_off = [&](int i) {
    const int tl = 16 * i + n, q = tl / 20, rem = tl - 20 * q;
    return (((2 * g + (w >> 1)) * 5 + (rem >> 2)) * 64 + (2 * (w & 1) + (kg >> 1)) * 16 + 4 * q + (rem & 3)) * 16 + (kg & 1) * 8;
  };
#pragma unroll
  for (int i = 0; i < 5; ++i) {
    u32x2 xq = *(const u32x2*)(lds + L_A + frag_off(i));
    f32x2 pr = *(const f32x2*)(lds + L_P1 + (16 * i + n) * 8);
    float d0 = lo16(xq.x) - pr.y, d1 = hi16(xq.x) - pr.y, d2 = lo16(xq.y) - pr.y, d3 = hi16(xq.y) - pr.y;
    u32 a = pk(d0, d1), b = pk(d2, d3);
    a = pk(lo16(a) * pr.x, hi16(a) * pr.x); b = pk(lo16(b) * pr.x, hi16(b) * pr.x);
    a = pk(lo16(a) * dk0, hi16(a) * dk1); b = pk(lo16(b) * dk2, hi16(b) * dk3);
    z[i] = f32x4{d0 + lo16(a), d1 + hi16(a), d2 + lo16(b), d3 + hi16(b)};
  }
  conv_finish(lds, lds + L_CV, ws, ch0, b2f(sd_h), w, lane, cacc, tA, tB);
  *(f32x4*)(lds + L_CV + tid * 16) = f32x4{sc.ar, sc.ai, sc.cr, sc.ci};
  *(f32x4*)(lds + L_CV + 4096 + tid * 16) = f32x4{sc.a20r, sc.a20i, sc.a40r, sc.a40i};
  *(f32x4*)(lds + L_CV + CVSZ + tid * 16) = f32x4{sc.aqr, sc.aqi, sc.bqr, sc.bqi};
  *(f32x2*)(lds + L_CV + CVSZ + 4096 + tid * 8) = f32x2{sc.a320r, sc.a320i};
  STAMP(7);
  PHASE();
  wait_flags(p.ws, W_FLG + c * 128, tF, lane, budget);
  STAMP(8);
  u32 gq[5][2];
  {
    gemm_y(lds, ws, W_H + c * 36 * 5120, Cw, w, g, lane, z);
    STAMP(9);
    PHASE();
    u32 off[5]; bool need[5]; u32x4 v[5];
#pragma unroll
    for (int r = 0; r < 5; ++r) {
      const int it = lane + 64 * r, ch = it / 20, tq = it - 20 * ch, q = 20 * c + tq;
      need[r] = q < 512;
      off[r] = W_GT + (64 * g + 16 * w + ch) * 8192 + (q < 512 ? q : 511) * 16;
    }
    gather<5>(ws, off, need, v, tA, tB, budget);
#pragma unroll
    for (int r = 0; r < 5; ++r) {
      const int it = lane + 64 * r, ch = it / 20, tq = it - 20 * ch;
      *(u32x2*)(lds + L_GI + (16 * w + ch) * 168 + tq * 8) = u32x2{v[r].x, v[r].z};
    }
    asm volatile("s_waitcnt lgkmcnt(0)" ::: "memory");
#pragma unroll
    for (int i = 0; i < 5; ++i) {
      u32x2 t = trread(lds, L_GI + (16 * w + 4 * kg + (n >> 2)) * 168 + 8 * (n & 3) + 32 * i);
      gq[i][0] = t.x; gq[i][1] = t.y;
    }
  }
  STAMP(10);
#pragma unroll
  for (int i = 0; i < 5; ++i) {
    float gv[4] = {lo16(gq[i][0]), hi16(gq[i][0]), lo16(gq[i][1]), hi16(gq[i][1])};
    float s[8] = {0, 0, 0, 0, 0, 0, 0, 0};
#pragma unroll
    for (int e = 0; e < 4; ++e) {
      float zz = z[i][e], gg = gv[e], zg = zz * gg;
      s[0] += zz; s[1] += zz * zz; s[2] += gg; s[3] += zg; s[4] += gg * gg; s[5] += zg * gg; s[6] += zg * zz; s[7] += zg * zg;
    }
    float a0 = red32(s[0], s[4]), a1 = red32(s[1], s[5]), a2 = red32(s[2], s[6]), a3 = red32(s[3], s[7]);
    *(f32x2*)(lds + L_HI + (w * 320 + kg * 80 + 16 * i + n) * 8) = f32x2{red16(a0, a2), red16(a1, a3)};
  }
  bar();
#pragma unroll
  for (int r = 0; r < 2; ++r) {
    const int it = tid + 256 * r;
    if (it < 320) {
      f32x2 a = *(const f32x2*)(lds + L_HI + it * 8), b = *(const f32x2*)(lds + L_HI + (320 + it) * 8);
      f32x2 e = *(const f32x2*)(lds + L_HI + (640 + it) * 8), f = *(const f32x2*)(lds + L_HI + (960 + it) * 8);
      a = (a + b) + (e + f);
      stc(ws, W_ST0 + ((c * 9 + g) * 320 + it) * 16, u32x4{__float_as_uint(a.x), tA, __float_as_uint(a.y), tB});
    }
  }
  {
    const int l3 = lane < 60 ? lane : 0, th = l3 / 20, row = 20 * w + l3 - 20 * th;
    u32 off[12]; bool need[12]; u32x4 v[12];
#pragma unroll
    for (int j = 0; j < 12; ++j) { off[j] = W_ST0 + ((c * 9 + 3 * th + (j >> 2)) * 320 + (j & 3) * 80 + row) * 16; need[j] = true; }
    gather<12>(ws, off, need, v, tA, tB, budget);
    float s[8];
#pragma unroll
    for (int u = 0; u < 4; ++u) {
      s[2 * u] = __uint_as_float(v[u].x) + __uint_as_float(v[4 + u].x) + __uint_as_float(v[8 + u].x);
      s[2 * u + 1] = __uint_as_float(v[u].z) + __uint_as_float(v[4 + u].z) + __uint_as_float(v[8 + u].z);
    }
#pragma unroll
    for (int u = 0; u < 8; ++u) s[u] = sum3(s[u], lane) * (1.f / D);
    const float mu = s[0], r2 = 1.f / sqrtf(fmaxf(s[1] - mu * mu, 0.f) + 1e-5f);
    const float M = mu + r2 * (s[3] - mu * s[2]);
    const float q2 = s[1] + 2.f * r2 * (s[6] - mu * s[3]) + r2 * r2 * (s[7] - 2.f * mu * s[5] + mu * mu * s[4]);
    const float r3 = 1.f / sqrtf(fmaxf(q2 - M * M, 0.f) + 1e-5f);
    if (lane < 20) *(f32x4*)(lds + L_PAR + row * 16) = f32x4{mu, r2, M, r3};
  }
  bar();
  STAMP(11);
  asm volatile("" : "+v"(dsk_h));
  dk0 = lo16(dsk_h.x); dk1 = hi16(dsk_h.x); dk2 = lo16(dsk_h.y); dk3 = hi16(dsk_h.y);
#pragma unroll
  for (int i = 0; i < 5; ++i) {
    f32x4 pr = *(const f32x4*)(lds + L_PAR + (16 * i + n) * 16);
    float gv[4] = {lo16(gq[i][0]), hi16(gq[i][0]), lo16(gq[i][1]), hi16(gq[i][1])};
    float u[4];
#pragma unroll
    for (int e = 0; e < 4; ++e) {
      float x2 = z[i][e] + (z[i][e] - pr.x) * pr.y * gv[e] - pr.z;
      u[e] = x2 * pr.w;
      z[i][e] = x2;
    }
    stc2(ws, W_U3 + c * 92160 + frag_off(i), u32x2{pk(u[0], u[1]), pk(u[2], u[3])});
    z[i] += f32x4{u[0] * dk0, u[1] * dk1, u[2] * dk2, u[3] * dk3};
  }
  drain();
  bar();
  if (tid == 0) __hip_atomic_store((gu64*)(p.ws + W_FLG + (NC + c) * 128) + g, tF, __ATOMIC_RELAXED, __HIP_MEMORY_SCOPE_AGENT);
  wait_flags(p.ws, W_FLG + (NC + c) * 128, tF, lane, budget);
  STAMP(12);
  PHASE();
  {
    u32x4 us[23];
    const u32 ub = W_U3 + c * 92160 + tid * 16;
#pragma unroll
    for (int j = 0; j < 22; ++j) us[j] = ldc(ws, ub + j * 4096);
    us[22] = ldc(ws, ub + (w < 2 ? 22 : 0) * 4096);
#pragma unroll
    for (int j = 0; j < 22; ++j) *(u32x4*)(lds + L_A + j * 4096 + tid * 16) = us[j];
    *(u32x4*)(lds + L_A + (w < 2 ? 22 : 0) * 4096 + tid * 16) = us[22];
  }
  bar();
  STAMP(13);
#pragma unroll
  for (int i = 0; i < 5; ++i) asm volatile("" : "+v"(gq[i][0]), "+v"(gq[i][1]));
  gemm_bu(lds, Bw, lane, acc);
  PHASE();
  {
    f32x4 a = *(const f32x4*)(lds + L_CV + tid * 16), b = *(const f32x4*)(lds + L_CV + 4096 + tid * 16);
    f32x4 e = *(const f32x4*)(lds + L_CV + CVSZ + tid * 16);
    f32x2 f = *(const f32x2*)(lds + L_CV + CVSZ + 4096 + tid * 8);
    sc.ar = a.x; sc.ai = a.y; sc.cr = a.z; sc.ci = a.w; sc.a20r = b.x; sc.a20i = b.y; sc.a40r = b.z; sc.a40i = b.w;
    sc.aqr = e.x; sc.aqi = e.y; sc.bqr = e.z; sc.bqi = e.w; sc.a320r = f.x; sc.a320i = f.y;
  }
  scan_a(sc, acc, ws, W_E + ((NC + c) * D + pch) * 16, lane, tA, tB, inr, ini);
  STAMP(14);
  scan_b(sc, acc, lds, ws, W_E + (NC * D + pch) * 16, W_H + ((NC + c) * 36 + 4 * g + w) * 5120, c, w, lane, tA, tB, inr, ini, budget);
  drain();
  bar();
  if (tid == 0) __hip_atomic_store((gu64*)(p.ws + W_FLG + (2 * NC + c) * 128) + g, tF, __ATOMIC_RELAXED, __HIP_MEMORY_SCOPE_AGENT);
  STAMP(15);
  wait_flags(p.ws, W_FLG + (2 * NC + c) * 128, tF, lane, budget);
  STAMP(16);
  {
    gemm_y(lds, ws, W_H + (NC + c) * 36 * 5120, Cw, w, g, lane, z);
#pragma unroll
    for (int i = 0; i < 5; ++i) {
      float s0 = (z[i].x + z[i].y) + (z[i].z + z[i].w);
      float s1 = (z[i].x * z[i].x + z[i].y * z[i].y) + (z[i].z * z[i].z + z[i].w * z[i].w);
      float s = red32(s0, s1);
      s = red16(s, s);
      if (!(kg & 1)) *(float*)(lds + L_SP + (w * 80 + 16 * i + n) * 8 + (kg >> 1) * 4) = s;
    }
  }
  bar();
  STAMP(17);
  if (tid < 80) {
    f32x2 a = *(const f32x2*)(lds + L_SP + tid * 8), b = *(const f32x2*)(lds + L_SP + (80 + tid) * 8);
    f32x2 e = *(const f32x2*)(lds + L_SP + (160 + tid) * 8), f = *(const f32x2*)(lds + L_SP + (240 + tid) * 8);
    a = (a + b) + (e + f);
    stc(ws, W_ST1 + ((c * 9 + g) * 80 + tid) * 16, u32x4{__float_as_uint(a.x), tA, __float_as_uint(a.y), tB});
  }
  {
    const int l3 = lane < 60 ? lane : 0, th = l3 / 20, row = 20 * w + l3 - 20 * th;
    u32 off[3]; bool need[3]; u32x4 v[3];
#pragma unroll
    for (int j = 0; j < 3; ++j) { off[j] = W_ST1 + ((c * 9 + 3 * th + j) * 80 + row) * 16; need[j] = true; }
    gather<3>(ws, off, need, v, tA, tB, budget);
    float s0 = __uint_as_float(v[0].x) + __uint_as_float(v[1].x) + __uint_as_float(v[2].x);
    float s1 = __uint_as_float(v[0].z) + __uint_as_float(v[1].z) + __uint_as_float(v[2].z);
    s0 = sum3(s0, lane) * (1.f / D);
    s1 = sum3(s1, lane) * (1.f / D);
    if (lane < 20) *(f32x2*)(lds + L_P2 + row * 8) = f32x2{s0, 1.f / sqrtf(fmaxf(s1 - s0 * s0, 0.f) + 1e-5f)};
  }
  bar();
  STAMP(18);
#pragma unroll
  for (int i = 0; i < 5; ++i) {
    f32x2 pr = *(const f32x2*)(lds + L_P2 + (16 * i + n) * 8);
    const float cen = *(const float*)(lds + L_P1 + (16 * i + n) * 8 + 4) + *(const float*)(lds + L_PAR + (16 * i + n) * 16 + 8);
    float gv[4] = {lo16(gq[i][0]), hi16(gq[i][0]), lo16(gq[i][1]), hi16(gq[i][1])};
    f32x4 o;
#pragma unroll
    for (int e = 0; e < 4; ++e) o[e] = (z[i][e] + cen) + (z[i][e] - pr.x) * pr.y * gv[e];
    const int t = t0 + 16 * i + n;
    if (t < S) *(f32x4*)(p.out + t * D + 64 * g + 16 * w + 4 * kg) = o;
  }
  if (c == NC - 1 && g == 0 && tid == 0) __hip_atomic_store((gu32*)p.ws, tA, __ATOMIC_RELAXED, __HIP_MEMORY_SCOPE_AGENT);
  STAMP(19);
}

extern "C" void kernel_launch(void* const* d_in, const int* in_sizes, int n_in, void* d_out, int out_size, void* d_ws,
                              size_t ws_size, hipStream_t stream) {
  Params p;
  p.x = (const u16*)d_in[0]; p.lre = (const u16*)d_in[1]; p.lim = (const u16*)d_in[2];
  p.bre = (const u16*)d_in[3]; p.bim = (const u16*)d_in[4]; p.cre = (const u16*)d_in[5]; p.cim = (const u16*)d_in[6];
  p.ldt = (const u16*)d_in[7]; p.dsk = (const u16*)d_in[8]; p.k = (const u16*)d_in[10]; p.v = (const u16*)d_in[11];
  p.shk = (const u16*)d_in[12]; p.sk = (const u16*)d_in[13]; p.sd = (const u16*)d_in[14];
  p.out = (float*)d_out; p.ws = (unsigned char*)d_ws;
  (void)hipFuncSetAttribute((const void*)fused_kernel, hipFuncAttributeMaxDynamicSharedMemorySize, L_TOT);
  hipLaunchKernelGGL(fused_kernel, dim3(256), dim3(256), L_TOT, stream, p);
}
```

```cpp
#include <hip/hip_runtime.h>
#include <stdint.h>

typedef unsigned short u16;
typedef unsigned int u32;
typedef unsigned long long u64;
typedef __attribute__((ext_vector_type(4))) unsigned int u32x4;
typedef __attribute__((ext_vector_type(2))) unsigned int u32x2;
typedef __attribute__((ext_vector_type(4))) float f32x4;
typedef __attribute__((ext_vector_type(2))) float f32x2;
typedef __attribute__((ext_vector_type(8))) __bf16 bf16x8;
typedef __attribute__((ext_vector_type(2))) __bf16 bf16x2;
typedef __attribute__((ext_vector_type(4))) short s16x4;
typedef __attribute__((address_space(1))) u64 gu64;
typedef __attribute__((address_space(1))) u32 gu32;
typedef __attribute__((address_space(3))) s16x4 lds_s16x4;
typedef __amdgpu_buffer_rsrc_t rsrc_t;

#define DI __device__ __forceinline__
#define SB() __builtin_amdgcn_sched_barrier(0)

constexpr int S = 2048, D = 576, NC = 26;
constexpr u32 W_FLG = 4096;
constexpr u32 W_E = 32768;
constexpr u32 W_ST = W_E + 2 * NC * D * 16;
constexpr u32 W_KVT = 4u << 20;
constexpr u32 W_GT = W_KVT + D * 8192;
constexpr u32 W_H = W_GT + D * 8192;
constexpr u32 W_U3 = W_H + 2 * NC * 36 * 5120;
constexpr u32 W_END = W_U3 + NC * 94208;
constexpr u32 W_DBG = 64u << 20;
constexpr int L_A = 0;
constexpr int CVSZ = 13056, CV_K1 = 4224, CV_R = 8448;
constexpr int RB = 23568;
constexpr u32 OOB = 0x7ffffff0u;
constexpr int L_CV = 4 * RB;
constexpr int L_HI = L_CV + 2 * CVSZ;
constexpr int L_GI = L_HI + 4 * 5376;
constexpr int L_SP = L_GI + 64 * 168;
constexpr int L_PAR = L_SP + 2560;
constexpr int L_P3 = L_PAR + 640;
constexpr int L_P1 = L_PAR + 1280;
constexpr int L_P2 = L_P1 + 640;
constexpr int L_RS = L_P2 + 640;
constexpr int L_TOT = L_RS + 2048;

struct Params {
  const u16 *x, *lre, *lim, *bre, *bim, *cre, *cim, *ldt, *dsk, *k, *v, *shk, *sk, *sd;
  float* out;
  unsigned char* ws;
};

DI float lo16(u32 w) { return __uint_as_float(w << 16); }
DI float hi16(u32 w) { return __uint_as_float(w & 0xffff0000u); }
DI float b2f(u16 h) { return __uint_as_float(((u32)h) << 16); }
DI u32 pk(float a, float b) {
  f32x2 v = {a, b};
  return __builtin_bit_cast(u32, __builtin_convertvector(v, bf16x2));
}
DI float rb(float a) { return lo16(pk(a, a)); }
DI f32x4 mma(u32x4 a, u32x4 b, f32x4 c) {
  return __builtin_amdgcn_mfma_f32_16x16x32_bf16(__builtin_bit_cast(bf16x8, a), __builtin_bit_cast(bf16x8, b), c, 0, 0, 0);
}
DI float dot2(u32 a, u32 b, float c) {
  return __builtin_amdgcn_fdot2_f32_bf16(__builtin_bit_cast(bf16x2, a), __builtin_bit_cast(bf16x2, b), c, false);
}
DI void bar() { asm volatile("s_waitcnt lgkmcnt(0)\n\ts_barrier" ::: "memory"); }
DI void drain() { asm volatile("s_waitcnt vmcnt(0)" ::: "memory"); }
DI void cfence() { asm volatile("" ::: "memory"); }
DI u32x4 ldc(rsrc_t r, u32 off) { return __builtin_amdgcn_raw_buffer_load_b128(r, off, 0, 16); }
DI u32x4 ldcs(rsrc_t r, u32 off, u32 soff) { return __builtin_amdgcn_raw_buffer_load_b128(r, off, soff, 16); }
DI void stc(rsrc_t r, u32 off, u32x4 v) { __builtin_amdgcn_raw_buffer_store_b128(v, r, off, 0, 16); }
DI void stc2(rsrc_t r, u32 off, u32x2 v) { __builtin_amdgcn_raw_buffer_store_b64(v, r, off, 0, 16); }
DI u32x4 ldg16(const u16* p) { return *(const u32x4*)p; }
DI u32x2 ldg8(const u16* p) { return *(const u32x2*)p; }
DI u32x2 trread(unsigned char* lds, int off) {
  s16x4 r = __builtin_amdgcn_ds_read_tr16_b64_v4i16((lds_s16x4*)(lds + off));
  return __builtin_bit_cast(u32x2, r);
}
DI void cmul(float ar, float ai, float br, float bi, float& cr, float& ci) {
  float r = ar * br - ai * bi, i = ar * bi + ai * br;
  cr = r; ci = i;
}
DI float red32(float a, float b) {
  auto r = __builtin_amdgcn_permlane32_swap(__float_as_uint(a), __float_as_uint(b), false, false);
  return __uint_as_float(r[0]) + __uint_as_float(r[1]);
}
DI float red16(float a, float b) {
  auto r = __builtin_amdgcn_permlane16_swap(__float_as_uint(a), __float_as_uint(b), false, false);
  return __uint_as_float(r[0]) + __uint_as_float(r[1]);
}

DI void pair16(float a, float& e, float& o) {
  auto r = __builtin_amdgcn_permlane16_swap(__float_as_uint(a), __float_as_uint(a), false, false);
  e = __uint_as_float(r[0]); o = __uint_as_float(r[1]);
}
DI void pair32(float a, float& e, float& o) {
  auto r = __builtin_amdgcn_permlane32_swap(__float_as_uint(a), __float_as_uint(a), false, false);
  e = __uint_as_float(r[0]); o = __uint_as_float(r[1]);
}
DI float sum3(float a, int lane) {
  return a + __int_as_float(__builtin_amdgcn_ds_bpermute(lane * 4 + 80, __float_as_int(a))) +
         __int_as_float(__builtin_amdgcn_ds_bpermute(lane * 4 + 160, __float_as_int(a)));
}
typedef __attribute__((address_space(3))) void lds_void;
typedef __attribute__((address_space(1))) const void gvoid;
template <int O1, int O2, int O3>
DI void ltr(unsigned char* lds, int wa, int ra, u32x4& r0, u32x4& r1, u32x4& r2, u32x4& r3) {
  *(u32x4*)(lds + wa) = r0; *(u32x4*)(lds + wa + 1040) = r1; *(u32x4*)(lds + wa + 2080) = r2; *(u32x4*)(lds + wa + 3120) = r3;
  cfence();
  r0 = *(const u32x4*)(lds + ra); r1 = *(const u32x4*)(lds + ra + O1);
  r2 = *(const u32x4*)(lds + ra + O2); r3 = *(const u32x4*)(lds + ra + O3);
  cfence();
}
template <int OFF>
DI void dma_g(const u16* base, u32 voff, u32 ldsa) {
  asm volatile("s_mov_b32 m0, %2\n\ts_nop 0\n\tglobal_load_lds_dwordx4 %0, %1 offset:%3" :: "v"(voff), "s"(base), "s"(ldsa - OFF), "n"(OFF) : "memory");
}
DI void dma_c(rsrc_t r, u32 voff, u32 soff, u32 ldsa) {
  asm volatile("s_mov_b32 m0, %3\n\ts_nop 0\n\tbuffer_load_dwordx4 %0, %1, %2 offen sc1 lds" :: "v"(voff), "s"(r), "s"(soff), "s"(ldsa) : "memory");
}
DI float quad_sum(float a) {
  a += __int_as_float(__builtin_amdgcn_update_dpp(0, __float_as_int(a), 0xb1, 0xf, 0xf, true));
  a += __int_as_float(__builtin_amdgcn_update_dpp(0, __float_as_int(a), 0x4e, 0xf, 0xf, true));
  return a;
}
constexpr unsigned SPIN_MAX = 1u << 20;
DI bool spin_next(unsigned& budget) {
  if (budget == 0) return false;
  --budget;
  __builtin_amdgcn_s_sleep(1);
  return true;
}
template <int N, bool PRE = false>
DI void gather(rsrc_t ws, const u32 (&off)[N], const bool (&need)[N], u32x4 (&v)[N], u32 tA, u32 tB, unsigned& budget) {
  if (PRE) {
    bool ok = true;
#pragma unroll
    for (int i = 0; i < N; ++i) ok &= (!need[i]) | ((v[i].y == tA) & (v[i].w == tB));
    if (__all(ok)) return;
  }
  for (;;) {
    cfence();
    bool ok = true;
#pragma unroll
    for (int i = 0; i < N; ++i) v[i] = ldc(ws, off[i]);
#pragma unroll
    for (int i = 0; i < N; ++i) ok &= (!need[i]) | ((v[i].y == tA) & (v[i].w == tB));
    if (__all(ok)) break;
    if (!spin_next(budget)) break;
  }
  cfence();
}
DI u64 poll_flags(unsigned char* wsp, u32 off, int lane) {
  return __hip_atomic_load((gu64*)(wsp + off) + (lane < 9 ? lane : 0), __ATOMIC_RELAXED, __HIP_MEMORY_SCOPE_AGENT);
}
DI void wait_flags(unsigned char* wsp, u32 off, u64 tag, int lane, unsigned& budget, u64 pre) {
  gu64* f = (gu64*)(wsp + off) + (lane < 9 ? lane : 0);
  if (!__all(pre == tag)) for (;;) {
    u64 x = __hip_atomic_load(f, __ATOMIC_RELAXED, __HIP_MEMORY_SCOPE_AGENT);
    if (__all(x == tag)) break;
    if (!spin_next(budget)) break;
  }
  __builtin_amdgcn_fence(__ATOMIC_ACQUIRE, "wavefront");
  cfence();
}

#define STAMP(i) do {} while (0)

template <int V> struct IC { static constexpr int v = V; constexpr operator int() const { return V; } };
template <int N, int K = 0, class F>
DI void static_for(F f) { if constexpr (K < N) { f(IC<K>{}); static_for<N, K + 1>(f); } }
constexpr int NPIN = 7;
template <int N>
DI void vmw() { asm volatile("s_waitcnt vmcnt(%0)" :: "n"(N) : "memory"); }
DI void conv_stage_k(unsigned char* cv, u32x4 kx, u32 knext, int tid) {
  *(u32x4*)(cv + 32 + 16 * tid) = kx;
  u32x4 s;
  s.x = __builtin_amdgcn_alignbit(kx.y, kx.x, 16);
  s.y = __builtin_amdgcn_alignbit(kx.z, kx.y, 16);
  s.z = __builtin_amdgcn_alignbit(kx.w, kx.z, 16);
  s.w = __builtin_amdgcn_alignbit(knext, kx.w, 16);
  *(u32x4*)(cv + CV_K1 + 32 + 16 * tid) = s;
  u32x4 z = {0, 0, 0, 0};
  if (tid == 0) {
    *(u32x4*)(cv) = z; *(u32x4*)(cv + 16) = z; *(u32x4*)(cv + CV_K1) = z;
    u32x4 f = {0, 0, 0, kx.x << 16};
    *(u32x4*)(cv + CV_K1 + 16) = f;
  }
  if (tid >= 64 && tid < 96) *(u32x4*)(cv + CV_R + 4096 + 16 * (tid - 64)) = z;
}
template <bool PRE>
DI void conv_gather(unsigned char* cv, rsrc_t ws, int ch0, int tid, u32x4 (&v)[4], u32 tA, u32 tB, unsigned& budget) {
  u32 off[4]; bool need[4];
#pragma unroll
  for (int r = 0; r < 4; ++r) { off[r] = W_KVT + ch0 * 8192 + (tid + 256 * r) * 16; need[r] = true; }
  gather<4, PRE>(ws, off, need, v, tA, tB, budget);
#pragma unroll
  for (int r = 0; r < 4; ++r) {
    u32x2 w = {v[r].x, v[r].z};
    *(u32x2*)(cv + (r >> 1) * CVSZ + CV_R + ((r & 1) * 256 + tid) * 8) = w;
  }
}
template <int R0, int R1, class F>
DI void conv_mma(unsigned char* lds, int cvo, int w, int lane, f32x4 (&acc)[8], F hook) {
  const int m = lane & 15, kg = lane >> 4;
  const int cb = cvo + (w >> 1) * CVSZ + (w & 1) * 256;
  int ka[4];
  ka[0] = cb + ((m & 1) ? 0 : CV_K1) + 16 * kg + 4 * ((m + 1) >> 1);
  ka[1] = ka[0] + 1024; ka[2] = ka[0] + 2048; ka[3] = ka[0] + 3072;
  int rbp = cb + CV_R + 480 - 32 * m + 16 * kg;
  asm volatile("" : "+v"(ka[0]), "+v"(ka[1]), "+v"(ka[2]), "+v"(ka[3]), "+v"(rbp));
  u32x4 A[8], B[8];
  auto ldA = [&](int r, int a) {
    const u32* q = (const u32*)(lds + ka[a >> 1] + 64 * r + 512 * (a & 1));
    A[a].x = q[0]; A[a].y = q[1]; A[a].z = q[2]; A[a].w = q[3];
  };
  auto ldB = [&](int r, int b) { B[b] = *(const u32x4*)(lds + rbp + 64 * r + 512 * (7 - b)); };
  ldB(R0, 0);
#pragma unroll
  for (int a = 7; a >= 0; --a) ldA(R0, a);
#pragma unroll
  for (int b = 1; b < 8; ++b) ldB(R0, b);
  SB();
#pragma unroll
  for (int r = R0; r < R1; ++r) {
#pragma unroll
    for (int b = 0; b < 8; ++b) {
#pragma unroll
      for (int a = 7 - b; a >= 0; --a) acc[a + b] = mma(A[a], B[b], acc[a + b]);
      SB();
      if (r + 1 < R1) { ldB(r + 1, b); ldA(r + 1, 7 - b); }
      hook(r, b);
      SB();
    }
  }
}
DI void conv_finish(unsigned char* lds, unsigned char* cv, rsrc_t ws, int ch0, float sdv, int w, int lane,
                    f32x4 (&acc)[8], u32 tA, u32 tB) {
  const int n = lane & 15, kg = lane >> 4, odd = w & 1;
  unsigned char* box = lds + L_HI + (w ^ 1) * 5376;
#pragma unroll
  for (int j = 0; j < 4; ++j) {
    f32x4 lo = acc[j], hi = acc[4 + j], snd, kp;
#pragma unroll
    for (int e = 0; e < 4; ++e) { snd[e] = odd ? lo[e] : hi[e]; kp[e] = odd ? hi[e] : lo[e]; }
    *(f32x4*)(box + j * 1024 + lane * 16) = snd;
    acc[j] = kp;
  }
  bar();
  const unsigned char* mine = lds + L_HI + w * 5376;
  const unsigned char* cb = cv + (w >> 1) * CVSZ + CV_R;
#pragma unroll
  for (int j = 0; j < 4; ++j) {
    f32x4 y = acc[j] + *(const f32x4*)(mine + j * 1024 + lane * 16);
    const int X = 256 * (4 * odd + j) + 16 * n + 4 * kg;
    u32x2 rv = *(const u32x2*)(cb + (2044 - X) * 2);
    u32 y01 = pk(y.x, y.y), y23 = pk(y.z, y.w);
    u32 s01 = pk(hi16(rv.y) * sdv, lo16(rv.y) * sdv), s23 = pk(hi16(rv.x) * sdv, lo16(rv.x) * sdv);
    u32x4 o;
    o.x = pk(lo16(y01) + lo16(s01), hi16(y01) + hi16(s01)); o.y = tA;
    o.z = pk(lo16(y23) + lo16(s23), hi16(y23) + hi16(s23)); o.w = tB;
    stc(ws, W_GT + (ch0 + (w >> 1)) * 8192 + X * 4, o);
  }
}

struct Scan {
  float ar, ai, cr, ci, a20r, a20i, a40r, a40i, aqr, aqi, bqr, bqi, a320r, a320i;
};

template <bool RS, class F>
DI void gemm_bu(const unsigned char* lds, u32x4 (&Bw)[18][2], int lane, f32x4 (&acc)[5][2], float& rsr, float& rsi, F pre) {
#pragma unroll
  for (int i = 0; i < 5; ++i) { acc[i][0] = f32x4{0, 0, 0, 0}; acc[i][1] = f32x4{0, 0, 0, 0}; }
  int ab0, ab1;
  {
    const int n = lane & 15, kg = lane >> 4;
    ab0 = L_A + (n >> 2) * RB + kg * 256 + (n & 3) * 64;
    ab1 = L_A + (n >> 2) * RB + 20480 + (kg & 1) * 256 + (n & 3) * 64 + (kg >> 1) * 16;
  }
  asm volatile("" : "+v"(ab0), "+v"(ab1));
  auto rdA = [&](int ks, int i) {
    return *(const u32x4*)(lds + (ks < 16 ? ab0 + i * 4096 + (ks >> 2) * 1024 + (ks & 3) * 16 : ab1 + i * 512 + (ks & 1) * 32));
  };
  f32x4 r0 = {0, 0, 0, 0}, r1 = {0, 0, 0, 0};
  const u32x4 ones = {0x3f803f80u, 0x3f803f80u, 0x3f803f80u, 0x3f803f80u};
  u32x4 a[2][5];
#pragma unroll
  for (int i = 0; i < 5; ++i) a[0][i] = rdA(0, i);
  SB();
#pragma unroll
  for (int ks = 0; ks < 18; ++ks) {
    asm("" : "+a"(Bw[ks][0]), "+a"(Bw[ks][1]));
#pragma unroll
    for (int i = 0; i < 5; ++i) {
      if (ks + 1 < 18) a[(ks + 1) & 1][i] = rdA(ks + 1, i);
      acc[i][0] = mma(a[ks & 1][i], Bw[ks][0], acc[i][0]);
      acc[i][1] = mma(a[ks & 1][i], Bw[ks][1], acc[i][1]);
      pre(ks, i);
      SB();
    }
    if (RS) { r0 = mma(ones, Bw[ks][0], r0); r1 = mma(ones, Bw[ks][1], r1); SB(); }
  }
  if (RS) { rsr = r0.x; rsi = r1.x; }
}
DI void scan_a(const Scan& sc, const f32x4 (&acc)[5][2], rsrc_t ws, u32 eoff, int lane, u32 tA, u32 tB, float& inr, float& ini) {
  float er = 0.f, ei = 0.f;
#pragma unroll
  for (int i = 0; i < 5; ++i)
#pragma unroll
    for (int r = 0; r < 4; ++r) {
      float nr = sc.ar * er + (acc[i][0][r] - sc.ai * ei), ni = sc.ar * ei + (acc[i][1][r] + sc.ai * er);
      er = nr; ei = ni;
    }
  const int q = lane >> 4;
  float e0r, e1r, e0i, e1i, pr, pi, p0r, p1r, p0i, p1i, tr, ti;
  pair16(er, e0r, e1r); pair16(ei, e0i, e1i);
  cmul(sc.a20r, sc.a20i, e0r, e0i, pr, pi);
  pr += e1r; pi += e1i;
  pair32(pr, p0r, p1r); pair32(pi, p0i, p1i);
  cmul(sc.a40r, sc.a40i, p0r, p0i, tr, ti);
  stc(ws, q == 0 ? eoff : OOB, u32x4{__float_as_uint(tr + p1r), tA, __float_as_uint(ti + p1i), tB});
  cmul(sc.a20r, sc.a20i, p0r, p0i, tr, ti);
  inr = q == 0 ? 0.f : q == 1 ? e0r : q == 2 ? p0r : tr + e0r;
  ini = q == 0 ? 0.f : q == 1 ? e0i : q == 2 ? p0i : ti + e0i;
}
template <bool PRE>
DI void scan_b(const Scan& sc, const f32x4 (&acc)[5][2], unsigned char* lds, rsrc_t ws, u32 ebase, u32 hoff, int c, int w,
               int lane, u32 tA, u32 tB, float inr, float ini, u32x4 (&v)[7], unsigned& budget) {
  const int n = lane & 15, q = lane >> 4;
  u32 off[7]; bool need[7];
#pragma unroll
  for (int j = 0; j < 7; ++j) {
    int cp = c - 1 - q - 4 * j;
    need[j] = cp >= 0;
    off[j] = ebase + (cp >= 0 ? cp : 0) * (D * 16);
  }
  gather<7, PRE>(ws, off, need, v, tA, tB, budget);
  float hr = 0.f, hi = 0.f;
#pragma unroll
  for (int j = 6; j >= 0; --j) {
    cmul(sc.a320r, sc.a320i, hr, hi, hr, hi);
    hr += need[j] ? __uint_as_float(v[j].x) : 0.f;
    hi += need[j] ? __uint_as_float(v[j].z) : 0.f;
  }
  cmul(sc.bqr, sc.bqi, hr, hi, hr, hi);
  hr = red16(hr, hr); hi = red16(hi, hi);
  hr = red32(hr, hr); hi = red32(hi, hi);
  cmul(sc.aqr, sc.aqi, hr, hi, hr, hi);
  hr += inr; hi += ini;
  unsigned char* img = lds + L_HI + w * 5376;
#pragma unroll
  for (int i = 0; i < 5; ++i) {
    float o_r[4], o_i[4];
#pragma unroll
    for (int r = 0; r < 4; ++r) {
      float nr = sc.ar * hr + (acc[i][0][r] - sc.ai * hi), ni = sc.ar * hi + (acc[i][1][r] + sc.ai * hr);
      hr = nr; hi = ni;
      o_r[r] = sc.cr * hr - sc.ci * hi;
      o_i[r] = -(sc.cr * hi) - sc.ci * hr;
    }
    *(u32x2*)(img + n * 168 + 40 * q + 8 * i) = u32x2{pk(o_r[0], o_r[1]), pk(o_r[2], o_r[3])};
    *(u32x2*)(img + (16 + n) * 168 + 40 * q + 8 * i) = u32x2{pk(o_i[0], o_i[1]), pk(o_i[2], o_i[3])};
  }
  asm volatile("s_waitcnt lgkmcnt(0)" ::: "memory");
  const int tro = L_HI + w * 5376 + (8 * q + (n >> 2)) * 168 + 8 * (n & 3);
#pragma unroll
  for (int i = 0; i < 5; ++i) {
    u32x2 a = trread(lds, tro + 32 * i), b = trread(lds, tro + 4 * 168 + 32 * i);
    stc(ws, hoff + i * 1024 + lane * 16, u32x4{a.x, a.y, b.x, b.y});
  }
}
DI void dma_c4(rsrc_t r, u32 voff, u32 soff, u32 mb) {
  asm volatile("s_mov_b32 m0, %3\n\ts_nop 0\n\tbuffer_load_dwordx4 %0, %1, %2 offen sc1 lds\n\t"
               "buffer_load_dwordx4 %0, %1, %2 offen offset:1024 sc1 lds\n\tbuffer_load_dwordx4 %0, %1, %2 offen offset:2048 sc1 lds\n\t"
               "buffer_load_dwordx4 %0, %1, %2 offen offset:3072 sc1 lds" :: "v"(voff), "s"(r), "s"(soff), "s"(mb) : "memory");
}
template <int NX, class F>
DI void gemm_y(unsigned char* lds, u32 lb, rsrc_t ws, u32 hbase, u32x4 (&Cw)[9][4], int w, int g, int lane, f32x4 (&y)[5], F mid) {
  f32x4 acc[4][5];
#pragma unroll
  for (int d = 0; d < 4; ++d)
#pragma unroll
    for (int i = 0; i < 5; ++i) acc[d][i] = d ? f32x4{0, 0, 0, 0} : y[i];
  u32 ho[3];
  {
    const int n = lane & 15, kg = lane >> 4;
    const u32 hb = hbase + (w >> 1) * 18 * 5120 + (w & 1) * 512 + n * 16;
    ho[0] = hb + ((kg + g) % 9) * 10240;
    ho[1] = hb + ((4 + kg + g) % 9) * 10240;
    ho[2] = hb + ((8 + g) % 9) * 10240 + (kg >> 1) * 5120 + (kg & 1) * 256;
  }
  const u32 sa = lb + L_A + w * RB, sh = lb + L_HI + w * 5376;
  auto dmaH = [&](int j) {
    const u32 so = ((j >> 1) & 1) * 5120 + (j & 1) * 256, mb = j == 4 ? sh : sa + (j % 5) * 5120;
    dma_c4(ws, ho[j >> 2], so, mb); dma_c(ws, ho[j >> 2], so + 4096, mb + 4096);
  };
  int ra = L_A + w * RB + lane * 16, rh = L_HI + w * 5376 + lane * 16;
  asm volatile("" : "+v"(ra), "+v"(rh));
  auto rdH = [&](int j, int i) { return *(const u32x4*)(lds + (j == 4 ? rh : ra + (j % 5) * 5120) + i * 1024); };
#pragma unroll
  for (int j = 0; j < 4; ++j) { dmaH(j); mid(10 + j); }
  u32x4 hf[2][5];
  mid(-2);
  vmw<15>();
  mid(-1);
#pragma unroll
  for (int i = 0; i < 5; ++i) hf[0][i] = rdH(0, i);
  SB();
  static_for<45>([&](auto I) {
    constexpr int j = I / 5, i = I % 5;
    if (i == 0 && j < NPIN) asm("" : "+a"(Cw[j][0]));
    if (j < 8) {
      vmw<(j < 5 ? 14 : j < 8 ? 39 - I + NX : 0)>();
      hf[(j + 1) & 1][i] = rdH(j + 1, i);
    }
    acc[3][i] = mma(Cw[j][3], hf[j & 1][i], acc[3][i]);
    acc[2][i] = mma(Cw[j][2], hf[j & 1][i], acc[2][i]);
    SB();
    if constexpr (j < 5) {
      constexpr int J = j + 4;
      dma_c(ws, ho[J >> 2], ((J >> 1) & 1) * 5120 + (J & 1) * 256 + i * 1024, (J == 4 ? sh : sa + (J % 5) * 5120) + i * 1024);
    }
    if (j == 8 && i > 0) {
#pragma unroll
      for (int d = 1; d < 4; ++d) *(f32x4*)(lds + ra + ((d - 1) * 5 + i - 1) * 1024) = acc[d][i > 0 ? i - 1 : 0];
    }
    SB();
    acc[1][i] = mma(Cw[j][1], hf[j & 1][i], acc[1][i]);
    acc[0][i] = mma(Cw[j][0], hf[j & 1][i], acc[0][i]);
    SB();
    if (i == 4) mid(j);
    SB();
  });
#pragma unroll
  for (int d = 1; d < 4; ++d) *(f32x4*)(lds + ra + ((d - 1) * 5 + 4) * 1024) = acc[d][4];
  bar();
  const unsigned char* b1 = lds + L_A + ((w + 3) & 3) * RB + lane * 16;
  const unsigned char* b2 = lds + L_A + ((w + 2) & 3) * RB + 5120 + lane * 16;
  const unsigned char* b3 = lds + L_A + ((w + 1) & 3) * RB + 10240 + lane * 16;
#pragma unroll
  for (int i = 0; i < 5; ++i)
    y[i] = (acc[0][i] + *(const f32x4*)(b1 + i * 1024)) + (*(const f32x4*)(b2 + i * 1024) + *(const f32x4*)(b3 + i * 1024));
}
DI void rs_part(unsigned char* lds, const f32x4 (&z)[5], int w, int lane) {
  const int n = lane & 15, kg = lane >> 4;
#pragma unroll
  for (int i = 0; i < 5; ++i) {
    float s0 = (z[i].x + z[i].y) + (z[i].z + z[i].w);
    float s1 = (z[i].x * z[i].x + z[i].y * z[i].y) + (z[i].z * z[i].z + z[i].w * z[i].w);
    float s = red32(s0, s1);
    *(float*)(lds + L_SP + (w * 80 + 16 * i + n) * 8 + (kg >> 1) * 4) = red16(s, s);
  }
}
DI void rs_pub(unsigned char* lds, rsrc_t ws, u32 base, int tid, u32 tA, u32 tB) {
  const int r = tid < 80 ? tid : 0;
  f32x2 a = *(const f32x2*)(lds + L_SP + r * 8), b = *(const f32x2*)(lds + L_SP + (80 + r) * 8);
  f32x2 e = *(const f32x2*)(lds + L_SP + (160 + r) * 8), f = *(const f32x2*)(lds + L_SP + (240 + r) * 8);
  a = (a + b) + (e + f);
  stc(ws, tid < 80 ? base + tid * 16 : OOB, u32x4{__float_as_uint(a.x), tA, __float_as_uint(a.y), tB});
}
DI u32 rs_off(int w, int lane) {
  const int l3 = lane < 60 ? lane : 0, th = l3 / 20;
  return (240 * th + 20 * w + l3 - 20 * th) * 16;
}
template <bool PRE>
DI void rs_get(unsigned char* lds, int lout, rsrc_t ws, u32 base, int w, int lane, u32x4 (&v)[3], u32 tA, u32 tB, unsigned& budget) {
  u32 off[3]; bool need[3];
#pragma unroll
  for (int j = 0; j < 3; ++j) { off[j] = base + rs_off(w, lane) + j * 1280; need[j] = true; }
  gather<3, PRE>(ws, off, need, v, tA, tB, budget);
  float s0 = __uint_as_float(v[0].x) + __uint_as_float(v[1].x) + __uint_as_float(v[2].x);
  float s1 = __uint_as_float(v[0].z) + __uint_as_float(v[1].z) + __uint_as_float(v[2].z);
  s0 = sum3(s0, lane) * (1.f / D);
  s1 = sum3(s1, lane) * (1.f / D);
  if (lane < 20) *(f32x2*)(lds + lout + (20 * w + lane) * 8) = f32x2{1.f / sqrtf(fmaxf(s1 - s0 * s0, 0.f) + 1e-5f), s0};
}
DI void norm_acc(const unsigned char* lds, int lp, int kg, f32x4 (&acc)[5][2], float rsr, float rsi) {
#pragma unroll
  for (int i = 0; i < 5; ++i) {
    f32x4 pa = *(const f32x4*)(lds + lp + (20 * kg + 4 * i) * 8), pb = *(const f32x4*)(lds + lp + (20 * kg + 4 * i) * 8 + 16);
    acc[i][0].x = (acc[i][0].x - pa.y * rsr) * pa.x; acc[i][1].x = (acc[i][1].x - pa.y * rsi) * pa.x;
    acc[i][0].y = (acc[i][0].y - pa.w * rsr) * pa.z; acc[i][1].y = (acc[i][1].y - pa.w * rsi) * pa.z;
    acc[i][0].z = (acc[i][0].z - pb.y * rsr) * pb.x; acc[i][1].z = (acc[i][1].z - pb.y * rsi) * pb.x;
    acc[i][0].w = (acc[i][0].w - pb.w * rsr) * pb.z; acc[i][1].w = (acc[i][1].w - pb.w * rsi) * pb.z;
  }
}
__global__ void __launch_bounds__(256, 1) fused_kernel(Params p) {
  extern __shared__ __attribute__((aligned(16))) unsigned char lds[];
  int tid = threadIdx.x, lane = tid & 63;
  const int w = __builtin_amdgcn_readfirstlane(tid >> 6);
  const int xcd = blockIdx.x & 7, slot = blockIdx.x >> 3;
  int c, g, spare = -1;
  if (slot < 27) { c = xcd * 3 + slot / 9; g = slot % 9; }
  else {
    int e = (xcd & 3) * 5 + (slot - 27);
    if (xcd < 4) { int e2 = (xcd & 1) * 5 + (slot - 27); if (e2 >= 9) return; c = 24 + (xcd >> 1); g = e2; }
    else { if (e >= 18) return; spare = e; c = 0; g = e >> 1; }
  }
#define DUMP() do {} while (0)
  STAMP(0);
  const rsrc_t ws = __builtin_amdgcn_make_buffer_rsrc(p.ws, 0, W_END, 0x00020000);
  u32 ep = *(const __attribute__((address_space(4))) u32*)p.ws;
#define TAGS() asm volatile("" : "+s"(ep)); const u32 tA = ep + 1u; \
  const u32 tB = tA * 0x9E3779B1u + 0x7F4A7C15u
  unsigned budget = SPIN_MAX;
  int n = lane & 15, kg = lane >> 4;
#define PHASE() do { asm volatile("" : "+v"(tid)); lane = tid & 63; n = lane & 15; kg = lane >> 4; pch = 64 * g + 16 * w + n; } while (0)

  if (spare >= 0) {
    const int ch0 = 64 * g + 52 + 6 * (spare & 1);
#pragma unroll
    for (int i = 0; i < 6; ++i) {
      const u16* kp = p.sk + (ch0 + i) * S + 8 * tid;
      u32x4 kx = ldg16(kp);
      u32 kn = tid < 255 ? kp[8] : 0;
      conv_stage_k(lds + i * CVSZ, kx, kn, tid);
    }
    u32 sd0 = p.sd[ch0 + (w >> 1)], sd1 = p.sd[ch0 + 2 + (w >> 1)], sd2 = p.sd[ch0 + 4 + (w >> 1)];
    TAGS();
    u32x4 kq[3][4];
    conv_gather<false>(lds, ws, ch0, tid, kq[0], tA, tB, budget);
#pragma unroll
    for (int r = 4; r < 12; ++r) kq[r >> 2][r & 3] = ldc(ws, W_KVT + ch0 * 8192 + (tid + 256 * r) * 16);
    conv_gather<true>(lds + 2 * CVSZ, ws, ch0 + 2, tid, kq[1], tA, tB, budget);
    conv_gather<true>(lds + 4 * CVSZ, ws, ch0 + 4, tid, kq[2], tA, tB, budget);
#pragma unroll 1
    for (int r = 0; r < 3; ++r) {
      const float sdv = lo16(r == 0 ? sd0 : r == 1 ? sd1 : sd2);
      bar();
      f32x4 acc[8];
#pragma unroll
      for (int j = 0; j < 8; ++j) acc[j] = f32x4{0, 0, 0, 0};
      conv_mma<0, 4>(lds, 2 * r * CVSZ, w, lane, acc, [](int, int) {});
      conv_finish(lds, lds + 2 * r * CVSZ, ws, ch0 + 2 * r, sdv, w, lane, acc, tA, tB);
    }
    STAMP(15);
    DUMP();
    return;
  }

  const int t0 = 80 * c;
  const u32 lb = (u32)(size_t)(lds_void*)lds;
  u32 xo[8];
  {
    const int r = (lane >> 2) & 3, jj = lane & 3;
#pragma unroll
    for (int i = 0; i < 5; ++i) {
      int t = t0 + 20 * w + 4 * i + r; t = t > S - 1 ? S - 1 : t;
      xo[i] = (t * D + 32 * kg + 8 * jj) * 2;
    }
#pragma unroll
    for (int u = 0; u < 3; ++u) {
      int t = t0 + 20 * w + 8 * u + 4 * (kg >> 1) + r; t = t > S - 1 ? S - 1 : t;
      xo[5 + u] = (t * D + 512 + 32 * (kg & 1) + 8 * jj) * 2;
    }
  }
  auto xdma = [&](int pc) {
    const u32 la = lb + L_A + w * RB + (pc >> 1) * 4096;
    if (pc & 1) { dma_g<512>(p.x, xo[pc >> 1], la + 2048); dma_g<768>(p.x, xo[pc >> 1], la + 3072); }
    else { dma_g<0>(p.x, xo[pc >> 1], la); dma_g<256>(p.x, xo[pc >> 1], la + 1024); }
    if (pc < 3) dma_g<0>(p.x, xo[5 + pc], lb + L_A + w * RB + 20480 + pc * 1024);
  };
  STAMP(26);
  u32x2 kk[8], vv[5];
  u32x4 shw[2];
  {
    const int col = 64 * g + 4 * (tid & 15), r0 = t0 + 5 * (tid >> 4);
#pragma unroll
    for (int j = 0; j < 8; ++j) { int t = r0 + j - 3; t = t < 0 ? 0 : (t > S - 1 ? S - 1 : t); kk[j] = ldg8(p.k + t * D + col); }
#pragma unroll
    for (int j = 0; j < 5; ++j) { int t = r0 + j; t = t > S - 1 ? S - 1 : t; vv[j] = ldg8(p.v + t * D + col); }
    shw[0] = ldg16(p.shk + col * 4); shw[1] = ldg16(p.shk + col * 4 + 8);
  }
  const int ch0 = 64 * g + 2 * c;
  u32x4 kx0 = ldg16(p.sk + ch0 * S + 8 * tid), kx1 = ldg16(p.sk + (ch0 + 1) * S + 8 * tid);
  u32 kn0 = p.sk[ch0 * S + 8 * tid + (tid < 255 ? 8 : 0)], kn1 = p.sk[(ch0 + 1) * S + 8 * tid + (tid < 255 ? 8 : 0)];
  int pch = 64 * g + 16 * w + n;
  u32 lre_h = p.lre[pch], lim_h = p.lim[pch], ldt_h = p.ldt[pch];
  __builtin_amdgcn_sched_barrier(0);
  u32x4 Bw[18][2];
  const int bo = (64 * g + 16 * w + (n & 12)) * D + 8 * (lane & 3);
  const u16 *br = p.bre + bo + 32 * kg, *bi = p.bim + bo + 32 * kg;
  const u16* bt = (kg < 2 ? p.bre : p.bim) + bo + 512 + 32 * (kg & 1);
  auto bld = [&](int G) {
    SB();
#pragma unroll
    for (int j = 0; j < 4; ++j) {
      if (G < 8) Bw[4 * (G >> 1) + j][G & 1] = ldg16(((G & 1) ? bi : br) + j * D + 128 * (G >> 1));
      else Bw[16 + (j >> 1)][j & 1] = ldg16(bt + j * D);
    }
    SB();
  };
#pragma unroll
  for (int G = 0; G < 5; ++G) bld(G);
  STAMP(20);
  {
    const int r0 = 5 * (tid >> 4);
    const bool first = (t0 + r0 == 0);
    asm volatile("" : "+v"(kk[0]), "+v"(kk[1]), "+v"(kk[2]), "+v"(kk[3]), "+v"(kk[4]), "+v"(kk[5]), "+v"(kk[6]), "+v"(kk[7]),
                 "+v"(vv[0]), "+v"(vv[1]), "+v"(vv[2]), "+v"(vv[3]), "+v"(vv[4]), "+v"(shw[0]), "+v"(shw[1]));
    asm volatile("" : "+v"(kx0), "+v"(kx1), "+v"(kn0), "+v"(kn1), "+v"(lre_h), "+v"(lim_h), "+v"(ldt_h));
    u16* img = (u16*)(lds + L_GI) + 4 * (tid & 15) * 84 + r0;
#pragma unroll
    for (int h = 0; h < 2; ++h) {
      f32x2 sw[4], kf[8];
      sw[0] = f32x2{lo16(shw[h].x), lo16(shw[h].z)}; sw[1] = f32x2{hi16(shw[h].x), hi16(shw[h].z)};
      sw[2] = f32x2{lo16(shw[h].y), lo16(shw[h].w)}; sw[3] = f32x2{hi16(shw[h].y), hi16(shw[h].w)};
#pragma unroll
      for (int j = 0; j < 8; ++j) {
        u32 q = kk[j][h];
        if (j < 3) q = first ? 0u : q;
        kf[j] = f32x2{lo16(q), hi16(q)};
      }
#pragma unroll
      for (int r = 0; r < 5; ++r) {
        SB();
        xdma(5 * h + r);
        SB();
        f32x2 a = kf[r] * sw[3];
#pragma unroll
        for (int j = 1; j < 4; ++j) a = __builtin_elementwise_fma(kf[r + j], sw[3 - j], a);
        a *= f32x2{lo16(vv[r][h]), hi16(vv[r][h])};
        const u32 o = pk(a.x, a.y);
        img[(2 * h) * 84 + r] = (u16)o;
        img[(2 * h + 1) * 84 + r] = (u16)(o >> 16);
      }
    }
  }
  STAMP(21);
  bld(5);
  conv_stage_k(lds + L_CV, kx0, tid < 255 ? kn0 : 0, tid);
  conv_stage_k(lds + L_CV + CVSZ, kx1, tid < 255 ? kn1 : 0, tid);
  STAMP(30);
  bld(6);
  Scan sc;
  {
    float lr = lo16(lre_h), li = lo16(lim_h), dt = rb(expf(lo16(ldt_h)));
    float e = expf(dt * lr), sn, cs;
    sincosf(dt * li, &sn, &cs);
    bld(7);
    sc.ar = e * cs; sc.ai = e * sn;
    float nr = sc.ar - 1.f, ni = sc.ai, dn = lr * lr + li * li;
    sc.cr = 2.f * ((nr * lr + ni * li) / dn); sc.ci = 2.f * ((ni * lr - nr * li) / dn);
    bld(8);
    float r2, i2, r4, i4, r5, i5, r80, i80, r160, i160, r60, i60, r240, i240;
    cmul(sc.ar, sc.ai, sc.ar, sc.ai, r2, i2); cmul(r2, i2, r2, i2, r4, i4); cmul(r4, i4, sc.ar, sc.ai, r5, i5);
    cmul(r5, i5, r5, i5, r2, i2); cmul(r2, i2, r2, i2, sc.a20r, sc.a20i);
    cmul(sc.a20r, sc.a20i, sc.a20r, sc.a20i, sc.a40r, sc.a40i);
    cmul(sc.a40r, sc.a40i, sc.a20r, sc.a20i, r60, i60);
    cmul(sc.a40r, sc.a40i, sc.a40r, sc.a40i, r80, i80);
    cmul(r80, i80, r80, i80, r160, i160); cmul(r160, i160, r80, i80, r240, i240);
    cmul(r160, i160, r160, i160, sc.a320r, sc.a320i);
    sc.aqr = kg == 0 ? 1.f : kg == 1 ? sc.a20r : kg == 2 ? sc.a40r : r60;
    sc.aqi = kg == 0 ? 0.f : kg == 1 ? sc.a20i : kg == 2 ? sc.a40i : i60;
    sc.bqr = kg == 0 ? 1.f : kg == 1 ? r80 : kg == 2 ? r160 : r240;
    sc.bqi = kg == 0 ? 0.f : kg == 1 ? i80 : kg == 2 ? i160 : i240;
  }
  STAMP(28);
  int twa = L_HI + w * 5376 + lane * 16, tra = L_HI + w * 5376 + (n & 3) * 1040 + (n >> 2) * 64;
  asm volatile("s_waitcnt vmcnt(16)" ::: "memory");
  {
    const u32 ones = 0x3f803f80u;
#pragma unroll
    for (int i = 0; i < 5; ++i) {
      u32x4 xv[5];
#pragma unroll
      for (int q = 0; q < 4; ++q) xv[q] = *(const u32x4*)(lds + L_A + w * RB + (i * 4 + q) * 1024 + lane * 16);
      xv[4] = *(const u32x4*)(lds + L_A + w * RB + 20480 + i * 512 + (lane & 31) * 16);
      SB();
      if (i < 4) {
#pragma unroll
        for (int m = 0; m < 2; ++m) ltr<16, 32, 48>(lds, twa, tra + kg * 256, Bw[4 * i][m], Bw[4 * i + 1][m], Bw[4 * i + 2][m], Bw[4 * i + 3][m]);
      } else ltr<512, 32, 544>(lds, twa, tra + (kg & 1) * 256 + (kg >> 1) * 16, Bw[16][0], Bw[16][1], Bw[17][0], Bw[17][1]);
      SB();
      float s1 = 0.f, s2 = 0.f;
#pragma unroll
      for (int e = 0; e < 4; ++e) { s1 = dot2(xv[4][e], ones, s1); s2 = dot2(xv[4][e], xv[4][e], s2); }
      s1 *= 0.5f; s2 *= 0.5f;
#pragma unroll
      for (int q = 0; q < 4; ++q)
#pragma unroll
        for (int e = 0; e < 4; ++e) { s1 = dot2(xv[q][e], ones, s1); s2 = dot2(xv[q][e], xv[q][e], s2); }
      float sm = red32(s1, s2);
      sm = quad_sum(red16(sm, sm));
      *(float*)(lds + L_SP + (20 * w + 4 * i + (n >> 2)) * 8 + (kg >> 1) * 4) = sm;
      SB();
    }
    asm volatile("s_waitcnt lgkmcnt(0)" ::: "memory");
    if (lane < 20) {
      f32x2 v = *(const f32x2*)(lds + L_SP + (20 * w + lane) * 8);
      float m = v.x * (1.f / D), var = fmaxf(v.y * (1.f / D) - m * m, 0.f);
      float den = rb(sqrtf(rb(rb(var) + 1e-5f)));
      *(f32x2*)(lds + L_P1 + (20 * w + lane) * 8) = f32x2{1.f / den, rb(m)};
    }
  }
#pragma unroll
  for (int ks = 0; ks < 18; ++ks) asm volatile("" : "+a"(Bw[ks][0]), "+a"(Bw[ks][1]));
  bar();
  STAMP(1);
  TAGS();
  const u64 tF = ((u64)tB << 32) | tA;
  f32x4 acc[5][2];
  float rsr, rsi;
  u32x4 kq[4];
  u32x4 Cw[9][4];
  const rsrc_t crs = __builtin_amdgcn_make_buffer_rsrc((void*)((w & 1) ? p.cim : p.cre), 0, D * D * 2, 0x00020000);
  auto cld = [&](int L) {
    const int d = L >> 3, j = L & 3, cb = ((64 * g + (n & 12)) * D + 288 * (w >> 1) + 8 * (lane & 3)) * 2 + j * D * 2;
    if (L >= 32) Cw[8][j] = __builtin_amdgcn_raw_buffer_load_b128(crs, cb + 64 * ((8 + g) % 9) + ((w + kg) & 3) * 32 * D, 0, 0);
    else Cw[L & 7][d] = __builtin_amdgcn_raw_buffer_load_b128(crs, cb + 64 * (((L & 4) + kg + g) % 9), ((w + d) & 3) * 32 * D, 0);
  };
  {
    u32x2 kvq;
    u32 kvo;
    SB();
    STAMP(23);
    gemm_bu<true>(lds, Bw, lane, acc, rsr, rsi, [&](int ks, int i) {
      if (ks < 5 && i == 0) {
        const int it = tid + 256 * ks, ch = it / 20, tq = it - 20 * ch, gq = 20 * c + tq;
        kvq = *(const u32x2*)(lds + L_GI + ch * 168 + tq * 8);
        kvo = gq < 512 ? W_KVT + (64 * g + ch) * 8192 + (511 - gq) * 16 : OOB;
      }
      if (ks < 5 && i == 4)
        stc(ws, kvo, u32x4{__builtin_amdgcn_alignbit(kvq.y, kvq.y, 16), tA, __builtin_amdgcn_alignbit(kvq.x, kvq.x, 16), tB});
      if (ks >= 6 && i == 2) cld(ks - 6);
      if (ks == 16 && i < 4) kq[i] = ldc(ws, W_KVT + ch0 * 8192 + (tid + 256 * i) * 16);
    });
  }
  STAMP(2);
  norm_acc(lds, L_P1, kg, acc, rsr, rsi);
  *(f32x2*)(lds + L_RS + tid * 8) = f32x2{rsr, rsi};
  float inr, ini;
  scan_a(sc, acc, ws, W_E + (c * D + pch) * 16, lane, tA, tB, inr, ini);
  STAMP(3);
  u32x2 dsk_h = ldg8(p.dsk + 64 * g + 16 * w + 4 * kg);
  const u16 sd_h = p.sd[ch0 + (w >> 1)];
  conv_gather<true>(lds + L_CV, ws, ch0, tid, kq, tA, tB, budget);
  bar();
  STAMP(4);
  f32x4 cacc[8];
  u32x4 ev[7];
#pragma unroll
  for (int j = 0; j < 8; ++j) cacc[j] = f32x4{0, 0, 0, 0};
  conv_mma<0, 2>(lds, L_CV, w, lane, cacc, [&](int r, int b) {
    if (b < 6) cld(12 + 6 * r + b);
    if (r == 1 && b == 2) {
#pragma unroll
      for (int j = 0; j < 7; ++j) { const int cp = c - 1 - kg - 4 * j; ev[j] = ldc(ws, W_E + pch * 16 + (cp >= 0 ? cp : 0) * (D * 16)); }
    }
  });
  STAMP(5);
  scan_b<true>(sc, acc, lds, ws, W_E + pch * 16, W_H + (c * 36 + 4 * g + w) * 5120, c, w, lane, tA, tB, inr, ini, ev, budget);
  STAMP(6);
  {
    conv_mma<2, 4>(lds, L_CV, w, lane, cacc, [&](int r, int b) {
      if (b < 6) cld(12 + 6 * r + b);
      if (r == 2 && b == 7) {
        asm volatile("s_waitcnt vmcnt(6)\n\ts_barrier" ::: "memory");
        stc2(ws, tid == 0 ? W_FLG + c * 128 + g * 8 : OOB, u32x2{tA, tB});
      }
    });
  }
  float dk0 = lo16(dsk_h.x), dk1 = hi16(dsk_h.x), dk2 = lo16(dsk_h.y), dk3 = hi16(dsk_h.y);
  f32x4 z[5];
  auto frag_off = [&](int i, int rb) {
    const int tl = 16 * i + n, q = tl / 20, rem = tl - 20 * q;
    return q * rb + (rem >> 2) * (g < 8 ? 4096 : 512) + (g < 8 ? g * 512 : 20480) + (w >> 1) * 256 + (rem & 3) * 64 + (w & 1) * 32 + kg * 8;
  };
#pragma unroll
  for (int i = 0; i < 5; ++i) {
    u32x2 xq = *(const u32x2*)(lds + L_A + frag_off(i, RB));
    f32x2 pr = *(const f32x2*)(lds + L_P1 + (16 * i + n) * 8);
    float d0 = lo16(xq.x) - pr.y, d1 = hi16(xq.x) - pr.y, d2 = lo16(xq.y) - pr.y, d3 = hi16(xq.y) - pr.y;
    u32 a = pk(d0, d1), b = pk(d2, d3);
    a = pk(lo16(a) * pr.x, hi16(a) * pr.x); b = pk(lo16(b) * pr.x, hi16(b) * pr.x);
    a = pk(lo16(a) * dk0, hi16(a) * dk1); b = pk(lo16(b) * dk2, hi16(b) * dk3);
    z[i] = f32x4{d0 + lo16(a), d1 + hi16(a), d2 + lo16(b), d3 + hi16(b)};
  }
  conv_finish(lds, lds + L_CV, ws, ch0, b2f(sd_h), w, lane, cacc, tA, tB);
  *(f32x4*)(lds + L_CV + tid * 16) = f32x4{sc.ar, sc.ai, sc.cr, sc.ci};
  *(f32x4*)(lds + L_CV + 4096 + tid * 16) = f32x4{sc.a20r, sc.a20i, sc.a40r, sc.a40i};
  *(f32x4*)(lds + L_CV + CVSZ + tid * 16) = f32x4{sc.aqr, sc.aqi, sc.bqr, sc.bqi};
  *(f32x2*)(lds + L_CV + CVSZ + 4096 + tid * 8) = f32x2{sc.a320r, sc.a320i};
  *(u32x2*)(lds + L_CV + CVSZ + 6144 + tid * 8) = dsk_h;
  STAMP(7);
  PHASE();
  twa = L_HI + w * 5376 + lane * 16; tra = L_HI + w * 5376 + (n & 3) * 1040 + (n >> 2) * 64;
  auto cltr = [&](int G) {
    if (G < 8) ltr<16, 32, 48>(lds, twa, tra + kg * 256, Cw[4 * (G & 1)][G >> 1], Cw[4 * (G & 1) + 1][G >> 1], Cw[4 * (G & 1) + 2][G >> 1], Cw[4 * (G & 1) + 3][G >> 1]);
    else ltr<256, 512, 768>(lds, twa, tra + kg * 16, Cw[8][0], Cw[8][1], Cw[8][2], Cw[8][3]);
  };
  {
    const u64 pre = poll_flags(p.ws, W_FLG + c * 128, lane);
    SB();
#pragma unroll
    for (int G = 0; G < 4; ++G) cltr(G);
    SB();
    STAMP(24);
    wait_flags(p.ws, W_FLG + c * 128, tF, lane, budget, pre);
  }
  STAMP(8);
  u32 gq[5][2];
  {
    u32 off[5]; bool need[5]; u32x4 v[5];
    auto goff = [&]() {
#pragma unroll
      for (int r = 0; r < 5; ++r) {
        const int it = lane + 64 * r, ch = it / 20, tq = it - 20 * ch, q = 20 * c + tq;
        need[r] = true;
        off[r] = W_GT + (64 * g + 16 * w + ch) * 8192 + (q < 512 ? q : 511) * 16;
      }
    };
    gemm_y<5>(lds, lb, ws, W_H + c * 36 * 5120, Cw, w, g, lane, z, [&](int j) {
      if (j >= 10) cltr(j - 6);
      if (j == -2) cltr(8);
      if (j == -2) STAMP(22);
      if (j == -1) STAMP(29);
      if (j == 4) STAMP(27);
      if (j == 8) STAMP(31);
      if (j != 4) return;
      goff();
#pragma unroll
      for (int r = 0; r < 5; ++r) v[r] = ldc(ws, off[r]);
    });
    STAMP(9);
    PHASE();
    rs_part(lds, z, w, lane);
    bar();
    rs_pub(lds, ws, W_ST + (c * 9 + g) * 1280, tid, tA, tB);
    goff();
    gather<5, true>(ws, off, need, v, tA, tB, budget);
#pragma unroll
    for (int r = 0; r < 5; ++r) {
      const int it = lane + 64 * r, ch = it / 20, tq = it - 20 * ch;
      *(u32x2*)(lds + L_GI + (16 * w + ch) * 168 + tq * 8) = u32x2{v[r].x, v[r].z};
    }
    asm volatile("s_waitcnt lgkmcnt(0)" ::: "memory");
#pragma unroll
    for (int i = 0; i < 5; ++i) {
      u32x2 t = trread(lds, L_GI + (16 * w + 4 * kg + (n >> 2)) * 168 + 8 * (n & 3) + 32 * i);
      gq[i][0] = t.x; gq[i][1] = t.y;
    }
  }
  STAMP(10);
  u32x4 sv[3];
  rs_get<false>(lds, L_PAR, ws, W_ST + c * 9 * 1280, w, lane, sv, tA, tB, budget);
  bar();
  STAMP(11);
#pragma unroll
  for (int i = 0; i < 5; ++i) {
    f32x2 pr = *(const f32x2*)(lds + L_PAR + (16 * i + n) * 8);
    float gv[4] = {lo16(gq[i][0]), hi16(gq[i][0]), lo16(gq[i][1]), hi16(gq[i][1])};
#pragma unroll
    for (int e = 0; e < 4; ++e) { float t = z[i][e] - pr.y; z[i][e] = t + t * pr.x * gv[e]; }
    stc2(ws, W_U3 + c * 94208 + frag_off(i, 23552), u32x2{pk(z[i].x, z[i].y), pk(z[i].z, z[i].w)});
  }
  SB();
  rs_part(lds, z, w, lane);
  drain();
  bar();
  stc2(ws, tid == 0 ? W_FLG + (NC + c) * 128 + g * 8 : OOB, u32x2{tA, tB});
  rs_pub(lds, ws, W_ST + ((NC + c) * 9 + g) * 1280, tid, tA, tB);
  wait_flags(p.ws, W_FLG + (NC + c) * 128, tF, lane, budget, tF + 1);
  STAMP(12);
  PHASE();
  {
    const u32 ub = W_U3 + c * 94208 + w * 23552 + lane * 16;
#pragma unroll
    for (int j = 0; j < 5; ++j) dma_c4(ws, ub, j * 4096, lb + L_A + w * RB + j * 4096);
#pragma unroll
    for (int j = 20; j < 23; ++j) dma_c(ws, ub, j * 1024, lb + L_A + w * RB + j * 1024);
  }
  drain();
  bar();
  STAMP(13);
#pragma unroll
  for (int i = 0; i < 5; ++i) asm volatile("" : "+v"(gq[i][0]), "+v"(gq[i][1]));
  gemm_bu<false>(lds, Bw, lane, acc, rsr, rsi, [&](int ks, int i) {
    if (ks == 12 && i < 3) sv[i] = ldc(ws, W_ST + (NC + c) * 9 * 1280 + rs_off(w, lane) + i * 1280);
  });
  STAMP(25);
  PHASE();
  rs_get<true>(lds, L_P3, ws, W_ST + (NC + c) * 9 * 1280, w, lane, sv, tA, tB, budget);
  bar();
  {
    const f32x2 q = *(const f32x2*)(lds + L_RS + tid * 8);
    norm_acc(lds, L_P3, kg, acc, q.x, q.y);
  }
  {
    f32x4 a = *(const f32x4*)(lds + L_CV + tid * 16), b = *(const f32x4*)(lds + L_CV + 4096 + tid * 16);
    f32x4 e = *(const f32x4*)(lds + L_CV + CVSZ + tid * 16);
    f32x2 f = *(const f32x2*)(lds + L_CV + CVSZ + 4096 + tid * 8);
    sc.ar = a.x; sc.ai = a.y; sc.cr = a.z; sc.ci = a.w; sc.a20r = b.x; sc.a20i = b.y; sc.a40r = b.z; sc.a40i = b.w;
    sc.aqr = e.x; sc.aqi = e.y; sc.bqr = e.z; sc.bqi = e.w; sc.a320r = f.x; sc.a320i = f.y;
  }
  scan_a(sc, acc, ws, W_E + ((NC + c) * D + pch) * 16, lane, tA, tB, inr, ini);
  STAMP(14);
  dsk_h = *(const u32x2*)(lds + L_CV + CVSZ + 6144 + tid * 8);
#pragma unroll
  for (int i = 0; i < 5; ++i) {
    f32x2 pr = *(const f32x2*)(lds + L_P3 + (16 * i + n) * 8);
    const f32x4 t = z[i] - pr.y;
    z[i] = t + t * pr.x * f32x4{lo16(dsk_h.x), hi16(dsk_h.x), lo16(dsk_h.y), hi16(dsk_h.y)};
  }
  SB();
  scan_b<false>(sc, acc, lds, ws, W_E + (NC * D + pch) * 16, W_H + ((NC + c) * 36 + 4 * g + w) * 5120, c, w, lane, tA, tB, inr, ini, ev, budget);
  drain();
  bar();
  stc2(ws, tid == 0 ? W_FLG + (2 * NC + c) * 128 + g * 8 : OOB, u32x2{tA, tB});
  STAMP(15);
  wait_flags(p.ws, W_FLG + (2 * NC + c) * 128, tF, lane, budget, tF + 1);
  STAMP(16);
  {
    gemm_y<0>(lds, lb, ws, W_H + (NC + c) * 36 * 5120, Cw, w, g, lane, z, [](int) {});
    rs_part(lds, z, w, lane);
  }
  bar();
  STAMP(17);
  rs_pub(lds, ws, W_ST + ((2 * NC + c) * 9 + g) * 1280, tid, tA, tB);
  rs_get<false>(lds, L_P2, ws, W_ST + (2 * NC + c) * 9 * 1280, w, lane, sv, tA, tB, budget);
  bar();
  STAMP(18);
#pragma unroll
  for (int i = 0; i < 5; ++i) {
    f32x2 pr = *(const f32x2*)(lds + L_P2 + (16 * i + n) * 8);
    const float cen = *(const float*)(lds + L_P1 + (16 * i + n) * 8 + 4) + *(const float*)(lds + L_PAR + (16 * i + n) * 8 + 4) +
                      *(const float*)(lds + L_P3 + (16 * i + n) * 8 + 4);
    float gv[4] = {lo16(gq[i][0]), hi16(gq[i][0]), lo16(gq[i][1]), hi16(gq[i][1])};
    f32x4 o;
#pragma unroll
    for (int e = 0; e < 4; ++e) o[e] = (z[i][e] + cen) + (z[i][e] - pr.y) * pr.x * gv[e];
    const int t = t0 + 16 * i + n;
    if (t < S) *(f32x4*)(p.out + t * D + 64 * g + 16 * w + 4 * kg) = o;
  }
  if (c == NC - 1 && g == 0 && tid == 0) __hip_atomic_store((gu32*)p.ws, tA, __ATOMIC_RELAXED, __HIP_MEMORY_SCOPE_AGENT);
  STAMP(19);
  DUMP();
}

extern "C" void kernel_launch(void* const* d_in, const int* in_sizes, int n_in, void* d_out, int out_size, void* d_ws,
                              size_t ws_size, hipStream_t stream) {
  Params p;
  p.x = (const u16*)d_in[0]; p.lre = (const u16*)d_in[1]; p.lim = (const u16*)d_in[2];
  p.bre = (const u16*)d_in[3]; p.bim = (const u16*)d_in[4]; p.cre = (const u16*)d_in[5]; p.cim = (const u16*)d_in[6];
  p.ldt = (const u16*)d_in[7]; p.dsk = (const u16*)d_in[8]; p.k = (const u16*)d_in[10]; p.v = (const u16*)d_in[11];
  p.shk = (const u16*)d_in[12]; p.sk = (const u16*)d_in[13]; p.sd = (const u16*)d_in[14];
  p.out = (float*)d_out; p.ws = (unsigned char*)d_ws;
  (void)hipFuncSetAttribute((const void*)fused_kernel, hipFuncAttributeMaxDynamicSharedMemorySize, L_TOT);
  hipLaunchKernelGGL(fused_kernel, dim3(256), dim3(256), L_TOT, stream, p);
}
```

```cpp
#include <hip/hip_runtime.h>
#include <stdint.h>

typedef unsigned short u16;
typedef unsigned int u32;
typedef unsigned long long u64;
typedef __attribute__((ext_vector_type(4))) unsigned int u32x4;
typedef __attribute__((ext_vector_type(2))) unsigned int u32x2;
typedef __attribute__((ext_vector_type(4))) float f32x4;
typedef __attribute__((ext_vector_type(2))) float f32x2;
typedef __attribute__((ext_vector_type(8))) __bf16 bf16x8;
typedef __attribute__((ext_vector_type(2))) __bf16 bf16x2;
typedef __attribute__((ext_vector_type(4))) short s16x4;
typedef __attribute__((address_space(1))) u64 gu64;
typedef __attribute__((address_space(1))) u32 gu32;
typedef __attribute__((address_space(3))) s16x4 lds_s16x4;
typedef __amdgpu_buffer_rsrc_t rsrc_t;

#define DI __device__ __forceinline__
#define SB() __builtin_amdgcn_sched_barrier(0)

constexpr int S = 2048, D = 576, NC = 26;
constexpr u32 W_FLG = 4096;
constexpr u32 W_E = 32768;
constexpr u32 W_ST = W_E + 2 * NC * D * 16;
constexpr u32 W_KVT = 4u << 20;
constexpr u32 W_GT = W_KVT + D * 8192;
constexpr u32 W_H = W_GT + D * 8192;
constexpr u32 W_U3 = W_H + 2 * NC * 36 * 5120;
constexpr u32 W_END = W_U3 + NC * 94208;
constexpr u32 W_DBG = 64u << 20;
constexpr int L_A = 0;
constexpr int CVSZ = 13056, CV_K1 = 4224, CV_R = 8448;
constexpr int RB = 23568;
constexpr u32 OOB = 0x7ffffff0u;
constexpr int L_CV = 4 * RB;
constexpr int L_HI = L_CV + 2 * CVSZ;
constexpr int L_GI = L_HI + 4 * 5376;
constexpr int L_SP = L_GI + 64 * 168;
constexpr int L_PAR = L_SP + 2560;
constexpr int L_P3 = L_PAR + 640;
constexpr int L_P1 = L_PAR + 1280;
constexpr int L_P2 = L_P1 + 640;
constexpr int L_RS = L_P2 + 640;
constexpr int L_TOT = L_RS + 2048 + 256;

struct Params {
  const u16 *x, *lre, *lim, *bre, *bim, *cre, *cim, *ldt, *dsk, *k, *v, *shk, *sk, *sd;
  float* out;
  unsigned char* ws;
};

DI float lo16(u32 w) { return __uint_as_float(w << 16); }
DI float hi16(u32 w) { return __uint_as_float(w & 0xffff0000u); }
DI float b2f(u16 h) { return __uint_as_float(((u32)h) << 16); }
DI u32 pk(float a, float b) {
  f32x2 v = {a, b};
  return __builtin_bit_cast(u32, __builtin_convertvector(v, bf16x2));
}
DI float rb(float a) { return lo16(pk(a, a)); }
DI f32x4 mma(u32x4 a, u32x4 b, f32x4 c) {
  return __builtin_amdgcn_mfma_f32_16x16x32_bf16(__builtin_bit_cast(bf16x8, a), __builtin_bit_cast(bf16x8, b), c, 0, 0, 0);
}
DI float dot2(u32 a, u32 b, float c) {
  return __builtin_amdgcn_fdot2_f32_bf16(__builtin_bit_cast(bf16x2, a), __builtin_bit_cast(bf16x2, b), c, false);
}
DI void bar() { asm volatile("s_waitcnt lgkmcnt(0)\n\ts_barrier" ::: "memory"); }
DI void drain() { asm volatile("s_waitcnt vmcnt(0)" ::: "memory"); }
DI void cfence() { asm volatile("" ::: "memory"); }
DI u32x4 ldc(rsrc_t r, u32 off) { return __builtin_amdgcn_raw_buffer_load_b128(r, off, 0, 16); }
DI u32x4 ldcs(rsrc_t r, u32 off, u32 soff) { return __builtin_amdgcn_raw_buffer_load_b128(r, off, soff, 16); }
DI void stc(rsrc_t r, u32 off, u32x4 v) { __builtin_amdgcn_raw_buffer_store_b128(v, r, off, 0, 16); }
DI void stc2(rsrc_t r, u32 off, u32x2 v) { __builtin_amdgcn_raw_buffer_store_b64(v, r, off, 0, 16); }
DI u32x4 ldg16(const u16* p) { return *(const u32x4*)p; }
DI u32x2 ldg8(const u16* p) { return *(const u32x2*)p; }
DI u32x2 trread(unsigned char* lds, int off) {
  s16x4 r = __builtin_amdgcn_ds_read_tr16_b64_v4i16((lds_s16x4*)(lds + off));
  return __builtin_bit_cast(u32x2, r);
}
DI void cmul(float ar, float ai, float br, float bi, float& cr, float& ci) {
  float r = ar * br - ai * bi, i = ar * bi + ai * br;
  cr = r; ci = i;
}
DI float red32(float a, float b) {
  auto r = __builtin_amdgcn_permlane32_swap(__float_as_uint(a), __float_as_uint(b), false, false);
  return __uint_as_float(r[0]) + __uint_as_float(r[1]);
}
DI float red16(float a, float b) {
  auto r = __builtin_amdgcn_permlane16_swap(__float_as_uint(a), __float_as_uint(b), false, false);
  return __uint_as_float(r[0]) + __uint_as_float(r[1]);
}

DI void pair16(float a, float& e, float& o) {
  auto r = __builtin_amdgcn_permlane16_swap(__float_as_uint(a), __float_as_uint(a), false, false);
  e = __uint_as_float(r[0]); o = __uint_as_float(r[1]);
}
DI void pair32(float a, float& e, float& o) {
  auto r = __builtin_amdgcn_permlane32_swap(__float_as_uint(a), __float_as_uint(a), false, false);
  e = __uint_as_float(r[0]); o = __uint_as_float(r[1]);
}
DI float sum3(float a, int lane) {
  return a + __int_as_float(__builtin_amdgcn_ds_bpermute(lane * 4 + 80, __float_as_int(a))) +
         __int_as_float(__builtin_amdgcn_ds_bpermute(lane * 4 + 160, __float_as_int(a)));
}
typedef __attribute__((address_space(3))) void lds_void;
typedef __attribute__((address_space(1))) const void gvoid;
template <int O1, int O2, int O3>
DI void ltr(unsigned char* lds, int wa, int ra, u32x4& r0, u32x4& r1, u32x4& r2, u32x4& r3) {
  *(u32x4*)(lds + wa) = r0; *(u32x4*)(lds + wa + 1040) = r1; *(u32x4*)(lds + wa + 2080) = r2; *(u32x4*)(lds + wa + 3120) = r3;
  cfence();
  r0 = *(const u32x4*)(lds + ra); r1 = *(const u32x4*)(lds + ra + O1);
  r2 = *(const u32x4*)(lds + ra + O2); r3 = *(const u32x4*)(lds + ra + O3);
  cfence();
}
template <int OFF>
DI void dma_g(const u16* base, u32 voff, u32 ldsa) {
  asm volatile("s_mov_b32 m0, %2\n\ts_nop 0\n\tglobal_load_lds_dwordx4 %0, %1 offset:%3" :: "v"(voff), "s"(base), "s"(ldsa - OFF), "n"(OFF) : "memory");
}
DI void dma_c(rsrc_t r, u32 voff, u32 soff, u32 ldsa) {
  asm volatile("s_mov_b32 m0, %3\n\ts_nop 0\n\tbuffer_load_dwordx4 %0, %1, %2 offen sc1 lds" :: "v"(voff), "s"(r), "s"(soff), "s"(ldsa) : "memory");
}
DI float quad_sum(float a) {
  a += __int_as_float(__builtin_amdgcn_update_dpp(0, __float_as_int(a), 0xb1, 0xf, 0xf, true));
  a += __int_as_float(__builtin_amdgcn_update_dpp(0, __float_as_int(a), 0x4e, 0xf, 0xf, true));
  return a;
}
constexpr unsigned SPIN_MAX = 1u << 20;
DI bool spin_next(unsigned& budget) {
  if (budget == 0) return false;
  --budget;
  __builtin_amdgcn_s_sleep(1);
  return true;
}
template <int N, bool PRE = false>
DI void gather(rsrc_t ws, const u32 (&off)[N], const bool (&need)[N], u32x4 (&v)[N], u32 tA, u32 tB, unsigned& budget) {
  if (PRE) {
    bool ok = true;
#pragma unroll
    for (int i = 0; i < N; ++i) ok &= (!need[i]) | ((v[i].y == tA) & (v[i].w == tB));
    if (__all(ok)) return;
  }
  for (;;) {
    cfence();
    bool ok = true;
#pragma unroll
    for (int i = 0; i < N; ++i) v[i] = ldc(ws, off[i]);
#pragma unroll
    for (int i = 0; i < N; ++i) ok &= (!need[i]) | ((v[i].y == tA) & (v[i].w == tB));
    if (__all(ok)) break;
    if (!spin_next(budget)) break;
  }
  cfence();
}
DI u64 poll_flags(unsigned char* wsp, u32 off, int lane) {
  return __hip_atomic_load((gu64*)(wsp + off) + (lane < 9 ? lane : 0), __ATOMIC_RELAXED, __HIP_MEMORY_SCOPE_AGENT);
}
DI void wait_flags(unsigned char* wsp, u32 off, u64 tag, int lane, unsigned& budget, u64 pre) {
  gu64* f = (gu64*)(wsp + off) + (lane < 9 ? lane : 0);
  if (!__all(pre == tag)) for (;;) {
    u64 x = __hip_atomic_load(f, __ATOMIC_RELAXED, __HIP_MEMORY_SCOPE_AGENT);
    if (__all(x == tag)) break;
    if (!spin_next(budget)) break;
  }
  __builtin_amdgcn_fence(__ATOMIC_ACQUIRE, "wavefront");
  cfence();
}

#define STAMP(i) do {} while (0)

template <int V> struct IC { static constexpr int v = V; constexpr operator int() const { return V; } };
template <int N, int K = 0, class F>
DI void static_for(F f) { if constexpr (K < N) { f(IC<K>{}); static_for<N, K + 1>(f); } }
constexpr int NPIN = 7;
template <int N>
DI void vmw() { asm volatile("s_waitcnt vmcnt(%0)" :: "n"(N) : "memory"); }
DI void conv_stage_k(unsigned char* cv, u32x4 kx, u32 knext, int tid) {
  *(u32x4*)(cv + 32 + 16 * tid) = kx;
  u32x4 s;
  s.x = __builtin_amdgcn_alignbit(kx.y, kx.x, 16);
  s.y = __builtin_amdgcn_alignbit(kx.z, kx.y, 16);
  s.z = __builtin_amdgcn_alignbit(kx.w, kx.z, 16);
  s.w = __builtin_amdgcn_alignbit(knext, kx.w, 16);
  *(u32x4*)(cv + CV_K1 + 32 + 16 * tid) = s;
  u32x4 z = {0, 0, 0, 0};
  if (tid == 0) {
    *(u32x4*)(cv) = z; *(u32x4*)(cv + 16) = z; *(u32x4*)(cv + CV_K1) = z;
    u32x4 f = {0, 0, 0, kx.x << 16};
    *(u32x4*)(cv + CV_K1 + 16) = f;
  }
  if (tid >= 64 && tid < 96) *(u32x4*)(cv + CV_R + 4096 + 16 * (tid - 64)) = z;
}
template <bool PRE>
DI void conv_gather(unsigned char* cv, rsrc_t ws, int ch0, int tid, u32x4 (&v)[4], u32 tA, u32 tB, unsigned& budget) {
  u32 off[4]; bool need[4];
#pragma unroll
  for (int r = 0; r < 4; ++r) { off[r] = W_KVT + ch0 * 8192 + (tid + 256 * r) * 16; need[r] = true; }
  gather<4, PRE>(ws, off, need, v, tA, tB, budget);
#pragma unroll
  for (int r = 0; r < 4; ++r) {
    u32x2 w = {v[r].x, v[r].z};
    *(u32x2*)(cv + (r >> 1) * CVSZ + CV_R + ((r & 1) * 256 + tid) * 8) = w;
  }
}
template <int R0, int R1, class F>
DI void conv_mma(unsigned char* lds, int cvo, int w, int lane, f32x4 (&acc)[8], F hook) {
  const int m = lane & 15, kg = lane >> 4;
  const int cb = cvo + (w >> 1) * CVSZ + (w & 1) * 256;
  int ka[4];
  ka[0] = cb + ((m & 1) ? 0 : CV_K1) + 16 * kg + 4 * ((m + 1) >> 1);
  ka[1] = ka[0] + 1024; ka[2] = ka[0] + 2048; ka[3] = ka[0] + 3072;
  int rbp = cb + CV_R + 480 - 32 * m + 16 * kg;
  asm volatile("" : "+v"(ka[0]), "+v"(ka[1]), "+v"(ka[2]), "+v"(ka[3]), "+v"(rbp));
  u32x4 A[8], B[8];
  auto ldA = [&](int r, int a) {
    const u32* q = (const u32*)(lds + ka[a >> 1] + 64 * r + 512 * (a & 1));
    A[a].x = q[0]; A[a].y = q[1]; A[a].z = q[2]; A[a].w = q[3];
  };
  auto ldB = [&](int r, int b) { B[b] = *(const u32x4*)(lds + rbp + 64 * r + 512 * (7 - b)); };
  ldB(R0, 0);
#pragma unroll
  for (int a = 7; a >= 0; --a) ldA(R0, a);
#pragma unroll
  for (int b = 1; b < 8; ++b) ldB(R0, b);
  SB();
#pragma unroll
  for (int r = R0; r < R1; ++r) {
#pragma unroll
    for (int b = 0; b < 8; ++b) {
#pragma unroll
      for (int a = 7 - b; a >= 0; --a) acc[a + b] = mma(A[a], B[b], acc[a + b]);
      SB();
      if (r + 1 < R1) { ldB(r + 1, b); ldA(r + 1, 7 - b); }
      hook(r, b);
      SB();
    }
  }
}
DI void conv_finish(unsigned char* lds, unsigned char* cv, rsrc_t ws, int ch0, float sdv, int w, int lane,
                    f32x4 (&acc)[8], u32 tA, u32 tB) {
  const int n = lane & 15, kg = lane >> 4, odd = w & 1;
  unsigned char* box = lds + L_HI + (w ^ 1) * 5376;
#pragma unroll
  for (int j = 0; j < 4; ++j) {
    f32x4 lo = acc[j], hi = acc[4 + j], snd, kp;
#pragma unroll
    for (int e = 0; e < 4; ++e) { snd[e] = odd ? lo[e] : hi[e]; kp[e] = odd ? hi[e] : lo[e]; }
    *(f32x4*)(box + j * 1024 + lane * 16) = snd;
    acc[j] = kp;
  }
  bar();
  const unsigned char* mine = lds + L_HI + w * 5376;
  const unsigned char* cb = cv + (w >> 1) * CVSZ + CV_R;
#pragma unroll
  for (int j = 0; j < 4; ++j) {
    f32x4 y = acc[j] + *(const f32x4*)(mine + j * 1024 + lane * 16);
    const int X = 256 * (4 * odd + j) + 16 * n + 4 * kg;
    u32x2 rv = *(const u32x2*)(cb + (2044 - X) * 2);
    u32 y01 = pk(y.x, y.y), y23 = pk(y.z, y.w);
    u32 s01 = pk(hi16(rv.y) * sdv, lo16(rv.y) * sdv), s23 = pk(hi16(rv.x) * sdv, lo16(rv.x) * sdv);
    u32x4 o;
    o.x = pk(lo16(y01) + lo16(s01), hi16(y01) + hi16(s01)); o.y = tA;
    o.z = pk(lo16(y23) + lo16(s23), hi16(y23) + hi16(s23)); o.w = tB;
    stc(ws, W_GT + (ch0 + (w >> 1)) * 8192 + X * 4, o);
  }
}

struct Scan {
  float ar, ai, cr, ci, a20r, a20i, a40r, a40i, aqr, aqi, bqr, bqi, a320r, a320i;
};

template <bool RS, class F>
DI void gemm_bu(const unsigned char* lds, u32x4 (&Bw)[18][2], int lane, f32x4 (&acc)[5][2], float& rsr, float& rsi, F pre) {
#pragma unroll
  for (int i = 0; i < 5; ++i) { acc[i][0] = f32x4{0, 0, 0, 0}; acc[i][1] = f32x4{0, 0, 0, 0}; }
  int ab0, ab1;
  {
    const int n = lane & 15, kg = lane >> 4;
    ab0 = L_A + (n >> 2) * RB + kg * 256 + (n & 3) * 64;
    ab1 = L_A + (n >> 2) * RB + 20480 + (kg & 1) * 256 + (n & 3) * 64 + (kg >> 1) * 16;
  }
  asm volatile("" : "+v"(ab0), "+v"(ab1));
  auto rdA = [&](int ks, int i) {
    return *(const u32x4*)(lds + (ks < 16 ? ab0 + i * 4096 + (ks >> 2) * 1024 + (ks & 3) * 16 : ab1 + i * 512 + (ks & 1) * 32));
  };
  f32x4 r0 = {0, 0, 0, 0}, r1 = {0, 0, 0, 0};
  const u32x4 ones = {0x3f803f80u, 0x3f803f80u, 0x3f803f80u, 0x3f803f80u};
  u32x4 a[2][5];
#pragma unroll
  for (int i = 0; i < 5; ++i) a[0][i] = rdA(0, i);
  SB();
#pragma unroll
  for (int ks = 0; ks < 18; ++ks) {
    asm("" : "+a"(Bw[ks][0]), "+a"(Bw[ks][1]));
#pragma unroll
    for (int i = 0; i < 5; ++i) {
      if (ks + 1 < 18) a[(ks + 1) & 1][i] = rdA(ks + 1, i);
      acc[i][0] = mma(a[ks & 1][i], Bw[ks][0], acc[i][0]);
      acc[i][1] = mma(a[ks & 1][i], Bw[ks][1], acc[i][1]);
      pre(ks, i);
      SB();
    }
    if (RS) { r0 = mma(ones, Bw[ks][0], r0); r1 = mma(ones, Bw[ks][1], r1); SB(); }
  }
  if (RS) { rsr = r0.x; rsi = r1.x; }
}
DI void scan_a(const Scan& sc, const f32x4 (&acc)[5][2], rsrc_t ws, u32 eoff, int lane, u32 tA, u32 tB, float& inr, float& ini) {
  float er = 0.f, ei = 0.f;
#pragma unroll
  for (int i = 0; i < 5; ++i)
#pragma unroll
    for (int r = 0; r < 4; ++r) {
      float nr = sc.ar * er + (acc[i][0][r] - sc.ai * ei), ni = sc.ar * ei + (acc[i][1][r] + sc.ai * er);
      er = nr; ei = ni;
    }
  const int q = lane >> 4;
  float e0r, e1r, e0i, e1i, pr, pi, p0r, p1r, p0i, p1i, tr, ti;
  pair16(er, e0r, e1r); pair16(ei, e0i, e1i);
  cmul(sc.a20r, sc.a20i, e0r, e0i, pr, pi);
  pr += e1r; pi += e1i;
  pair32(pr, p0r, p1r); pair32(pi, p0i, p1i);
  cmul(sc.a40r, sc.a40i, p0r, p0i, tr, ti);
  stc(ws, q == 0 ? eoff : OOB, u32x4{__float_as_uint(tr + p1r), tA, __float_as_uint(ti + p1i), tB});
  cmul(sc.a20r, sc.a20i, p0r, p0i, tr, ti);
  inr = q == 0 ? 0.f : q == 1 ? e0r : q == 2 ? p0r : tr + e0r;
  ini = q == 0 ? 0.f : q == 1 ? e0i : q == 2 ? p0i : ti + e0i;
}
template <bool PRE>
DI void scan_b(const Scan& sc, const f32x4 (&acc)[5][2], unsigned char* lds, rsrc_t ws, u32 ebase, u32 hoff, int c, int w,
               int lane, u32 tA, u32 tB, float inr, float ini, u32x4 (&v)[7], unsigned& budget) {
  const int n = lane & 15, q = lane >> 4;
  u32 off[7]; bool need[7];
#pragma unroll
  for (int j = 0; j < 7; ++j) {
    int cp = c - 1 - q - 4 * j;
    need[j] = cp >= 0;
    off[j] = ebase + (cp >= 0 ? cp : 0) * (D * 16);
  }
  gather<7, PRE>(ws, off, need, v, tA, tB, budget);
  float hr = 0.f, hi = 0.f;
#pragma unroll
  for (int j = 6; j >= 0; --j) {
    cmul(sc.a320r, sc.a320i, hr, hi, hr, hi);
    hr += need[j] ? __uint_as_float(v[j].x) : 0.f;
    hi += need[j] ? __uint_as_float(v[j].z) : 0.f;
  }
  cmul(sc.bqr, sc.bqi, hr, hi, hr, hi);
  hr = red16(hr, hr); hi = red16(hi, hi);
  hr = red32(hr, hr); hi = red32(hi, hi);
  cmul(sc.aqr, sc.aqi, hr, hi, hr, hi);
  hr += inr; hi += ini;
  unsigned char* img = lds + L_HI + w * 5376;
#pragma unroll
  for (int i = 0; i < 5; ++i) {
    float o_r[4], o_i[4];
#pragma unroll
    for (int r = 0; r < 4; ++r) {
      float nr = sc.ar * hr + (acc[i][0][r] - sc.ai * hi), ni = sc.ar * hi + (acc[i][1][r] + sc.ai * hr);
      hr = nr; hi = ni;
      o_r[r] = sc.cr * hr - sc.ci * hi;
      o_i[r] = -(sc.cr * hi) - sc.ci * hr;
    }
    *(u32x2*)(img + n * 168 + 40 * q + 8 * i) = u32x2{pk(o_r[0], o_r[1]), pk(o_r[2], o_r[3])};
    *(u32x2*)(img + (16 + n) * 168 + 40 * q + 8 * i) = u32x2{pk(o_i[0], o_i[1]), pk(o_i[2], o_i[3])};
  }
  asm volatile("s_waitcnt lgkmcnt(0)" ::: "memory");
  const int tro = L_HI + w * 5376 + (8 * q + (n >> 2)) * 168 + 8 * (n & 3);
#pragma unroll
  for (int i = 0; i < 5; ++i) {
    u32x2 a = trread(lds, tro + 32 * i), b = trread(lds, tro + 4 * 168 + 32 * i);
    stc(ws, hoff + i * 1024 + lane * 16, u32x4{a.x, a.y, b.x, b.y});
  }
}
DI void dma_c4(rsrc_t r, u32 voff, u32 soff, u32 mb) {
  asm volatile("s_mov_b32 m0, %3\n\ts_nop 0\n\tbuffer_load_dwordx4 %0, %1, %2 offen sc1 lds\n\t"
               "buffer_load_dwordx4 %0, %1, %2 offen offset:1024 sc1 lds\n\tbuffer_load_dwordx4 %0, %1, %2 offen offset:2048 sc1 lds\n\t"
               "buffer_load_dwordx4 %0, %1, %2 offen offset:3072 sc1 lds" :: "v"(voff), "s"(r), "s"(soff), "s"(mb) : "memory");
}
template <int NX, class F>
DI void gemm_y(unsigned char* lds, u32 lb, rsrc_t ws, u32 hbase, u32x4 (&Cw)[9][4], int w, int g, int lane, f32x4 (&y)[5], F mid) {
  f32x4 acc[4][5];
#pragma unroll
  for (int d = 0; d < 4; ++d)
#pragma unroll
    for (int i = 0; i < 5; ++i) acc[d][i] = d ? f32x4{0, 0, 0, 0} : y[i];
  u32 ho[3];
  {
    const int n = lane & 15, kg = lane >> 4;
    const u32 hb = hbase + (w >> 1) * 18 * 5120 + (w & 1) * 512 + n * 16;
    ho[0] = hb + ((kg + g) % 9) * 10240;
    ho[1] = hb + ((4 + kg + g) % 9) * 10240;
    ho[2] = hb + ((8 + g) % 9) * 10240 + (kg >> 1) * 5120 + (kg & 1) * 256;
  }
  const u32 sa = lb + L_A + w * RB, sh = lb + L_HI + w * 5376;
  auto dmaH = [&](int j) {
    const u32 so = ((j >> 1) & 1) * 5120 + (j & 1) * 256, mb = j == 4 ? sh : sa + (j % 5) * 5120;
    dma_c4(ws, ho[j >> 2], so, mb); dma_c(ws, ho[j >> 2], so + 4096, mb + 4096);
  };
  int ra = L_A + w * RB + lane * 16, rh = L_HI + w * 5376 + lane * 16;
  asm volatile("" : "+v"(ra), "+v"(rh));
  auto rdH = [&](int j, int i) { return *(const u32x4*)(lds + (j == 4 ? rh : ra + (j % 5) * 5120) + i * 1024); };
#pragma unroll
  for (int j = 0; j < 4; ++j) { dmaH(j); mid(10 + j); }
  u32x4 hf[2][5];
  mid(-2);
  vmw<15>();
  mid(-1);
#pragma unroll
  for (int i = 0; i < 5; ++i) hf[0][i] = rdH(0, i);
  SB();
  static_for<45>([&](auto I) {
    constexpr int j = I / 5, i = I % 5;
    if (i == 0 && j < NPIN) asm("" : "+a"(Cw[j][0]));
    if (j < 8) {
      vmw<(j < 5 ? 14 : j < 8 ? 39 - I + NX : 0)>();
      hf[(j + 1) & 1][i] = rdH(j + 1, i);
    }
    acc[3][i] = mma(Cw[j][3], hf[j & 1][i], acc[3][i]);
    acc[2][i] = mma(Cw[j][2], hf[j & 1][i], acc[2][i]);
    SB();
    if constexpr (j < 5) {
      constexpr int J = j + 4;
      dma_c(ws, ho[J >> 2], ((J >> 1) & 1) * 5120 + (J & 1) * 256 + i * 1024, (J == 4 ? sh : sa + (J % 5) * 5120) + i * 1024);
    }
    if (j == 8 && i > 0) {
#pragma unroll
      for (int d = 1; d < 4; ++d) *(f32x4*)(lds + ra + ((d - 1) * 5 + i - 1) * 1024) = acc[d][i > 0 ? i - 1 : 0];
    }
    SB();
    acc[1][i] = mma(Cw[j][1], hf[j & 1][i], acc[1][i]);
    acc[0][i] = mma(Cw[j][0], hf[j & 1][i], acc[0][i]);
    SB();
    if (i == 4) mid(j);
    SB();
  });
#pragma unroll
  for (int d = 1; d < 4; ++d) *(f32x4*)(lds + ra + ((d - 1) * 5 + 4) * 1024) = acc[d][4];
  bar();
  const unsigned char* b1 = lds + L_A + ((w + 3) & 3) * RB + lane * 16;
  const unsigned char* b2 = lds + L_A + ((w + 2) & 3) * RB + 5120 + lane * 16;
  const unsigned char* b3 = lds + L_A + ((w + 1) & 3) * RB + 10240 + lane * 16;
#pragma unroll
  for (int i = 0; i < 5; ++i)
    y[i] = (acc[0][i] + *(const f32x4*)(b1 + i * 1024)) + (*(const f32x4*)(b2 + i * 1024) + *(const f32x4*)(b3 + i * 1024));
}
DI void rs_part(unsigned char* lds, const f32x4 (&z)[5], int w, int lane) {
  const int n = lane & 15, kg = lane >> 4;
#pragma unroll
  for (int i = 0; i < 5; ++i) {
    float s0 = (z[i].x + z[i].y) + (z[i].z + z[i].w);
    float s1 = (z[i].x * z[i].x + z[i].y * z[i].y) + (z[i].z * z[i].z + z[i].w * z[i].w);
    float s = red32(s0, s1);
    *(float*)(lds + L_SP + (w * 80 + 16 * i + n) * 8 + (kg >> 1) * 4) = red16(s, s);
  }
}
DI void rs_pub(unsigned char* lds, rsrc_t ws, u32 base, int tid, u32 tA, u32 tB) {
  const int r = tid < 80 ? tid : 0;
  f32x2 a = *(const f32x2*)(lds + L_SP + r * 8), b = *(const f32x2*)(lds + L_SP + (80 + r) * 8);
  f32x2 e = *(const f32x2*)(lds + L_SP + (160 + r) * 8), f = *(const f32x2*)(lds + L_SP + (240 + r) * 8);
  a = (a + b) + (e + f);
  stc(ws, tid < 80 ? base + tid * 16 : OOB, u32x4{__float_as_uint(a.x), tA, __float_as_uint(a.y), tB});
}
DI u32 rs_off(int w, int lane) {
  const int l3 = lane < 60 ? lane : 0, th = l3 / 20;
  return (240 * th + 20 * w + l3 - 20 * th) * 16;
}
template <bool PRE>
DI void rs_get(unsigned char* lds, int lout, rsrc_t ws, u32 base, int w, int lane, u32x4 (&v)[3], u32 tA, u32 tB, unsigned& budget) {
  u32 off[3]; bool need[3];
#pragma unroll
  for (int j = 0; j < 3; ++j) { off[j] = base + rs_off(w, lane) + j * 1280; need[j] = true; }
  gather<3, PRE>(ws, off, need, v, tA, tB, budget);
  float s0 = __uint_as_float(v[0].x) + __uint_as_float(v[1].x) + __uint_as_float(v[2].x);
  float s1 = __uint_as_float(v[0].z) + __uint_as_float(v[1].z) + __uint_as_float(v[2].z);
  s0 = sum3(s0, lane) * (1.f / D);
  s1 = sum3(s1, lane) * (1.f / D);
  if (lane < 20) *(f32x2*)(lds + lout + (20 * w + lane) * 8) = f32x2{1.f / sqrtf(fmaxf(s1 - s0 * s0, 0.f) + 1e-5f), s0};
}
DI void norm_acc(const unsigned char* lds, int lp, int kg, f32x4 (&acc)[5][2], float rsr, float rsi) {
#pragma unroll
  for (int i = 0; i < 5; ++i) {
    f32x4 pa = *(const f32x4*)(lds + lp + (20 * kg + 4 * i) * 8), pb = *(const f32x4*)(lds + lp + (20 * kg + 4 * i) * 8 + 16);
    acc[i][0].x = (acc[i][0].x - pa.y * rsr) * pa.x; acc[i][1].x = (acc[i][1].x - pa.y * rsi) * pa.x;
    acc[i][0].y = (acc[i][0].y - pa.w * rsr) * pa.z; acc[i][1].y = (acc[i][1].y - pa.w * rsi) * pa.z;
    acc[i][0].z = (acc[i][0].z - pb.y * rsr) * pb.x; acc[i][1].z = (acc[i][1].z - pb.y * rsi) * pb.x;
    acc[i][0].w = (acc[i][0].w - pb.w * rsr) * pb.z; acc[i][1].w = (acc[i][1].w - pb.w * rsi) * pb.z;
  }
}
__global__ void __launch_bounds__(256, 1) fused_kernel(Params p) {
  extern __shared__ __attribute__((aligned(16))) unsigned char lds[];
  int tid = threadIdx.x, lane = tid & 63;
  const int w = __builtin_amdgcn_readfirstlane(tid >> 6);
  const int xcd = blockIdx.x & 7, slot = blockIdx.x >> 3;
  int c, g, spare = -1;
  if (slot < 27) { c = xcd * 3 + slot / 9; g = slot % 9; }
  else {
    int e = (xcd & 3) * 5 + (slot - 27);
    if (xcd < 4) { int e2 = (xcd & 1) * 5 + (slot - 27); if (e2 >= 9) return; c = 24 + (xcd >> 1); g = e2; }
    else { if (e >= 18) return; spare = e; c = 0; g = e >> 1; }
  }
#define DUMP() do {} while (0)
  STAMP(0);
  {
    u64 pc; u32 len;
    asm volatile("s_getpc_b64 %0\n.Lpf%=:\n\ts_mov_b32 %1, .Lkend_mark-.Lpf%=" : "=s"(pc), "=s"(len));
    const u32 o = (((slot & 7) * 4 + w) * 16 + lane) * 128;
    u64 a = pc + ((int)o < (int)len ? o : 0u);
    if (lane < 16) asm volatile("s_mov_b32 m0, %1\n\ts_nop 0\n\tglobal_load_lds_dword %0, off" :: "v"(a), "s"((u32)(size_t)(lds_void*)lds + L_TOT - 256) : "memory");
  }
  const rsrc_t ws = __builtin_amdgcn_make_buffer_rsrc(p.ws, 0, W_END, 0x00020000);
  u32 ep = *(const __attribute__((address_space(4))) u32*)p.ws;
#define TAGS() asm volatile("" : "+s"(ep)); const u32 tA = ep + 1u; \
  const u32 tB = tA * 0x9E3779B1u + 0x7F4A7C15u
  unsigned budget = SPIN_MAX;
  int n = lane & 15, kg = lane >> 4;
#define PHASE() do { asm volatile("" : "+v"(tid)); lane = tid & 63; n = lane & 15; kg = lane >> 4; pch = 64 * g + 16 * w + n; } while (0)

  if (spare >= 0) {
    const int ch0 = 64 * g + 52 + 6 * (spare & 1);
#pragma unroll
    for (int i = 0; i < 6; ++i) {
      const u16* kp = p.sk + (ch0 + i) * S + 8 * tid;
      u32x4 kx = ldg16(kp);
      u32 kn = tid < 255 ? kp[8] : 0;
      conv_stage_k(lds + i * CVSZ, kx, kn, tid);
    }
    u32 sd0 = p.sd[ch0 + (w >> 1)], sd1 = p.sd[ch0 + 2 + (w >> 1)], sd2 = p.sd[ch0 + 4 + (w >> 1)];
    TAGS();
    u32x4 kq[3][4];
    conv_gather<false>(lds, ws, ch0, tid, kq[0], tA, tB, budget);
#pragma unroll
    for (int r = 4; r < 12; ++r) kq[r >> 2][r & 3] = ldc(ws, W_KVT + ch0 * 8192 + (tid + 256 * r) * 16);
    conv_gather<true>(lds + 2 * CVSZ, ws, ch0 + 2, tid, kq[1], tA, tB, budget);
    conv_gather<true>(lds + 4 * CVSZ, ws, ch0 + 4, tid, kq[2], tA, tB, budget);
#pragma unroll 1
    for (int r = 0; r < 3; ++r) {
      const float sdv = lo16(r == 0 ? sd0 : r == 1 ? sd1 : sd2);
      bar();
      f32x4 acc[8];
#pragma unroll
      for (int j = 0; j < 8; ++j) acc[j] = f32x4{0, 0, 0, 0};
      conv_mma<0, 4>(lds, 2 * r * CVSZ, w, lane, acc, [](int, int) {});
      conv_finish(lds, lds + 2 * r * CVSZ, ws, ch0 + 2 * r, sdv, w, lane, acc, tA, tB);
    }
    asm volatile(".Lkend_mark:");
    STAMP(15);
    DUMP();
    return;
  }

  const int t0 = 80 * c;
  const u32 lb = (u32)(size_t)(lds_void*)lds;
  u32 xo[8];
  {
    const int r = (lane >> 2) & 3, jj = lane & 3;
#pragma unroll
    for (int i = 0; i < 5; ++i) {
      int t = t0 + 20 * w + 4 * i + r; t = t > S - 1 ? S - 1 : t;
      xo[i] = (t * D + 32 * kg + 8 * jj) * 2;
    }
#pragma unroll
    for (int u = 0; u < 3; ++u) {
      int t = t0 + 20 * w + 8 * u + 4 * (kg >> 1) + r; t = t > S - 1 ? S - 1 : t;
      xo[5 + u] = (t * D + 512 + 32 * (kg & 1) + 8 * jj) * 2;
    }
  }
  auto xdma = [&](int pc) {
    const u32 la = lb + L_A + w * RB + (pc >> 1) * 4096;
    if (pc & 1) { dma_g<512>(p.x, xo[pc >> 1], la + 2048); dma_g<768>(p.x, xo[pc >> 1], la + 3072); }
    else { dma_g<0>(p.x, xo[pc >> 1], la); dma_g<256>(p.x, xo[pc >> 1], la + 1024); }
    if (pc < 3) dma_g<0>(p.x, xo[5 + pc], lb + L_A + w * RB + 20480 + pc * 1024);
  };
  STAMP(26);
  u32x2 kk[8], vv[5];
  u32x4 shw[2];
  {
    const int col = 64 * g + 4 * (tid & 15), r0 = t0 + 5 * (tid >> 4);
#pragma unroll
    for (int j = 0; j < 8; ++j) { int t = r0 + j - 3; t = t < 0 ? 0 : (t > S - 1 ? S - 1 : t); kk[j] = ldg8(p.k + t * D + col); }
#pragma unroll
    for (int j = 0; j < 5; ++j) { int t = r0 + j; t = t > S - 1 ? S - 1 : t; vv[j] = ldg8(p.v + t * D + col); }
    shw[0] = ldg16(p.shk + col * 4); shw[1] = ldg16(p.shk + col * 4 + 8);
  }
  const int ch0 = 64 * g + 2 * c;
  u32x4 kx0 = ldg16(p.sk + ch0 * S + 8 * tid), kx1 = ldg16(p.sk + (ch0 + 1) * S + 8 * tid);
  u32 kn0 = p.sk[ch0 * S + 8 * tid + (tid < 255 ? 8 : 0)], kn1 = p.sk[(ch0 + 1) * S + 8 * tid + (tid < 255 ? 8 : 0)];
  int pch = 64 * g + 16 * w + n;
  u32 lre_h = p.lre[pch], lim_h = p.lim[pch], ldt_h = p.ldt[pch];
  __builtin_amdgcn_sched_barrier(0);
  u32x4 Bw[18][2];
  const int bo = (64 * g + 16 * w + (n & 12)) * D + 8 * (lane & 3);
  const u16 *br = p.bre + bo + 32 * kg, *bi = p.bim + bo + 32 * kg;
  const u16* bt = (kg < 2 ? p.bre : p.bim) + bo + 512 + 32 * (kg & 1);
  auto bld = [&](int G) {
    SB();
#pragma unroll
    for (int j = 0; j < 4; ++j) {
      if (G < 8) Bw[4 * (G >> 1) + j][G & 1] = ldg16(((G & 1) ? bi : br) + j * D + 128 * (G >> 1));
      else Bw[16 + (j >> 1)][j & 1] = ldg16(bt + j * D);
    }
    SB();
  };
#pragma unroll
  for (int G = 0; G < 5; ++G) bld(G);
  STAMP(20);
  {
    const int r0 = 5 * (tid >> 4);
    const bool first = (t0 + r0 == 0);
    asm volatile("" : "+v"(kk[0]), "+v"(kk[1]), "+v"(kk[2]), "+v"(kk[3]), "+v"(kk[4]), "+v"(kk[5]), "+v"(kk[6]), "+v"(kk[7]),
                 "+v"(vv[0]), "+v"(vv[1]), "+v"(vv[2]), "+v"(vv[3]), "+v"(vv[4]), "+v"(shw[0]), "+v"(shw[1]));
    asm volatile("" : "+v"(kx0), "+v"(kx1), "+v"(kn0), "+v"(kn1), "+v"(lre_h), "+v"(lim_h), "+v"(ldt_h));
    u16* img = (u16*)(lds + L_GI) + 4 * (tid & 15) * 84 + r0;
#pragma unroll
    for (int h = 0; h < 2; ++h) {
      f32x2 sw[4], kf[8];
      sw[0] = f32x2{lo16(shw[h].x), lo16(shw[h].z)}; sw[1] = f32x2{hi16(shw[h].x), hi16(shw[h].z)};
      sw[2] = f32x2{lo16(shw[h].y), lo16(shw[h].w)}; sw[3] = f32x2{hi16(shw[h].y), hi16(shw[h].w)};
#pragma unroll
      for (int j = 0; j < 8; ++j) {
        u32 q = kk[j][h];
        if (j < 3) q = first ? 0u : q;
        kf[j] = f32x2{lo16(q), hi16(q)};
      }
#pragma unroll
      for (int r = 0; r < 5; ++r) {
        SB();
        xdma(5 * h + r);
        SB();
        f32x2 a = kf[r] * sw[3];
#pragma unroll
        for (int j = 1; j < 4; ++j) a = __builtin_elementwise_fma(kf[r + j], sw[3 - j], a);
        a *= f32x2{lo16(vv[r][h]), hi16(vv[r][h])};
        const u32 o = pk(a.x, a.y);
        img[(2 * h) * 84 + r] = (u16)o;
        img[(2 * h + 1) * 84 + r] = (u16)(o >> 16);
      }
    }
  }
  STAMP(21);
  bld(5);
  conv_stage_k(lds + L_CV, kx0, tid < 255 ? kn0 : 0, tid);
  conv_stage_k(lds + L_CV + CVSZ, kx1, tid < 255 ? kn1 : 0, tid);
  STAMP(30);
  bld(6);
  Scan sc;
  {
    float lr = lo16(lre_h), li = lo16(lim_h), dt = rb(expf(lo16(ldt_h)));
    float e = expf(dt * lr), sn, cs;
    sincosf(dt * li, &sn, &cs);
    bld(7);
    sc.ar = e * cs; sc.ai = e * sn;
    float nr = sc.ar - 1.f, ni = sc.ai, dn = lr * lr + li * li;
    sc.cr = 2.f * ((nr * lr + ni * li) / dn); sc.ci = 2.f * ((ni * lr - nr * li) / dn);
    bld(8);
    float r2, i2, r4, i4, r5, i5, r80, i80, r160, i160, r60, i60, r240, i240;
    cmul(sc.ar, sc.ai, sc.ar, sc.ai, r2, i2); cmul(r2, i2, r2, i2, r4, i4); cmul(r4, i4, sc.ar, sc.ai, r5, i5);
    cmul(r5, i5, r5, i5, r2, i2); cmul(r2, i2, r2, i2, sc.a20r, sc.a20i);
    cmul(sc.a20r, sc.a20i, sc.a20r, sc.a20i, sc.a40r, sc.a40i);
    cmul(sc.a40r, sc.a40i, sc.a20r, sc.a20i, r60, i60);
    cmul(sc.a40r, sc.a40i, sc.a40r, sc.a40i, r80, i80);
    cmul(r80, i80, r80, i80, r160, i160); cmul(r160, i160, r80, i80, r240, i240);
    cmul(r160, i160, r160, i160, sc.a320r, sc.a320i);
    sc.aqr = kg == 0 ? 1.f : kg == 1 ? sc.a20r : kg == 2 ? sc.a40r : r60;
    sc.aqi = kg == 0 ? 0.f : kg == 1 ? sc.a20i : kg == 2 ? sc.a40i : i60;
    sc.bqr = kg == 0 ? 1.f : kg == 1 ? r80 : kg == 2 ? r160 : r240;
    sc.bqi = kg == 0 ? 0.f : kg == 1 ? i80 : kg == 2 ? i160 : i240;
  }
  STAMP(28);
  int twa = L_HI + w * 5376 + lane * 16, tra = L_HI + w * 5376 + (n & 3) * 1040 + (n >> 2) * 64;
  asm volatile("s_waitcnt vmcnt(16)" ::: "memory");
  {
    const u32 ones = 0x3f803f80u;
#pragma unroll
    for (int i = 0; i < 5; ++i) {
      u32x4 xv[5];
#pragma unroll
      for (int q = 0; q < 4; ++q) xv[q] = *(const u32x4*)(lds + L_A + w * RB + (i * 4 + q) * 1024 + lane * 16);
      xv[4] = *(const u32x4*)(lds + L_A + w * RB + 20480 + i * 512 + (lane & 31) * 16);
      SB();
      if (i < 4) {
#pragma unroll
        for (int m = 0; m < 2; ++m) ltr<16, 32, 48>(lds, twa, tra + kg * 256, Bw[4 * i][m], Bw[4 * i + 1][m], Bw[4 * i + 2][m], Bw[4 * i + 3][m]);
      } else ltr<512, 32, 544>(lds, twa, tra + (kg & 1) * 256 + (kg >> 1) * 16, Bw[16][0], Bw[16][1], Bw[17][0], Bw[17][1]);
      SB();
      float s1 = 0.f, s2 = 0.f;
#pragma unroll
      for (int e = 0; e < 4; ++e) { s1 = dot2(xv[4][e], ones, s1); s2 = dot2(xv[4][e], xv[4][e], s2); }
      s1 *= 0.5f; s2 *= 0.5f;
#pragma unroll
      for (int q = 0; q < 4; ++q)
#pragma unroll
        for (int e = 0; e < 4; ++e) { s1 = dot2(xv[q][e], ones, s1); s2 = dot2(xv[q][e], xv[q][e], s2); }
      float sm = red32(s1, s2);
      sm = quad_sum(red16(sm, sm));
      *(float*)(lds + L_SP + (20 * w + 4 * i + (n >> 2)) * 8 + (kg >> 1) * 4) = sm;
      SB();
    }
    asm volatile("s_waitcnt lgkmcnt(0)" ::: "memory");
    if (lane < 20) {
      f32x2 v = *(const f32x2*)(lds + L_SP + (20 * w + lane) * 8);
      float m = v.x * (1.f / D), var = fmaxf(v.y * (1.f / D) - m * m, 0.f);
      float den = rb(sqrtf(rb(rb(var) + 1e-5f)));
      *(f32x2*)(lds + L_P1 + (20 * w + lane) * 8) = f32x2{1.f / den, rb(m)};
    }
  }
#pragma unroll
  for (int ks = 0; ks < 18; ++ks) asm volatile("" : "+a"(Bw[ks][0]), "+a"(Bw[ks][1]));
  bar();
  STAMP(1);
  TAGS();
  const u64 tF = ((u64)tB << 32) | tA;
  f32x4 acc[5][2];
  float rsr, rsi;
  u32x4 kq[4];
  u32x4 Cw[9][4];
  const rsrc_t crs = __builtin_amdgcn_make_buffer_rsrc((void*)((w & 1) ? p.cim : p.cre), 0, D * D * 2, 0x00020000);
  auto cld = [&](int L) {
    const int d = L >> 3, j = L & 3, cb = ((64 * g + (n & 12)) * D + 288 * (w >> 1) + 8 * (lane & 3)) * 2 + j * D * 2;
    if (L >= 32) Cw[8][j] = __builtin_amdgcn_raw_buffer_load_b128(crs, cb + 64 * ((8 + g) % 9) + ((w + kg) & 3) * 32 * D, 0, 0);
    else Cw[L & 7][d] = __builtin_amdgcn_raw_buffer_load_b128(crs, cb + 64 * (((L & 4) + kg + g) % 9), ((w + d) & 3) * 32 * D, 0);
  };
  {
    u32x2 kvq;
    u32 kvo;
    SB();
    STAMP(23);
    gemm_bu<true>(lds, Bw, lane, acc, rsr, rsi, [&](int ks, int i) {
      if (ks < 5 && i == 0) {
        const int it = tid + 256 * ks, ch = it / 20, tq = it - 20 * ch, gq = 20 * c + tq;
        kvq = *(const u32x2*)(lds + L_GI + ch * 168 + tq * 8);
        kvo = gq < 512 ? W_KVT + (64 * g + ch) * 8192 + (511 - gq) * 16 : OOB;
      }
      if (ks < 5 && i == 4)
        stc(ws, kvo, u32x4{__builtin_amdgcn_alignbit(kvq.y, kvq.y, 16), tA, __builtin_amdgcn_alignbit(kvq.x, kvq.x, 16), tB});
      if (ks >= 6 && i == 2) cld(ks - 6);
      if (ks == 16 && i < 4) kq[i] = ldc(ws, W_KVT + ch0 * 8192 + (tid + 256 * i) * 16);
    });
  }
  STAMP(2);
  norm_acc(lds, L_P1, kg, acc, rsr, rsi);
  *(f32x2*)(lds + L_RS + tid * 8) = f32x2{rsr, rsi};
  float inr, ini;
  scan_a(sc, acc, ws, W_E + (c * D + pch) * 16, lane, tA, tB, inr, ini);
  STAMP(3);
  u32x2 dsk_h = ldg8(p.dsk + 64 * g + 16 * w + 4 * kg);
  const u16 sd_h = p.sd[ch0 + (w >> 1)];
  conv_gather<true>(lds + L_CV, ws, ch0, tid, kq, tA, tB, budget);
  bar();
  STAMP(4);
  f32x4 cacc[8];
  u32x4 ev[7];
#pragma unroll
  for (int j = 0; j < 8; ++j) cacc[j] = f32x4{0, 0, 0, 0};
  conv_mma<0, 2>(lds, L_CV, w, lane, cacc, [&](int r, int b) {
    if (b < 6) cld(12 + 6 * r + b);
    if (r == 1 && b == 2) {
#pragma unroll
      for (int j = 0; j < 7; ++j) { const int cp = c - 1 - kg - 4 * j; ev[j] = ldc(ws, W_E + pch * 16 + (cp >= 0 ? cp : 0) * (D * 16)); }
    }
  });
  STAMP(5);
  scan_b<true>(sc, acc, lds, ws, W_E + pch * 16, W_H + (c * 36 + 4 * g + w) * 5120, c, w, lane, tA, tB, inr, ini, ev, budget);
  STAMP(6);
  {
    conv_mma<2, 4>(lds, L_CV, w, lane, cacc, [&](int r, int b) {
      if (b < 6) cld(12 + 6 * r + b);
      if (r == 2 && b == 7) {
        asm volatile("s_waitcnt vmcnt(6)\n\ts_barrier" ::: "memory");
        stc2(ws, tid == 0 ? W_FLG + c * 128 + g * 8 : OOB, u32x2{tA, tB});
      }
    });
  }
  float dk0 = lo16(dsk_h.x), dk1 = hi16(dsk_h.x), dk2 = lo16(dsk_h.y), dk3 = hi16(dsk_h.y);
  f32x4 z[5];
  auto frag_off = [&](int i, int rb) {
    const int tl = 16 * i + n, q = tl / 20, rem = tl - 20 * q;
    return q * rb + (rem >> 2) * (g < 8 ? 4096 : 512) + (g < 8 ? g * 512 : 20480) + (w >> 1) * 256 + (rem & 3) * 64 + (w & 1) * 32 + kg * 8;
  };
#pragma unroll
  for (int i = 0; i < 5; ++i) {
    u32x2 xq = *(const u32x2*)(lds + L_A + frag_off(i, RB));
    f32x2 pr = *(const f32x2*)(lds + L_P1 + (16 * i + n) * 8);
    float d0 = lo16(xq.x) - pr.y, d1 = hi16(xq.x) - pr.y, d2 = lo16(xq.y) - pr.y, d3 = hi16(xq.y) - pr.y;
    u32 a = pk(d0, d1), b = pk(d2, d3);
    a = pk(lo16(a) * pr.x, hi16(a) * pr.x); b = pk(lo16(b) * pr.x, hi16(b) * pr.x);
    a = pk(lo16(a) * dk0, hi16(a) * dk1); b = pk(lo16(b) * dk2, hi16(b) * dk3);
    z[i] = f32x4{d0 + lo16(a), d1 + hi16(a), d2 + lo16(b), d3 + hi16(b)};
  }
  conv_finish(lds, lds + L_CV, ws, ch0, b2f(sd_h), w, lane, cacc, tA, tB);
  *(f32x4*)(lds + L_CV + tid * 16) = f32x4{sc.ar, sc.ai, sc.cr, sc.ci};
  *(f32x4*)(lds + L_CV + 4096 + tid * 16) = f32x4{sc.a20r, sc.a20i, sc.a40r, sc.a40i};
  *(f32x4*)(lds + L_CV + CVSZ + tid * 16) = f32x4{sc.aqr, sc.aqi, sc.bqr, sc.bqi};
  *(f32x2*)(lds + L_CV + CVSZ + 4096 + tid * 8) = f32x2{sc.a320r, sc.a320i};
  *(u32x2*)(lds + L_CV + CVSZ + 6144 + tid * 8) = dsk_h;
  STAMP(7);
  PHASE();
  twa = L_HI + w * 5376 + lane * 16; tra = L_HI + w * 5376 + (n & 3) * 1040 + (n >> 2) * 64;
  auto cltr = [&](int G) {
    if (G < 8) ltr<16, 32, 48>(lds, twa, tra + kg * 256, Cw[4 * (G & 1)][G >> 1], Cw[4 * (G & 1) + 1][G >> 1], Cw[4 * (G & 1) + 2][G >> 1], Cw[4 * (G & 1) + 3][G >> 1]);
    else ltr<256, 512, 768>(lds, twa, tra + kg * 16, Cw[8][0], Cw[8][1], Cw[8][2], Cw[8][3]);
  };
  {
    const u64 pre = poll_flags(p.ws, W_FLG + c * 128, lane);
    SB();
#pragma unroll
    for (int G = 0; G < 4; ++G) cltr(G);
    SB();
    STAMP(24);
    wait_flags(p.ws, W_FLG + c * 128, tF, lane, budget, pre);
  }
  STAMP(8);
  u32 gq[5][2];
  {
    u32 off[5]; bool need[5]; u32x4 v[5];
    auto goff = [&]() {
#pragma unroll
      for (int r = 0; r < 5; ++r) {
        const int it = lane + 64 * r, ch = it / 20, tq = it - 20 * ch, q = 20 * c + tq;
        need[r] = true;
        off[r] = W_GT + (64 * g + 16 * w + ch) * 8192 + (q < 512 ? q : 511) * 16;
      }
    };
    gemm_y<5>(lds, lb, ws, W_H + c * 36 * 5120, Cw, w, g, lane, z, [&](int j) {
      if (j >= 10) cltr(j - 6);
      if (j == -2) cltr(8);
      if (j == -2) STAMP(22);
      if (j == -1) STAMP(29);
      if (j == 4) STAMP(27);
      if (j == 8) STAMP(31);
      if (j != 4) return;
      goff();
#pragma unroll
      for (int r = 0; r < 5; ++r) v[r] = ldc(ws, off[r]);
    });
    STAMP(9);
    PHASE();
    rs_part(lds, z, w, lane);
    bar();
    rs_pub(lds, ws, W_ST + (c * 9 + g) * 1280, tid, tA, tB);
    goff();
    gather<5, true>(ws, off, need, v, tA, tB, budget);
#pragma unroll
    for (int r = 0; r < 5; ++r) {
      const int it = lane + 64 * r, ch = it / 20, tq = it - 20 * ch;
      *(u32x2*)(lds + L_GI + (16 * w + ch) * 168 + tq * 8) = u32x2{v[r].x, v[r].z};
    }
    asm volatile("s_waitcnt lgkmcnt(0)" ::: "memory");
#pragma unroll
    for (int i = 0; i < 5; ++i) {
      u32x2 t = trread(lds, L_GI + (16 * w + 4 * kg + (n >> 2)) * 168 + 8 * (n & 3) + 32 * i);
      gq[i][0] = t.x; gq[i][1] = t.y;
    }
  }
  STAMP(10);
  u32x4 sv[3];
  rs_get<false>(lds, L_PAR, ws, W_ST + c * 9 * 1280, w, lane, sv, tA, tB, budget);
  bar();
  STAMP(11);
#pragma unroll
  for (int i = 0; i < 5; ++i) {
    f32x2 pr = *(const f32x2*)(lds + L_PAR + (16 * i + n) * 8);
    float gv[4] = {lo16(gq[i][0]), hi16(gq[i][0]), lo16(gq[i][1]), hi16(gq[i][1])};
#pragma unroll
    for (int e = 0; e < 4; ++e) { float t = z[i][e] - pr.y; z[i][e] = t + t * pr.x * gv[e]; }
    stc2(ws, W_U3 + c * 94208 + frag_off(i, 23552), u32x2{pk(z[i].x, z[i].y), pk(z[i].z, z[i].w)});
  }
  SB();
  rs_part(lds, z, w, lane);
  drain();
  bar();
  stc2(ws, tid == 0 ? W_FLG + (NC + c) * 128 + g * 8 : OOB, u32x2{tA, tB});
  rs_pub(lds, ws, W_ST + ((NC + c) * 9 + g) * 1280, tid, tA, tB);
  wait_flags(p.ws, W_FLG + (NC + c) * 128, tF, lane, budget, tF + 1);
  STAMP(12);
  PHASE();
  {
    const u32 ub = W_U3 + c * 94208 + w * 23552 + lane * 16;
#pragma unroll
    for (int j = 0; j < 5; ++j) dma_c4(ws, ub, j * 4096, lb + L_A + w * RB + j * 4096);
#pragma unroll
    for (int j = 20; j < 23; ++j) dma_c(ws, ub, j * 1024, lb + L_A + w * RB + j * 1024);
  }
  drain();
  bar();
  STAMP(13);
#pragma unroll
  for (int i = 0; i < 5; ++i) asm volatile("" : "+v"(gq[i][0]), "+v"(gq[i][1]));
  gemm_bu<false>(lds, Bw, lane, acc, rsr, rsi, [&](int ks, int i) {
    if (ks == 12 && i < 3) sv[i] = ldc(ws, W_ST + (NC + c) * 9 * 1280 + rs_off(w, lane) + i * 1280);
  });
  STAMP(25);
  PHASE();
  rs_get<true>(lds, L_P3, ws, W_ST + (NC + c) * 9 * 1280, w, lane, sv, tA, tB, budget);
  bar();
  {
    const f32x2 q = *(const f32x2*)(lds + L_RS + tid * 8);
    norm_acc(lds, L_P3, kg, acc, q.x, q.y);
  }
  {
    f32x4 a = *(const f32x4*)(lds + L_CV + tid * 16), b = *(const f32x4*)(lds + L_CV + 4096 + tid * 16);
    f32x4 e = *(const f32x4*)(lds + L_CV + CVSZ + tid * 16);
    f32x2 f = *(const f32x2*)(lds + L_CV + CVSZ + 4096 + tid * 8);
    sc.ar = a.x; sc.ai = a.y; sc.cr = a.z; sc.ci = a.w; sc.a20r = b.x; sc.a20i = b.y; sc.a40r = b.z; sc.a40i = b.w;
    sc.aqr = e.x; sc.aqi = e.y; sc.bqr = e.z; sc.bqi = e.w; sc.a320r = f.x; sc.a320i = f.y;
  }
  scan_a(sc, acc, ws, W_E + ((NC + c) * D + pch) * 16, lane, tA, tB, inr, ini);
  STAMP(14);
  dsk_h = *(const u32x2*)(lds + L_CV + CVSZ + 6144 + tid * 8);
#pragma unroll
  for (int i = 0; i < 5; ++i) {
    f32x2 pr = *(const f32x2*)(lds + L_P3 + (16 * i + n) * 8);
    const f32x4 t = z[i] - pr.y;
    z[i] = t + t * pr.x * f32x4{lo16(dsk_h.x), hi16(dsk_h.x), lo16(dsk_h.y), hi16(dsk_h.y)};
  }
  SB();
  scan_b<false>(sc, acc, lds, ws, W_E + (NC * D + pch) * 16, W_H + ((NC + c) * 36 + 4 * g + w) * 5120, c, w, lane, tA, tB, inr, ini, ev, budget);
  drain();
  bar();
  stc2(ws, tid == 0 ? W_FLG + (2 * NC + c) * 128 + g * 8 : OOB, u32x2{tA, tB});
  STAMP(15);
  wait_flags(p.ws, W_FLG + (2 * NC + c) * 128, tF, lane, budget, tF + 1);
  STAMP(16);
  {
    gemm_y<0>(lds, lb, ws, W_H + (NC + c) * 36 * 5120, Cw, w, g, lane, z, [](int) {});
    rs_part(lds, z, w, lane);
  }
  bar();
  STAMP(17);
  rs_pub(lds, ws, W_ST + ((2 * NC + c) * 9 + g) * 1280, tid, tA, tB);
  rs_get<false>(lds, L_P2, ws, W_ST + (2 * NC + c) * 9 * 1280, w, lane, sv, tA, tB, budget);
  bar();
  STAMP(18);
  const rsrc_t ors = __builtin_amdgcn_make_buffer_rsrc(p.out, 0, S * D * 4, 0x00020000);
#pragma unroll
  for (int i = 0; i < 5; ++i) {
    f32x2 pr = *(const f32x2*)(lds + L_P2 + (16 * i + n) * 8);
    const float cen = *(const float*)(lds + L_P1 + (16 * i + n) * 8 + 4) + *(const float*)(lds + L_PAR + (16 * i + n) * 8 + 4) +
                      *(const float*)(lds + L_P3 + (16 * i + n) * 8 + 4);
    float gv[4] = {lo16(gq[i][0]), hi16(gq[i][0]), lo16(gq[i][1]), hi16(gq[i][1])};
    f32x4 o;
#pragma unroll
    for (int e = 0; e < 4; ++e) o[e] = (z[i][e] + cen) + (z[i][e] - pr.y) * pr.x * gv[e];
    const int t = t0 + 16 * i + n;
    __builtin_amdgcn_raw_buffer_store_b128(__builtin_bit_cast(u32x4, o), ors, (t * D + 64 * g + 16 * w + 4 * kg) * 4, 0, 19);
  }
  if (c == NC - 1 && g == 0 && tid == 0) __hip_atomic_store((gu32*)p.ws, tA, __ATOMIC_RELAXED, __HIP_MEMORY_SCOPE_AGENT);
  STAMP(19);
  DUMP();
}

extern "C" void kernel_launch(void* const* d_in, const int* in_sizes, int n_in, void* d_out, int out_size, void* d_ws,
                              size_t ws_size, hipStream_t stream) {
  Params p;
  p.x = (const u16*)d_in[0]; p.lre = (const u16*)d_in[1]; p.lim = (const u16*)d_in[2];
  p.bre = (const u16*)d_in[3]; p.bim = (const u16*)d_in[4]; p.cre = (const u16*)d_in[5]; p.cim = (const u16*)d_in[6];
  p.ldt = (const u16*)d_in[7]; p.dsk = (const u16*)d_in[8]; p.k = (const u16*)d_in[10]; p.v = (const u16*)d_in[11];
  p.shk = (const u16*)d_in[12]; p.sk = (const u16*)d_in[13]; p.sd = (const u16*)d_in[14];
  p.out = (float*)d_out; p.ws = (unsigned char*)d_ws;
  (void)hipFuncSetAttribute((const void*)fused_kernel, hipFuncAttributeMaxDynamicSharedMemorySize, L_TOT);
  hipLaunchKernelGGL(fused_kernel, dim3(256), dim3(256), L_TOT, stream, p);
}
```

```cpp
#include <hip/hip_runtime.h>
#include <stdint.h>

typedef unsigned short u16;
typedef unsigned int u32;
typedef unsigned long long u64;
typedef __attribute__((ext_vector_type(4))) unsigned int u32x4;
typedef __attribute__((ext_vector_type(2))) unsigned int u32x2;
typedef __attribute__((ext_vector_type(4))) float f32x4;
typedef __attribute__((ext_vector_type(2))) float f32x2;
typedef __attribute__((ext_vector_type(8))) __bf16 bf16x8;
typedef __attribute__((ext_vector_type(2))) __bf16 bf16x2;
typedef __attribute__((ext_vector_type(4))) short s16x4;
typedef __attribute__((address_space(1))) u64 gu64;
typedef __attribute__((address_space(1))) u32 gu32;
typedef __attribute__((address_space(3))) s16x4 lds_s16x4;
typedef __amdgpu_buffer_rsrc_t rsrc_t;

#define DI __device__ __forceinline__
#define SB() __builtin_amdgcn_sched_barrier(0)

constexpr int S = 2048, D = 576, NC = 26;
constexpr u32 W_FLG = 4096;
constexpr u32 W_E = 32768;
constexpr u32 W_ST = W_E + 2 * NC * D * 16;
constexpr u32 W_KVT = 4u << 20;
constexpr u32 W_GT = W_KVT + D * 8192;
constexpr u32 W_H = W_GT + D * 8192;
constexpr u32 W_U3 = W_H + 2 * NC * 36 * 5120;
constexpr u32 W_END = W_U3 + NC * 94208;
constexpr u32 W_DBG = 64u << 20;
constexpr int L_A = 0;
constexpr int CVSZ = 13056, CV_K1 = 4224, CV_R = 8448;
constexpr int RB = 23568;
constexpr u32 OOB = 0x7ffffff0u;
constexpr int L_CV = 4 * RB;
constexpr int L_HI = L_CV + 2 * CVSZ;
constexpr int L_GI = L_HI + 4 * 5376;
constexpr int L_SP = L_GI + 64 * 168;
constexpr int L_PAR = L_SP + 2560;
constexpr int L_P3 = L_PAR + 640;
constexpr int L_P1 = L_PAR + 1280;
constexpr int L_P2 = L_P1 + 640;
constexpr int L_RS = L_P2 + 640;
constexpr int L_TOT = L_RS + 2048 + 1024;

struct Params {
  const u16 *x, *lre, *lim, *bre, *bim, *cre, *cim, *ldt, *dsk, *k, *v, *shk, *sk, *sd;
  float* out;
  unsigned char* ws;
};

DI float lo16(u32 w) { return __uint_as_float(w << 16); }
DI float hi16(u32 w) { return __uint_as_float(w & 0xffff0000u); }
DI float b2f(u16 h) { return __uint_as_float(((u32)h) << 16); }
DI u32 pk(float a, float b) {
  f32x2 v = {a, b};
  return __builtin_bit_cast(u32, __builtin_convertvector(v, bf16x2));
}
DI float rb(float a) { return lo16(pk(a, a)); }
DI f32x4 mma(u32x4 a, u32x4 b, f32x4 c) {
  return __builtin_amdgcn_mfma_f32_16x16x32_bf16(__builtin_bit_cast(bf16x8, a), __builtin_bit_cast(bf16x8, b), c, 0, 0, 0);
}
DI float dot2(u32 a, u32 b, float c) {
  return __builtin_amdgcn_fdot2_f32_bf16(__builtin_bit_cast(bf16x2, a), __builtin_bit_cast(bf16x2, b), c, false);
}
DI void bar() { asm volatile("s_waitcnt lgkmcnt(0)\n\ts_barrier" ::: "memory"); }
DI void drain() { asm volatile("s_waitcnt vmcnt(0)" ::: "memory"); }
DI void cfence() { asm volatile("" ::: "memory"); }
DI u32x4 ldc(rsrc_t r, u32 off) { return __builtin_amdgcn_raw_buffer_load_b128(r, off, 0, 16); }
DI u32x4 ldcs(rsrc_t r, u32 off, u32 soff) { return __builtin_amdgcn_raw_buffer_load_b128(r, off, soff, 16); }
DI void stc(rsrc_t r, u32 off, u32x4 v) { __builtin_amdgcn_raw_buffer_store_b128(v, r, off, 0, 16); }
DI void stc2(rsrc_t r, u32 off, u32x2 v) { __builtin_amdgcn_raw_buffer_store_b64(v, r, off, 0, 16); }
DI u32x4 ldg16(const u16* p) { return *(const u32x4*)p; }
DI u32x2 ldg8(const u16* p) { return *(const u32x2*)p; }
DI u32x2 trread(unsigned char* lds, int off) {
  s16x4 r = __builtin_amdgcn_ds_read_tr16_b64_v4i16((lds_s16x4*)(lds + off));
  return __builtin_bit_cast(u32x2, r);
}
DI void cmul(float ar, float ai, float br, float bi, float& cr, float& ci) {
  float r = ar * br - ai * bi, i = ar * bi + ai * br;
  cr = r; ci = i;
}
DI float red32(float a, float b) {
  auto r = __builtin_amdgcn_permlane32_swap(__float_as_uint(a), __float_as_uint(b), false, false);
  return __uint_as_float(r[0]) + __uint_as_float(r[1]);
}
DI float red16(float a, float b) {
  auto r = __builtin_amdgcn_permlane16_swap(__float_as_uint(a), __float_as_uint(b), false, false);
  return __uint_as_float(r[0]) + __uint_as_float(r[1]);
}

DI void pair16(float a, float& e, float& o) {
  auto r = __builtin_amdgcn_permlane16_swap(__float_as_uint(a), __float_as_uint(a), false, false);
  e = __uint_as_float(r[0]); o = __uint_as_float(r[1]);
}
DI void pair32(float a, float& e, float& o) {
  auto r = __builtin_amdgcn_permlane32_swap(__float_as_uint(a), __float_as_uint(a), false, false);
  e = __uint_as_float(r[0]); o = __uint_as_float(r[1]);
}
DI float sum3(float a, int lane) {
  return a + __int_as_float(__builtin_amdgcn_ds_bpermute(lane * 4 + 80, __float_as_int(a))) +
         __int_as_float(__builtin_amdgcn_ds_bpermute(lane * 4 + 160, __float_as_int(a)));
}
typedef __attribute__((address_space(3))) void lds_void;
typedef __attribute__((address_space(1))) const void gvoid;
template <int O1, int O2, int O3>
DI void ltr(unsigned char* lds, int wa, int ra, u32x4& r0, u32x4& r1, u32x4& r2, u32x4& r3) {
  *(u32x4*)(lds + wa) = r0; *(u32x4*)(lds + wa + 1040) = r1; *(u32x4*)(lds + wa + 2080) = r2; *(u32x4*)(lds + wa + 3120) = r3;
  cfence();
  r0 = *(const u32x4*)(lds + ra); r1 = *(const u32x4*)(lds + ra + O1);
  r2 = *(const u32x4*)(lds + ra + O2); r3 = *(const u32x4*)(lds + ra + O3);
  cfence();
}
template <int OFF>
DI void dma_g(const u16* base, u32 voff, u32 ldsa) {
  asm volatile("s_mov_b32 m0, %2\n\ts_nop 0\n\tglobal_load_lds_dwordx4 %0, %1 offset:%3" :: "v"(voff), "s"(base), "s"(ldsa - OFF), "n"(OFF) : "memory");
}
DI void dma_c(rsrc_t r, u32 voff, u32 soff, u32 ldsa) {
  asm volatile("s_mov_b32 m0, %3\n\ts_nop 0\n\tbuffer_load_dwordx4 %0, %1, %2 offen sc1 lds" :: "v"(voff), "s"(r), "s"(soff), "s"(ldsa) : "memory");
}
DI float quad_sum(float a) {
  a += __int_as_float(__builtin_amdgcn_update_dpp(0, __float_as_int(a), 0xb1, 0xf, 0xf, true));
  a += __int_as_float(__builtin_amdgcn_update_dpp(0, __float_as_int(a), 0x4e, 0xf, 0xf, true));
  return a;
}
constexpr unsigned SPIN_MAX = 1u << 20;
DI bool spin_next(unsigned& budget) {
  if (budget == 0) return false;
  --budget;
  __builtin_amdgcn_s_sleep(1);
  return true;
}
template <int N, bool PRE = false>
DI void gather(rsrc_t ws, const u32 (&off)[N], const bool (&need)[N], u32x4 (&v)[N], u32 tA, u32 tB, unsigned& budget) {
  if (PRE) {
    bool ok = true;
#pragma unroll
    for (int i = 0; i < N; ++i) ok &= (!need[i]) | ((v[i].y == tA) & (v[i].w == tB));
    if (__all(ok)) return;
  }
  for (;;) {
    cfence();
    bool ok = true;
#pragma unroll
    for (int i = 0; i < N; ++i) v[i] = ldc(ws, off[i]);
#pragma unroll
    for (int i = 0; i < N; ++i) ok &= (!need[i]) | ((v[i].y == tA) & (v[i].w == tB));
    if (__all(ok)) break;
    if (!spin_next(budget)) break;
  }
  cfence();
}
DI u64 poll_flags(unsigned char* wsp, u32 off, int lane) {
  return __hip_atomic_load((gu64*)(wsp + off) + (lane < 9 ? lane : 0), __ATOMIC_RELAXED, __HIP_MEMORY_SCOPE_AGENT);
}
DI void wait_flags(unsigned char* wsp, u32 off, u64 tag, int lane, unsigned& budget, u64 pre) {
  gu64* f = (gu64*)(wsp + off) + (lane < 9 ? lane : 0);
  if (!__all(pre == tag)) for (;;) {
    u64 x = __hip_atomic_load(f, __ATOMIC_RELAXED, __HIP_MEMORY_SCOPE_AGENT);
    if (__all(x == tag)) break;
    if (!spin_next(budget)) break;
  }
  __builtin_amdgcn_fence(__ATOMIC_ACQUIRE, "wavefront");
  cfence();
}

#define STAMP(i) do {} while (0)

template <int V> struct IC { static constexpr int v = V; constexpr operator int() const { return V; } };
template <int N, int K = 0, class F>
DI void static_for(F f) { if constexpr (K < N) { f(IC<K>{}); static_for<N, K + 1>(f); } }
constexpr int NPIN = 7;
template <int N>
DI void vmw() { asm volatile("s_waitcnt vmcnt(%0)" :: "n"(N) : "memory"); }
DI void conv_stage_k(unsigned char* cv, u32x4 kx, u32 knext, int tid) {
  *(u32x4*)(cv + 32 + 16 * tid) = kx;
  u32x4 s;
  s.x = __builtin_amdgcn_alignbit(kx.y, kx.x, 16);
  s.y = __builtin_amdgcn_alignbit(kx.z, kx.y, 16);
  s.z = __builtin_amdgcn_alignbit(kx.w, kx.z, 16);
  s.w = __builtin_amdgcn_alignbit(knext, kx.w, 16);
  *(u32x4*)(cv + CV_K1 + 32 + 16 * tid) = s;
  u32x4 z = {0, 0, 0, 0};
  if (tid == 0) {
    *(u32x4*)(cv) = z; *(u32x4*)(cv + 16) = z; *(u32x4*)(cv + CV_K1) = z;
    u32x4 f = {0, 0, 0, kx.x << 16};
    *(u32x4*)(cv + CV_K1 + 16) = f;
  }
  if (tid >= 64 && tid < 96) *(u32x4*)(cv + CV_R + 4096 + 16 * (tid - 64)) = z;
}
template <bool PRE>
DI void conv_gather(unsigned char* cv, rsrc_t ws, int ch0, int tid, u32x4 (&v)[4], u32 tA, u32 tB, unsigned& budget) {
  u32 off[4]; bool need[4];
#pragma unroll
  for (int r = 0; r < 4; ++r) { off[r] = W_KVT + ch0 * 8192 + (tid + 256 * r) * 16; need[r] = true; }
  gather<4, PRE>(ws, off, need, v, tA, tB, budget);
#pragma unroll
  for (int r = 0; r < 4; ++r) {
    u32x2 w = {v[r].x, v[r].z};
    *(u32x2*)(cv + (r >> 1) * CVSZ + CV_R + ((r & 1) * 256 + tid) * 8) = w;
  }
}
template <int R0, int R1, class F>
DI void conv_mma(unsigned char* lds, int cvo, int w, int lane, f32x4 (&acc)[8], F hook) {
  const int m = lane & 15, kg = lane >> 4;
  const int cb = cvo + (w >> 1) * CVSZ + (w & 1) * 256;
  int ka[4];
  ka[0] = cb + ((m & 1) ? 0 : CV_K1) + 16 * kg + 4 * ((m + 1) >> 1);
  ka[1] = ka[0] + 1024; ka[2] = ka[0] + 2048; ka[3] = ka[0] + 3072;
  int rbp = cb + CV_R + 480 - 32 * m + 16 * kg;
  asm volatile("" : "+v"(ka[0]), "+v"(ka[1]), "+v"(ka[2]), "+v"(ka[3]), "+v"(rbp));
  u32x4 A[8], B[8];
  auto ldA = [&](int r, int a) {
    const u32* q = (const u32*)(lds + ka[a >> 1] + 64 * r + 512 * (a & 1));
    A[a].x = q[0]; A[a].y = q[1]; A[a].z = q[2]; A[a].w = q[3];
  };
  auto ldB = [&](int r, int b) { B[b] = *(const u32x4*)(lds + rbp + 64 * r + 512 * (7 - b)); };
  ldB(R0, 0);
#pragma unroll
  for (int a = 7; a >= 0; --a) ldA(R0, a);
#pragma unroll
  for (int b = 1; b < 8; ++b) ldB(R0, b);
  SB();
#pragma unroll
  for (int r = R0; r < R1; ++r) {
#pragma unroll
    for (int b = 0; b < 8; ++b) {
#pragma unroll
      for (int a = 7 - b; a >= 0; --a) acc[a + b] = mma(A[a], B[b], acc[a + b]);
      SB();
      if (r + 1 < R1) { ldB(r + 1, b); ldA(r + 1, 7 - b); }
      hook(r, b);
      SB();
    }
  }
}
DI void conv_finish(unsigned char* lds, unsigned char* cv, rsrc_t ws, int ch0, float sdv, int w, int lane,
                    f32x4 (&acc)[8], u32 tA, u32 tB) {
  const int n = lane & 15, kg = lane >> 4, odd = w & 1;
  unsigned char* box = lds + L_HI + (w ^ 1) * 5376;
#pragma unroll
  for (int j = 0; j < 4; ++j) {
    f32x4 lo = acc[j], hi = acc[4 + j], snd, kp;
#pragma unroll
    for (int e = 0; e < 4; ++e) { snd[e] = odd ? lo[e] : hi[e]; kp[e] = odd ? hi[e] : lo[e]; }
    *(f32x4*)(box + j * 1024 + lane * 16) = snd;
    acc[j] = kp;
  }
  bar();
  const unsigned char* mine = lds + L_HI + w * 5376;
  const unsigned char* cb = cv + (w >> 1) * CVSZ + CV_R;
#pragma unroll
  for (int j = 0; j < 4; ++j) {
    f32x4 y = acc[j] + *(const f32x4*)(mine + j * 1024 + lane * 16);
    const int X = 256 * (4 * odd + j) + 16 * n + 4 * kg;
    u32x2 rv = *(const u32x2*)(cb + (2044 - X) * 2);
    u32 y01 = pk(y.x, y.y), y23 = pk(y.z, y.w);
    u32 s01 = pk(hi16(rv.y) * sdv, lo16(rv.y) * sdv), s23 = pk(hi16(rv.x) * sdv, lo16(rv.x) * sdv);
    u32x4 o;
    o.x = pk(lo16(y01) + lo16(s01), hi16(y01) + hi16(s01)); o.y = tA;
    o.z = pk(lo16(y23) + lo16(s23), hi16(y23) + hi16(s23)); o.w = tB;
    stc(ws, W_GT + (ch0 + (w >> 1)) * 8192 + X * 4, o);
  }
}

struct Scan {
  float ar, ai, cr, ci, a20r, a20i, a40r, a40i, aqr, aqi, bqr, bqi, a320r, a320i;
};

template <bool RS, class F>
DI void gemm_bu(const unsigned char* lds, u32x4 (&Bw)[18][2], int lane, f32x4 (&acc)[5][2], float& rsr, float& rsi, F pre) {
#pragma unroll
  for (int i = 0; i < 5; ++i) { acc[i][0] = f32x4{0, 0, 0, 0}; acc[i][1] = f32x4{0, 0, 0, 0}; }
  int ab0, ab1;
  {
    const int n = lane & 15, kg = lane >> 4;
    ab0 = L_A + (n >> 2) * RB + kg * 256 + (n & 3) * 64;
    ab1 = L_A + (n >> 2) * RB + 20480 + (kg & 1) * 256 + (n & 3) * 64 + (kg >> 1) * 16;
  }
  asm volatile("" : "+v"(ab0), "+v"(ab1));
  auto rdA = [&](int ks, int i) {
    return *(const u32x4*)(lds + (ks < 16 ? ab0 + i * 4096 + (ks >> 2) * 1024 + (ks & 3) * 16 : ab1 + i * 512 + (ks & 1) * 32));
  };
  f32x4 r0 = {0, 0, 0, 0}, r1 = {0, 0, 0, 0};
  const u32x4 ones = {0x3f803f80u, 0x3f803f80u, 0x3f803f80u, 0x3f803f80u};
  u32x4 a[2][5];
#pragma unroll
  for (int i = 0; i < 5; ++i) a[0][i] = rdA(0, i);
  SB();
#pragma unroll
  for (int ks = 0; ks < 18; ++ks) {
    asm("" : "+a"(Bw[ks][0]), "+a"(Bw[ks][1]));
#pragma unroll
    for (int i = 0; i < 5; ++i) {
      if (ks + 1 < 18) a[(ks + 1) & 1][i] = rdA(ks + 1, i);
      acc[i][0] = mma(a[ks & 1][i], Bw[ks][0], acc[i][0]);
      acc[i][1] = mma(a[ks & 1][i], Bw[ks][1], acc[i][1]);
      pre(ks, i);
      SB();
    }
    if (RS) { r0 = mma(ones, Bw[ks][0], r0); r1 = mma(ones, Bw[ks][1], r1); SB(); }
  }
  if (RS) { rsr = r0.x; rsi = r1.x; }
}
DI void scan_a(const Scan& sc, const f32x4 (&acc)[5][2], rsrc_t ws, u32 eoff, int lane, u32 tA, u32 tB, float& inr, float& ini) {
  float er = 0.f, ei = 0.f;
#pragma unroll
  for (int i = 0; i < 5; ++i)
#pragma unroll
    for (int r = 0; r < 4; ++r) {
      float nr = sc.ar * er + (acc[i][0][r] - sc.ai * ei), ni = sc.ar * ei + (acc[i][1][r] + sc.ai * er);
      er = nr; ei = ni;
    }
  const int q = lane >> 4;
  float e0r, e1r, e0i, e1i, pr, pi, p0r, p1r, p0i, p1i, tr, ti;
  pair16(er, e0r, e1r); pair16(ei, e0i, e1i);
  cmul(sc.a20r, sc.a20i, e0r, e0i, pr, pi);
  pr += e1r; pi += e1i;
  pair32(pr, p0r, p1r); pair32(pi, p0i, p1i);
  cmul(sc.a40r, sc.a40i, p0r, p0i, tr, ti);
  stc(ws, q == 0 ? eoff : OOB, u32x4{__float_as_uint(tr + p1r), tA, __float_as_uint(ti + p1i), tB});
  cmul(sc.a20r, sc.a20i, p0r, p0i, tr, ti);
  inr = q == 0 ? 0.f : q == 1 ? e0r : q == 2 ? p0r : tr + e0r;
  ini = q == 0 ? 0.f : q == 1 ? e0i : q == 2 ? p0i : ti + e0i;
}
template <bool PRE>
DI void scan_b(const Scan& sc, const f32x4 (&acc)[5][2], unsigned char* lds, rsrc_t ws, u32 ebase, u32 hoff, int c, int w,
               int lane, u32 tA, u32 tB, float inr, float ini, u32x4 (&v)[7], unsigned& budget) {
  const int n = lane & 15, q = lane >> 4;
  u32 off[7]; bool need[7];
#pragma unroll
  for (int j = 0; j < 7; ++j) {
    int cp = c - 1 - q - 4 * j;
    need[j] = cp >= 0;
    off[j] = ebase + (cp >= 0 ? cp : 0) * (D * 16);
  }
  gather<7, PRE>(ws, off, need, v, tA, tB, budget);
  float hr = 0.f, hi = 0.f;
#pragma unroll
  for (int j = 6; j >= 0; --j) {
    cmul(sc.a320r, sc.a320i, hr, hi, hr, hi);
    hr += need[j] ? __uint_as_float(v[j].x) : 0.f;
    hi += need[j] ? __uint_as_float(v[j].z) : 0.f;
  }
  cmul(sc.bqr, sc.bqi, hr, hi, hr, hi);
  hr = red16(hr, hr); hi = red16(hi, hi);
  hr = red32(hr, hr); hi = red32(hi, hi);
  cmul(sc.aqr, sc.aqi, hr, hi, hr, hi);
  hr += inr; hi += ini;
  unsigned char* img = lds + L_HI + w * 5376;
#pragma unroll
  for (int i = 0; i < 5; ++i) {
    float o_r[4], o_i[4];
#pragma unroll
    for (int r = 0; r < 4; ++r) {
      float nr = sc.ar * hr + (acc[i][0][r] - sc.ai * hi), ni = sc.ar * hi + (acc[i][1][r] + sc.ai * hr);
      hr = nr; hi = ni;
      o_r[r] = sc.cr * hr - sc.ci * hi;
      o_i[r] = -(sc.cr * hi) - sc.ci * hr;
    }
    *(u32x2*)(img + n * 168 + 40 * q + 8 * i) = u32x2{pk(o_r[0], o_r[1]), pk(o_r[2], o_r[3])};
    *(u32x2*)(img + (16 + n) * 168 + 40 * q + 8 * i) = u32x2{pk(o_i[0], o_i[1]), pk(o_i[2], o_i[3])};
  }
  asm volatile("s_waitcnt lgkmcnt(0)" ::: "memory");
  const int tro = L_HI + w * 5376 + (8 * q + (n >> 2)) * 168 + 8 * (n & 3);
#pragma unroll
  for (int i = 0; i < 5; ++i) {
    u32x2 a = trread(lds, tro + 32 * i), b = trread(lds, tro + 4 * 168 + 32 * i);
    stc(ws, hoff + i * 1024 + lane * 16, u32x4{a.x, a.y, b.x, b.y});
  }
}
DI void dma_c4(rsrc_t r, u32 voff, u32 soff, u32 mb) {
  asm volatile("s_mov_b32 m0, %3\n\ts_nop 0\n\tbuffer_load_dwordx4 %0, %1, %2 offen sc1 lds\n\t"
               "buffer_load_dwordx4 %0, %1, %2 offen offset:1024 sc1 lds\n\tbuffer_load_dwordx4 %0, %1, %2 offen offset:2048 sc1 lds\n\t"
               "buffer_load_dwordx4 %0, %1, %2 offen offset:3072 sc1 lds" :: "v"(voff), "s"(r), "s"(soff), "s"(mb) : "memory");
}
template <int NX, class F>
DI void gemm_y(unsigned char* lds, u32 lb, rsrc_t ws, u32 hbase, u32x4 (&Cw)[9][4], int w, int g, int lane, f32x4 (&y)[5], F mid) {
  f32x4 acc[4][5];
#pragma unroll
  for (int d = 0; d < 4; ++d)
#pragma unroll
    for (int i = 0; i < 5; ++i) acc[d][i] = d ? f32x4{0, 0, 0, 0} : y[i];
  u32 ho[3];
  {
    const int n = lane & 15, kg = lane >> 4;
    const u32 hb = hbase + (w >> 1) * 18 * 5120 + (w & 1) * 512 + n * 16;
    ho[0] = hb + ((kg + g) % 9) * 10240;
    ho[1] = hb + ((4 + kg + g) % 9) * 10240;
    ho[2] = hb + ((8 + g) % 9) * 10240 + (kg >> 1) * 5120 + (kg & 1) * 256;
  }
  const u32 sa = lb + L_A + w * RB, sh = lb + L_HI + w * 5376;
  auto dmaH = [&](int j) {
    const u32 so = ((j >> 1) & 1) * 5120 + (j & 1) * 256, mb = j == 4 ? sh : sa + (j % 5) * 5120;
    dma_c4(ws, ho[j >> 2], so, mb); dma_c(ws, ho[j >> 2], so + 4096, mb + 4096);
  };
  int ra = L_A + w * RB + lane * 16, rh = L_HI + w * 5376 + lane * 16;
  asm volatile("" : "+v"(ra), "+v"(rh));
  auto rdH = [&](int j, int i) { return *(const u32x4*)(lds + (j == 4 ? rh : ra + (j % 5) * 5120) + i * 1024); };
#pragma unroll
  for (int j = 0; j < 4; ++j) { dmaH(j); mid(10 + j); }
  u32x4 hf[2][5];
  mid(-2);
  vmw<15>();
  mid(-1);
#pragma unroll
  for (int i = 0; i < 5; ++i) hf[0][i] = rdH(0, i);
  SB();
  static_for<45>([&](auto I) {
    constexpr int j = I / 5, i = I % 5;
    if (i == 0 && j < NPIN) asm("" : "+a"(Cw[j][0]));
    if (j < 8) {
      vmw<(j < 5 ? 14 : j < 8 ? 39 - I + NX : 0)>();
      hf[(j + 1) & 1][i] = rdH(j + 1, i);
    }
    acc[3][i] = mma(Cw[j][3], hf[j & 1][i], acc[3][i]);
    acc[2][i] = mma(Cw[j][2], hf[j & 1][i], acc[2][i]);
    SB();
    if constexpr (j < 5) {
      constexpr int J = j + 4;
      dma_c(ws, ho[J >> 2], ((J >> 1) & 1) * 5120 + (J & 1) * 256 + i * 1024, (J == 4 ? sh : sa + (J % 5) * 5120) + i * 1024);
    }
    if (j == 8 && i > 0) {
#pragma unroll
      for (int d = 1; d < 4; ++d) *(f32x4*)(lds + ra + ((d - 1) * 5 + i - 1) * 1024) = acc[d][i > 0 ? i - 1 : 0];
    }
    SB();
    acc[1][i] = mma(Cw[j][1], hf[j & 1][i], acc[1][i]);
    acc[0][i] = mma(Cw[j][0], hf[j & 1][i], acc[0][i]);
    SB();
    if (i == 4) mid(j);
    SB();
  });
#pragma unroll
  for (int d = 1; d < 4; ++d) *(f32x4*)(lds + ra + ((d - 1) * 5 + 4) * 1024) = acc[d][4];
  bar();
  const unsigned char* b1 = lds + L_A + ((w + 3) & 3) * RB + lane * 16;
  const unsigned char* b2 = lds + L_A + ((w + 2) & 3) * RB + 5120 + lane * 16;
  const unsigned char* b3 = lds + L_A + ((w + 1) & 3) * RB + 10240 + lane * 16;
#pragma unroll
  for (int i = 0; i < 5; ++i)
    y[i] = (acc[0][i] + *(const f32x4*)(b1 + i * 1024)) + (*(const f32x4*)(b2 + i * 1024) + *(const f32x4*)(b3 + i * 1024));
}
DI void rs_part(unsigned char* lds, const f32x4 (&z)[5], int w, int lane) {
  const int n = lane & 15, kg = lane >> 4;
#pragma unroll
  for (int i = 0; i < 5; ++i) {
    float s0 = (z[i].x + z[i].y) + (z[i].z + z[i].w);
    float s1 = (z[i].x * z[i].x + z[i].y * z[i].y) + (z[i].z * z[i].z + z[i].w * z[i].w);
    float s = red32(s0, s1);
    *(float*)(lds + L_SP + (w * 80 + 16 * i + n) * 8 + (kg >> 1) * 4) = red16(s, s);
  }
}
DI void rs_pub(unsigned char* lds, rsrc_t ws, u32 base, int tid, u32 tA, u32 tB) {
  const int r = tid < 80 ? tid : 0;
  f32x2 a = *(const f32x2*)(lds + L_SP + r * 8), b = *(const f32x2*)(lds + L_SP + (80 + r) * 8);
  f32x2 e = *(const f32x2*)(lds + L_SP + (160 + r) * 8), f = *(const f32x2*)(lds + L_SP + (240 + r) * 8);
  a = (a + b) + (e + f);
  stc(ws, tid < 80 ? base + tid * 16 : OOB, u32x4{__float_as_uint(a.x), tA, __float_as_uint(a.y), tB});
}
DI u32 rs_off(int w, int lane) {
  const int l3 = lane < 60 ? lane : 0, th = l3 / 20;
  return (240 * th + 20 * w + l3 - 20 * th) * 16;
}
template <bool PRE>
DI void rs_get(unsigned char* lds, int lout, rsrc_t ws, u32 base, int w, int lane, u32x4 (&v)[3], u32 tA, u32 tB, unsigned& budget) {
  u32 off[3]; bool need[3];
#pragma unroll
  for (int j = 0; j < 3; ++j) { off[j] = base + rs_off(w, lane) + j * 1280; need[j] = true; }
  gather<3, PRE>(ws, off, need, v, tA, tB, budget);
  float s0 = __uint_as_float(v[0].x) + __uint_as_float(v[1].x) + __uint_as_float(v[2].x);
  float s1 = __uint_as_float(v[0].z) + __uint_as_float(v[1].z) + __uint_as_float(v[2].z);
  s0 = sum3(s0, lane) * (1.f / D);
  s1 = sum3(s1, lane) * (1.f / D);
  if (lane < 20) *(f32x2*)(lds + lout + (20 * w + lane) * 8) = f32x2{1.f / sqrtf(fmaxf(s1 - s0 * s0, 0.f) + 1e-5f), s0};
}
DI void norm_acc(const unsigned char* lds, int lp, int kg, f32x4 (&acc)[5][2], float rsr, float rsi) {
#pragma unroll
  for (int i = 0; i < 5; ++i) {
    f32x4 pa = *(const f32x4*)(lds + lp + (20 * kg + 4 * i) * 8), pb = *(const f32x4*)(lds + lp + (20 * kg + 4 * i) * 8 + 16);
    acc[i][0].x = (acc[i][0].x - pa.y * rsr) * pa.x; acc[i][1].x = (acc[i][1].x - pa.y * rsi) * pa.x;
    acc[i][0].y = (acc[i][0].y - pa.w * rsr) * pa.z; acc[i][1].y = (acc[i][1].y - pa.w * rsi) * pa.z;
    acc[i][0].z = (acc[i][0].z - pb.y * rsr) * pb.x; acc[i][1].z = (acc[i][1].z - pb.y * rsi) * pb.x;
    acc[i][0].w = (acc[i][0].w - pb.w * rsr) * pb.z; acc[i][1].w = (acc[i][1].w - pb.w * rsi) * pb.z;
  }
}
__global__ void __launch_bounds__(256, 1) fused_kernel(Params p) {
  extern __shared__ __attribute__((aligned(16))) unsigned char lds[];
  int tid = threadIdx.x, lane = tid & 63;
  const int w = __builtin_amdgcn_readfirstlane(tid >> 6);
  const int xcd = blockIdx.x & 7, slot = blockIdx.x >> 3;
  int c, g, spare = -1;
  if (slot < 27) { c = xcd * 3 + slot / 9; g = slot % 9; }
  else {
    int e = (xcd & 3) * 5 + (slot - 27);
    if (xcd < 4) { int e2 = (xcd & 1) * 5 + (slot - 27); c = 24 + (xcd >> 1); g = e2; if (e2 >= 9) spare = 20 + (xcd >> 1); }
    else { spare = e; c = 0; g = e >> 1; }
  }
#define DUMP() do {} while (0)
  STAMP(0);
  {
    u64 pc; u32 len;
    asm volatile("s_getpc_b64 %0\n.Lpf%=:\n\ts_mov_b32 %1, .Lkend_mark-.Lpf%=" : "=s"(pc), "=s"(len));
    const u32 o = (((slot & 15) * 4 + w) * 64 + lane) * 16;
    asm volatile("s_mov_b32 m0, %2\n\ts_nop 0\n\tglobal_load_lds_dwordx4 %0, %1" :: "v"((int)o < (int)len ? o : 0u), "s"(pc & ~15ull),
                 "s"((u32)(size_t)(lds_void*)lds + L_TOT - 1024) : "memory");
  }
  const rsrc_t ws = __builtin_amdgcn_make_buffer_rsrc(p.ws, 0, W_END, 0x00020000);
  u32 ep = *(const __attribute__((address_space(4))) u32*)p.ws;
#define TAGS() asm volatile("" : "+s"(ep)); const u32 tA = ep + 1u; \
  const u32 tB = tA * 0x9E3779B1u + 0x7F4A7C15u
  unsigned budget = SPIN_MAX;
  int n = lane & 15, kg = lane >> 4;
#define PHASE() do { asm volatile("" : "+v"(tid)); lane = tid & 63; n = lane & 15; kg = lane >> 4; pch = 64 * g + 16 * w + n; } while (0)

  if (spare >= 0) {
    auto warm = [&]() {
      constexpr u32 NA = (W_ST + 3 * NC * 9 * 1280) / 128;
#pragma unroll
      for (int r = 0; r < 11; ++r) {
        const u32 L = (r * 4 + spare - 18) * 256 + tid, o = L < NA ? L * 128 : OOB;
        asm volatile("s_mov_b32 m0, %2\n\ts_nop 0\n\tbuffer_load_dword %0, %1, 0 offen sc1 lds"
                     :: "v"(o), "s"(ws), "s"((u32)(size_t)(lds_void*)lds + L_TOT - 256) : "memory");
      }
    };
    if (spare >= 18) { warm(); drain(); return; }
    const int ch0 = 64 * g + 52 + 6 * (spare & 1);
#pragma unroll
    for (int i = 0; i < 6; ++i) {
      const u16* kp = p.sk + (ch0 + i) * S + 8 * tid;
      u32x4 kx = ldg16(kp);
      u32 kn = tid < 255 ? kp[8] : 0;
      conv_stage_k(lds + i * CVSZ, kx, kn, tid);
    }
    u32 sd0 = p.sd[ch0 + (w >> 1)], sd1 = p.sd[ch0 + 2 + (w >> 1)], sd2 = p.sd[ch0 + 4 + (w >> 1)];
    TAGS();
    u32x4 kq[3][4];
    conv_gather<false>(lds, ws, ch0, tid, kq[0], tA, tB, budget);
#pragma unroll
    for (int r = 4; r < 12; ++r) kq[r >> 2][r & 3] = ldc(ws, W_KVT + ch0 * 8192 + (tid + 256 * r) * 16);
    conv_gather<true>(lds + 2 * CVSZ, ws, ch0 + 2, tid, kq[1], tA, tB, budget);
    conv_gather<true>(lds + 4 * CVSZ, ws, ch0 + 4, tid, kq[2], tA, tB, budget);
#pragma unroll 1
    for (int r = 0; r < 3; ++r) {
      const float sdv = lo16(r == 0 ? sd0 : r == 1 ? sd1 : sd2);
      bar();
      f32x4 acc[8];
#pragma unroll
      for (int j = 0; j < 8; ++j) acc[j] = f32x4{0, 0, 0, 0};
      conv_mma<0, 4>(lds, 2 * r * CVSZ, w, lane, acc, [](int, int) {});
      conv_finish(lds, lds + 2 * r * CVSZ, ws, ch0 + 2 * r, sdv, w, lane, acc, tA, tB);
    }
    asm volatile(".Lkend_mark:");
    STAMP(15);
    DUMP();
    return;
  }

  const int t0 = 80 * c;
  const u32 lb = (u32)(size_t)(lds_void*)lds;
  u32 xo[8];
  {
    const int r = (lane >> 2) & 3, jj = lane & 3;
#pragma unroll
    for (int i = 0; i < 5; ++i) {
      int t = t0 + 20 * w + 4 * i + r; t = t > S - 1 ? S - 1 : t;
      xo[i] = (t * D + 32 * kg + 8 * jj) * 2;
    }
#pragma unroll
    for (int u = 0; u < 3; ++u) {
      int t = t0 + 20 * w + 8 * u + 4 * (kg >> 1) + r; t = t > S - 1 ? S - 1 : t;
      xo[5 + u] = (t * D + 512 + 32 * (kg & 1) + 8 * jj) * 2;
    }
  }
  auto xdma = [&](int pc) {
    const u32 la = lb + L_A + w * RB + (pc >> 1) * 4096;
    if (pc & 1) { dma_g<512>(p.x, xo[pc >> 1], la + 2048); dma_g<768>(p.x, xo[pc >> 1], la + 3072); }
    else { dma_g<0>(p.x, xo[pc >> 1], la); dma_g<256>(p.x, xo[pc >> 1], la + 1024); }
    if (pc < 3) dma_g<0>(p.x, xo[5 + pc], lb + L_A + w * RB + 20480 + pc * 1024);
  };
  STAMP(26);
  u32x2 kk[8], vv[5];
  u32x4 shw[2];
  {
    const int col = 64 * g + 4 * (tid & 15), r0 = t0 + 5 * (tid >> 4);
#pragma unroll
    for (int j = 0; j < 8; ++j) { int t = r0 + j - 3; t = t < 0 ? 0 : (t > S - 1 ? S - 1 : t); kk[j] = ldg8(p.k + t * D + col); }
#pragma unroll
    for (int j = 0; j < 5; ++j) { int t = r0 + j; t = t > S - 1 ? S - 1 : t; vv[j] = ldg8(p.v + t * D + col); }
    shw[0] = ldg16(p.shk + col * 4); shw[1] = ldg16(p.shk + col * 4 + 8);
  }
  const int ch0 = 64 * g + 2 * c;
  u32x4 kx0 = ldg16(p.sk + ch0 * S + 8 * tid), kx1 = ldg16(p.sk + (ch0 + 1) * S + 8 * tid);
  u32 kn0 = p.sk[ch0 * S + 8 * tid + (tid < 255 ? 8 : 0)], kn1 = p.sk[(ch0 + 1) * S + 8 * tid + (tid < 255 ? 8 : 0)];
  int pch = 64 * g + 16 * w + n;
  u32 lre_h = p.lre[pch], lim_h = p.lim[pch], ldt_h = p.ldt[pch];
  __builtin_amdgcn_sched_barrier(0);
  u32x4 Bw[18][2];
  const int bo = (64 * g + 16 * w + (n & 12)) * D + 8 * (lane & 3);
  const u16 *br = p.bre + bo + 32 * kg, *bi = p.bim + bo + 32 * kg;
  const u16* bt = (kg < 2 ? p.bre : p.bim) + bo + 512 + 32 * (kg & 1);
  auto bld = [&](int G) {
    SB();
#pragma unroll
    for (int j = 0; j < 4; ++j) {
      if (G < 8) Bw[4 * (G >> 1) + j][G & 1] = ldg16(((G & 1) ? bi : br) + j * D + 128 * (G >> 1));
      else Bw[16 + (j >> 1)][j & 1] = ldg16(bt + j * D);
    }
    SB();
  };
#pragma unroll
  for (int G = 0; G < 5; ++G) bld(G);
  int twa = L_HI + w * 5376 + lane * 16, tra = L_HI + w * 5376 + (n & 3) * 1040 + (n >> 2) * 64;
  auto bltr = [&](int G) {
    const int i = G >> 1, m = G & 1;
    if (G < 8) ltr<16, 32, 48>(lds, twa, tra + kg * 256, Bw[4 * i][m], Bw[4 * i + 1][m], Bw[4 * i + 2][m], Bw[4 * i + 3][m]);
    else ltr<512, 32, 544>(lds, twa, tra + (kg & 1) * 256 + (kg >> 1) * 16, Bw[16][0], Bw[16][1], Bw[17][0], Bw[17][1]);
  };
  STAMP(20);
  {
    const int r0 = 5 * (tid >> 4);
    const bool first = (t0 + r0 == 0);
    asm volatile("" : "+v"(kk[0]), "+v"(kk[1]), "+v"(kk[2]), "+v"(kk[3]), "+v"(kk[4]), "+v"(kk[5]), "+v"(kk[6]), "+v"(kk[7]),
                 "+v"(vv[0]), "+v"(vv[1]), "+v"(vv[2]), "+v"(vv[3]), "+v"(vv[4]), "+v"(shw[0]), "+v"(shw[1]));
    asm volatile("" : "+v"(kx0), "+v"(kx1), "+v"(kn0), "+v"(kn1), "+v"(lre_h), "+v"(lim_h), "+v"(ldt_h));
    u16* img = (u16*)(lds + L_GI) + 4 * (tid & 15) * 84 + r0;
#pragma unroll
    for (int h = 0; h < 2; ++h) {
      f32x2 sw[4], kf[8];
      sw[0] = f32x2{lo16(shw[h].x), lo16(shw[h].z)}; sw[1] = f32x2{hi16(shw[h].x), hi16(shw[h].z)};
      sw[2] = f32x2{lo16(shw[h].y), lo16(shw[h].w)}; sw[3] = f32x2{hi16(shw[h].y), hi16(shw[h].w)};
#pragma unroll
      for (int j = 0; j < 8; ++j) {
        u32 q = kk[j][h];
        if (j < 3) q = first ? 0u : q;
        kf[j] = f32x2{lo16(q), hi16(q)};
      }
#pragma unroll
      for (int r = 0; r < 5; ++r) {
        SB();
        if (h == 0) bltr(r);
        else { xdma(2 * r); xdma(2 * r + 1); }
        SB();
        f32x2 a = kf[r] * sw[3];
#pragma unroll
        for (int j = 1; j < 4; ++j) a = __builtin_elementwise_fma(kf[r + j], sw[3 - j], a);
        a *= f32x2{lo16(vv[r][h]), hi16(vv[r][h])};
        const u32 o = pk(a.x, a.y);
        img[(2 * h) * 84 + r] = (u16)o;
        img[(2 * h + 1) * 84 + r] = (u16)(o >> 16);
      }
    }
  }
  STAMP(21);
  bld(5);
  conv_stage_k(lds + L_CV, kx0, tid < 255 ? kn0 : 0, tid);
  conv_stage_k(lds + L_CV + CVSZ, kx1, tid < 255 ? kn1 : 0, tid);
  STAMP(30);
  bld(6);
  Scan sc;
  {
    float lr = lo16(lre_h), li = lo16(lim_h), dt = rb(expf(lo16(ldt_h)));
    float e = expf(dt * lr), sn, cs;
    sincosf(dt * li, &sn, &cs);
    bld(7);
    sc.ar = e * cs; sc.ai = e * sn;
    float nr = sc.ar - 1.f, ni = sc.ai, dn = lr * lr + li * li;
    sc.cr = 2.f * ((nr * lr + ni * li) / dn); sc.ci = 2.f * ((ni * lr - nr * li) / dn);
    bld(8);
    float r2, i2, r4, i4, r5, i5, r80, i80, r160, i160, r60, i60, r240, i240;
    cmul(sc.ar, sc.ai, sc.ar, sc.ai, r2, i2); cmul(r2, i2, r2, i2, r4, i4); cmul(r4, i4, sc.ar, sc.ai, r5, i5);
    cmul(r5, i5, r5, i5, r2, i2); cmul(r2, i2, r2, i2, sc.a20r, sc.a20i);
    cmul(sc.a20r, sc.a20i, sc.a20r, sc.a20i, sc.a40r, sc.a40i);
    cmul(sc.a40r, sc.a40i, sc.a20r, sc.a20i, r60, i60);
    cmul(sc.a40r, sc.a40i, sc.a40r, sc.a40i, r80, i80);
    cmul(r80, i80, r80, i80, r160, i160); cmul(r160, i160, r80, i80, r240, i240);
    cmul(r160, i160, r160, i160, sc.a320r, sc.a320i);
    sc.aqr = kg == 0 ? 1.f : kg == 1 ? sc.a20r : kg == 2 ? sc.a40r : r60;
    sc.aqi = kg == 0 ? 0.f : kg == 1 ? sc.a20i : kg == 2 ? sc.a40i : i60;
    sc.bqr = kg == 0 ? 1.f : kg == 1 ? r80 : kg == 2 ? r160 : r240;
    sc.bqi = kg == 0 ? 0.f : kg == 1 ? i80 : kg == 2 ? i160 : i240;
  }
  STAMP(28);
  asm volatile("s_waitcnt vmcnt(16)" ::: "memory");
  {
    const u32 ones = 0x3f803f80u;
#pragma unroll
    for (int i = 0; i < 5; ++i) {
      u32x4 xv[5];
#pragma unroll
      for (int q = 0; q < 4; ++q) xv[q] = *(const u32x4*)(lds + L_A + w * RB + (i * 4 + q) * 1024 + lane * 16);
      xv[4] = *(const u32x4*)(lds + L_A + w * RB + 20480 + i * 512 + (lane & 31) * 16);
      SB();
      if (i > 0) bltr(4 + i);
      SB();
      float s1 = 0.f, s2 = 0.f;
#pragma unroll
      for (int e = 0; e < 4; ++e) { s1 = dot2(xv[4][e], ones, s1); s2 = dot2(xv[4][e], xv[4][e], s2); }
      s1 *= 0.5f; s2 *= 0.5f;
#pragma unroll
      for (int q = 0; q < 4; ++q)
#pragma unroll
        for (int e = 0; e < 4; ++e) { s1 = dot2(xv[q][e], ones, s1); s2 = dot2(xv[q][e], xv[q][e], s2); }
      float sm = red32(s1, s2);
      sm = quad_sum(red16(sm, sm));
      *(float*)(lds + L_SP + (20 * w + 4 * i + (n >> 2)) * 8 + (kg >> 1) * 4) = sm;
      SB();
    }
    asm volatile("s_waitcnt lgkmcnt(0)" ::: "memory");
    if (lane < 20) {
      f32x2 v = *(const f32x2*)(lds + L_SP + (20 * w + lane) * 8);
      float m = v.x * (1.f / D), var = fmaxf(v.y * (1.f / D) - m * m, 0.f);
      float den = rb(sqrtf(rb(rb(var) + 1e-5f)));
      *(f32x2*)(lds + L_P1 + (20 * w + lane) * 8) = f32x2{1.f / den, rb(m)};
    }
  }
#pragma unroll
  for (int ks = 0; ks < 18; ++ks) asm volatile("" : "+a"(Bw[ks][0]), "+a"(Bw[ks][1]));
  bar();
  STAMP(1);
  TAGS();
  const u64 tF = ((u64)tB << 32) | tA;
  f32x4 acc[5][2];
  float rsr, rsi;
  u32x4 kq[4];
  u32x4 Cw[9][4];
  const rsrc_t crs = __builtin_amdgcn_make_buffer_rsrc((void*)((w & 1) ? p.cim : p.cre), 0, D * D * 2, 0x00020000);
  auto cld = [&](int L) {
    const int d = L >> 3, j = L & 3, cb = ((64 * g + (n & 12)) * D + 288 * (w >> 1) + 8 * (lane & 3)) * 2 + j * D * 2;
    if (L >= 32) Cw[8][j] = __builtin_amdgcn_raw_buffer_load_b128(crs, cb + 64 * ((8 + g) % 9) + ((w + kg) & 3) * 32 * D, 0, 0);
    else Cw[L & 7][d] = __builtin_amdgcn_raw_buffer_load_b128(crs, cb + 64 * (((L & 4) + kg + g) % 9), ((w + d) & 3) * 32 * D, 0);
  };
  {
    u32x2 kvq;
    u32 kvo;
    SB();
    STAMP(23);
    gemm_bu<true>(lds, Bw, lane, acc, rsr, rsi, [&](int ks, int i) {
      if (ks < 5 && i == 0) {
        const int it = tid + 256 * ks, ch = it / 20, tq = it - 20 * ch, gq = 20 * c + tq;
        kvq = *(const u32x2*)(lds + L_GI + ch * 168 + tq * 8);
        kvo = gq < 512 ? W_KVT + (64 * g + ch) * 8192 + (511 - gq) * 16 : OOB;
      }
      if (ks < 5 && i == 4)
        stc(ws, kvo, u32x4{__builtin_amdgcn_alignbit(kvq.y, kvq.y, 16), tA, __builtin_amdgcn_alignbit(kvq.x, kvq.x, 16), tB});
      if (ks >= 6 && i == 2) cld(ks - 6);
      if (ks == 16 && i < 4) kq[i] = ldc(ws, W_KVT + ch0 * 8192 + (tid + 256 * i) * 16);
    });
  }
  STAMP(2);
  norm_acc(lds, L_P1, kg, acc, rsr, rsi);
  *(f32x2*)(lds + L_RS + tid * 8) = f32x2{rsr, rsi};
  float inr, ini;
  scan_a(sc, acc, ws, W_E + (c * D + pch) * 16, lane, tA, tB, inr, ini);
  STAMP(3);
  u32x2 dsk_h = ldg8(p.dsk + 64 * g + 16 * w + 4 * kg);
  const u16 sd_h = p.sd[ch0 + (w >> 1)];
  conv_gather<true>(lds + L_CV, ws, ch0, tid, kq, tA, tB, budget);
  bar();
  STAMP(4);
  f32x4 cacc[8];
  u32x4 ev[7];
#pragma unroll
  for (int j = 0; j < 8; ++j) cacc[j] = f32x4{0, 0, 0, 0};
  conv_mma<0, 2>(lds, L_CV, w, lane, cacc, [&](int r, int b) {
    if (b < 6) cld(12 + 6 * r + b);
    if (r == 1 && b == 2) {
#pragma unroll
      for (int j = 0; j < 7; ++j) { const int cp = c - 1 - kg - 4 * j; ev[j] = ldc(ws, W_E + pch * 16 + (cp >= 0 ? cp : 0) * (D * 16)); }
    }
  });
  STAMP(5);
  scan_b<true>(sc, acc, lds, ws, W_E + pch * 16, W_H + (c * 36 + 4 * g + w) * 5120, c, w, lane, tA, tB, inr, ini, ev, budget);
  STAMP(6);
  {
    conv_mma<2, 4>(lds, L_CV, w, lane, cacc, [&](int r, int b) {
      if (b < 6) cld(12 + 6 * r + b);
      if (r == 3 && b == 7) {
        const u32 o = W_U3 + c * 94208 + w * 23552 + (lane >> 2) * (g < 8 ? 4096 : 512) + (g < 8 ? g * 512 : 20480) + (lane & 3) * 128;
        asm volatile("s_mov_b32 m0, %2\n\ts_nop 0\n\tbuffer_load_dword %0, %1, 0 offen sc1 lds"
                     :: "v"(lane < 20 ? o : OOB), "s"(ws), "s"(lb + L_TOT - 256) : "memory");
      }
      if (r == 2 && b == 7) {
        asm volatile("s_waitcnt vmcnt(6)\n\ts_barrier" ::: "memory");
        stc2(ws, tid == 0 ? W_FLG + c * 128 + g * 8 : OOB, u32x2{tA, tB});
      }
    });
  }
  float dk0 = lo16(dsk_h.x), dk1 = hi16(dsk_h.x), dk2 = lo16(dsk_h.y), dk3 = hi16(dsk_h.y);
  f32x4 z[5];
  auto frag_off = [&](int i, int rb) {
    const int tl = 16 * i + n, q = tl / 20, rem = tl - 20 * q;
    return q * rb + (rem >> 2) * (g < 8 ? 4096 : 512) + (g < 8 ? g * 512 : 20480) + (w >> 1) * 256 + (rem & 3) * 64 + (w & 1) * 32 + kg * 8;
  };
#pragma unroll
  for (int i = 0; i < 5; ++i) {
    u32x2 xq = *(const u32x2*)(lds + L_A + frag_off(i, RB));
    f32x2 pr = *(const f32x2*)(lds + L_P1 + (16 * i + n) * 8);
    float d0 = lo16(xq.x) - pr.y, d1 = hi16(xq.x) - pr.y, d2 = lo16(xq.y) - pr.y, d3 = hi16(xq.y) - pr.y;
    u32 a = pk(d0, d1), b = pk(d2, d3);
    a = pk(lo16(a) * pr.x, hi16(a) * pr.x); b = pk(lo16(b) * pr.x, hi16(b) * pr.x);
    a = pk(lo16(a) * dk0, hi16(a) * dk1); b = pk(lo16(b) * dk2, hi16(b) * dk3);
    z[i] = f32x4{d0 + lo16(a), d1 + hi16(a), d2 + lo16(b), d3 + hi16(b)};
  }
  conv_finish(lds, lds + L_CV, ws, ch0, b2f(sd_h), w, lane, cacc, tA, tB);
  *(f32x4*)(lds + L_CV + tid * 16) = f32x4{sc.ar, sc.ai, sc.cr, sc.ci};
  *(f32x4*)(lds + L_CV + 4096 + tid * 16) = f32x4{sc.a20r, sc.a20i, sc.a40r, sc.a40i};
  *(f32x4*)(lds + L_CV + CVSZ + tid * 16) = f32x4{sc.aqr, sc.aqi, sc.bqr, sc.bqi};
  *(f32x2*)(lds + L_CV + CVSZ + 4096 + tid * 8) = f32x2{sc.a320r, sc.a320i};
  *(u32x2*)(lds + L_CV + CVSZ + 6144 + tid * 8) = dsk_h;
  STAMP(7);
  PHASE();
  twa = L_HI + w * 5376 + lane * 16; tra = L_HI + w * 5376 + (n & 3) * 1040 + (n >> 2) * 64;
  auto cltr = [&](int G) {
    if (G < 8) ltr<16, 32, 48>(lds, twa, tra + kg * 256, Cw[4 * (G & 1)][G >> 1], Cw[4 * (G & 1) + 1][G >> 1], Cw[4 * (G & 1) + 2][G >> 1], Cw[4 * (G & 1) + 3][G >> 1]);
    else ltr<256, 512, 768>(lds, twa, tra + kg * 16, Cw[8][0], Cw[8][1], Cw[8][2], Cw[8][3]);
  };
  {
    const u64 pre = poll_flags(p.ws, W_FLG + c * 128, lane);
    SB();
#pragma unroll
    for (int G = 0; G < 4; ++G) cltr(G);
    SB();
    STAMP(24);
    wait_flags(p.ws, W_FLG + c * 128, tF, lane, budget, pre);
  }
  STAMP(8);
  u32 gq[5][2];
  {
    u32 off[5]; bool need[5]; u32x4 v[5];
    auto goff = [&]() {
#pragma unroll
      for (int r = 0; r < 5; ++r) {
        const int it = lane + 64 * r, ch = it / 20, tq = it - 20 * ch, q = 20 * c + tq;
        need[r] = true;
        off[r] = W_GT + (64 * g + 16 * w + ch) * 8192 + (q < 512 ? q : 511) * 16;
      }
    };
    gemm_y<5>(lds, lb, ws, W_H + c * 36 * 5120, Cw, w, g, lane, z, [&](int j) {
      if (j >= 10) cltr(j - 6);
      if (j == -2) cltr(8);
      if (j == -2) STAMP(22);
      if (j == -1) STAMP(29);
      if (j == 4) STAMP(27);
      if (j == 8) STAMP(31);
      if (j != 4) return;
      goff();
#pragma unroll
      for (int r = 0; r < 5; ++r) v[r] = ldc(ws, off[r]);
    });
    STAMP(9);
    PHASE();
    rs_part(lds, z, w, lane);
    bar();
    rs_pub(lds, ws, W_ST + (c * 9 + g) * 1280, tid, tA, tB);
    goff();
    gather<5, true>(ws, off, need, v, tA, tB, budget);
#pragma unroll
    for (int r = 0; r < 5; ++r) {
      const int it = lane + 64 * r, ch = it / 20, tq = it - 20 * ch;
      *(u32x2*)(lds + L_GI + (16 * w + ch) * 168 + tq * 8) = u32x2{v[r].x, v[r].z};
    }
    asm volatile("s_waitcnt lgkmcnt(0)" ::: "memory");
#pragma unroll
    for (int i = 0; i < 5; ++i) {
      u32x2 t = trread(lds, L_GI + (16 * w + 4 * kg + (n >> 2)) * 168 + 8 * (n & 3) + 32 * i);
      gq[i][0] = t.x; gq[i][1] = t.y;
    }
  }
  STAMP(10);
  u32x4 sv[3];
  rs_get<false>(lds, L_PAR, ws, W_ST + c * 9 * 1280, w, lane, sv, tA, tB, budget);
  bar();
  STAMP(11);
#pragma unroll
  for (int i = 0; i < 5; ++i) {
    f32x2 pr = *(const f32x2*)(lds + L_PAR + (16 * i + n) * 8);
    float gv[4] = {lo16(gq[i][0]), hi16(gq[i][0]), lo16(gq[i][1]), hi16(gq[i][1])};
#pragma unroll
    for (int e = 0; e < 4; ++e) { float t = z[i][e] - pr.y; z[i][e] = t + t * pr.x * gv[e]; }
    stc2(ws, W_U3 + c * 94208 + frag_off(i, 23552), u32x2{pk(z[i].x, z[i].y), pk(z[i].z, z[i].w)});
  }
  SB();
  rs_part(lds, z, w, lane);
  drain();
  bar();
  stc2(ws, tid == 0 ? W_FLG + (NC + c) * 128 + g * 8 : OOB, u32x2{tA, tB});
  rs_pub(lds, ws, W_ST + ((NC + c) * 9 + g) * 1280, tid, tA, tB);
  wait_flags(p.ws, W_FLG + (NC + c) * 128, tF, lane, budget, tF + 1);
  STAMP(12);
  PHASE();
  {
    const u32 ub = W_U3 + c * 94208 + w * 23552 + lane * 16;
#pragma unroll
    for (int j = 0; j < 5; ++j) dma_c4(ws, ub, j * 4096, lb + L_A + w * RB + j * 4096);
#pragma unroll
    for (int j = 20; j < 23; ++j) dma_c(ws, ub, j * 1024, lb + L_A + w * RB + j * 1024);
  }
  drain();
  bar();
  STAMP(13);
#pragma unroll
  for (int i = 0; i < 5; ++i) asm volatile("" : "+v"(gq[i][0]), "+v"(gq[i][1]));
  gemm_bu<false>(lds, Bw, lane, acc, rsr, rsi, [&](int ks, int i) {
    if (ks == 12 && i < 3) sv[i] = ldc(ws, W_ST + (NC + c) * 9 * 1280 + rs_off(w, lane) + i * 1280);
  });
  STAMP(25);
  PHASE();
  rs_get<true>(lds, L_P3, ws, W_ST + (NC + c) * 9 * 1280, w, lane, sv, tA, tB, budget);
  bar();
  {
    const f32x2 q = *(const f32x2*)(lds + L_RS + tid * 8);
    norm_acc(lds, L_P3, kg, acc, q.x, q.y);
  }
  {
    f32x4 a = *(const f32x4*)(lds + L_CV + tid * 16), b = *(const f32x4*)(lds + L_CV + 4096 + tid * 16);
    f32x4 e = *(const f32x4*)(lds + L_CV + CVSZ + tid * 16);
    f32x2 f = *(const f32x2*)(lds + L_CV + CVSZ + 4096 + tid * 8);
    sc.ar = a.x; sc.ai = a.y; sc.cr = a.z; sc.ci = a.w; sc.a20r = b.x; sc.a20i = b.y; sc.a40r = b.z; sc.a40i = b.w;
    sc.aqr = e.x; sc.aqi = e.y; sc.bqr = e.z; sc.bqi = e.w; sc.a320r = f.x; sc.a320i = f.y;
  }
  scan_a(sc, acc, ws, W_E + ((NC + c) * D + pch) * 16, lane, tA, tB, inr, ini);
  STAMP(14);
  dsk_h = *(const u32x2*)(lds + L_CV + CVSZ + 6144 + tid * 8);
#pragma unroll
  for (int i = 0; i < 5; ++i) {
    f32x2 pr = *(const f32x2*)(lds + L_P3 + (16 * i + n) * 8);
    const f32x4 t = z[i] - pr.y;
    z[i] = t + t * pr.x * f32x4{lo16(dsk_h.x), hi16(dsk_h.x), lo16(dsk_h.y), hi16(dsk_h.y)};
  }
  SB();
  scan_b<false>(sc, acc, lds, ws, W_E + (NC * D + pch) * 16, W_H + ((NC + c) * 36 + 4 * g + w) * 5120, c, w, lane, tA, tB, inr, ini, ev, budget);
  drain();
  bar();
  stc2(ws, tid == 0 ? W_FLG + (2 * NC + c) * 128 + g * 8 : OOB, u32x2{tA, tB});
  STAMP(15);
  wait_flags(p.ws, W_FLG + (2 * NC + c) * 128, tF, lane, budget, tF + 1);
  STAMP(16);
  {
    gemm_y<0>(lds, lb, ws, W_H + (NC + c) * 36 * 5120, Cw, w, g, lane, z, [](int) {});
    rs_part(lds, z, w, lane);
  }
  bar();
  STAMP(17);
  rs_pub(lds, ws, W_ST + ((2 * NC + c) * 9 + g) * 1280, tid, tA, tB);
  rs_get<false>(lds, L_P2, ws, W_ST + (2 * NC + c) * 9 * 1280, w, lane, sv, tA, tB, budget);
  bar();
  STAMP(18);
  const rsrc_t ors = __builtin_amdgcn_make_buffer_rsrc(p.out, 0, S * D * 4, 0x00020000);
#pragma unroll
  for (int i = 0; i < 5; ++i) {
    f32x2 pr = *(const f32x2*)(lds + L_P2 + (16 * i + n) * 8);
    const float cen = *(const float*)(lds + L_P1 + (16 * i + n) * 8 + 4) + *(const float*)(lds + L_PAR + (16 * i + n) * 8 + 4) +
                      *(const float*)(lds + L_P3 + (16 * i + n) * 8 + 4);
    float gv[4] = {lo16(gq[i][0]), hi16(gq[i][0]), lo16(gq[i][1]), hi16(gq[i][1])};
    f32x4 o;
#pragma unroll
    for (int e = 0; e < 4; ++e) o[e] = (z[i][e] + cen) + (z[i][e] - pr.y) * pr.x * gv[e];
    const int t = t0 + 16 * i + n;
    __builtin_amdgcn_raw_buffer_store_b128(__builtin_bit_cast(u32x4, o), ors, (t * D + 64 * g + 16 * w + 4 * kg) * 4, 0, 19);
  }
  if (c == NC - 1 && g == 0 && tid == 0) __hip_atomic_store((gu32*)p.ws, tA, __ATOMIC_RELAXED, __HIP_MEMORY_SCOPE_AGENT);
  STAMP(19);
  DUMP();
}

extern "C" void kernel_launch(void* const* d_in, const int* in_sizes, int n_in, void* d_out, int out_size, void* d_ws,
                              size_t ws_size, hipStream_t stream) {
  Params p;
  p.x = (const u16*)d_in[0]; p.lre = (const u16*)d_in[1]; p.lim = (const u16*)d_in[2];
  p.bre = (const u16*)d_in[3]; p.bim = (const u16*)d_in[4]; p.cre = (const u16*)d_in[5]; p.cim = (const u16*)d_in[6];
  p.ldt = (const u16*)d_in[7]; p.dsk = (const u16*)d_in[8]; p.k = (const u16*)d_in[10]; p.v = (const u16*)d_in[11];
  p.shk = (const u16*)d_in[12]; p.sk = (const u16*)d_in[13]; p.sd = (const u16*)d_in[14];
  p.out = (float*)d_out; p.ws = (unsigned char*)d_ws;
  (void)hipFuncSetAttribute((const void*)fused_kernel, hipFuncAttributeMaxDynamicSharedMemorySize, L_TOT);
  hipLaunchKernelGGL(fused_kernel, dim3(256), dim3(256), L_TOT, stream, p);
}
```

```cpp
#include <hip/hip_runtime.h>
#include <stdint.h>

typedef unsigned short u16;
typedef unsigned int u32;
typedef unsigned long long u64;
typedef __attribute__((ext_vector_type(4))) unsigned int u32x4;
typedef __attribute__((ext_vector_type(2))) unsigned int u32x2;
typedef __attribute__((ext_vector_type(4))) float f32x4;
typedef __attribute__((ext_vector_type(2))) float f32x2;
typedef __attribute__((ext_vector_type(8))) __bf16 bf16x8;
typedef __attribute__((ext_vector_type(2))) __bf16 bf16x2;
typedef __attribute__((ext_vector_type(4))) short s16x4;
typedef __attribute__((address_space(1))) u64 gu64;
typedef __attribute__((address_space(1))) u32 gu32;
typedef __attribute__((address_space(3))) s16x4 lds_s16x4;
typedef __amdgpu_buffer_rsrc_t rsrc_t;

#define DI __device__ __forceinline__
#define SB() __builtin_amdgcn_sched_barrier(0)

constexpr int S = 2048, D = 576, NC = 26;
constexpr u32 W_FLG = 4096;
constexpr u32 W_E = 32768;
constexpr u32 W_ST = W_E + 2 * NC * D * 16;
constexpr u32 W_KVT = 4u << 20;
constexpr u32 W_GT = W_KVT + D * 8192;
constexpr u32 W_H = W_GT + D * 8192;
constexpr u32 W_U3 = W_H + 2 * NC * 36 * 5120;
constexpr u32 W_END = W_U3 + NC * 94208;
constexpr u32 W_DBG = 64u << 20;
constexpr int L_A = 0;
constexpr int CVSZ = 13056, CV_K1 = 4224, CV_R = 8448;
constexpr int RB = 23568;
constexpr u32 OOB = 0x7ffffff0u;
constexpr int L_CV = 4 * RB;
constexpr int L_HI = L_CV + 2 * CVSZ;
constexpr int L_GI = L_HI + 4 * 5376;
constexpr int L_SP = L_GI + 64 * 168;
constexpr int L_PAR = L_SP + 2560;
constexpr int L_P3 = L_PAR + 640;
constexpr int L_P1 = L_PAR + 1280;
constexpr int L_P2 = L_P1 + 640;
constexpr int L_RS = L_P2 + 640;
constexpr int L_TOT = L_RS + 2048 + 1024;

struct Params {
  const u16 *x, *lre, *lim, *bre, *bim, *cre, *cim, *ldt, *dsk, *k, *v, *shk, *sk, *sd;
  float* out;
  unsigned char* ws;
};

DI float lo16(u32 w) { return __uint_as_float(w << 16); }
DI float hi16(u32 w) { return __uint_as_float(w & 0xffff0000u); }
DI float b2f(u16 h) { return __uint_as_float(((u32)h) << 16); }
DI u32 pk(float a, float b) {
  f32x2 v = {a, b};
  return __builtin_bit_cast(u32, __builtin_convertvector(v, bf16x2));
}
DI float rb(float a) { return lo16(pk(a, a)); }
DI f32x4 mma(u32x4 a, u32x4 b, f32x4 c) {
  return __builtin_amdgcn_mfma_f32_16x16x32_bf16(__builtin_bit_cast(bf16x8, a), __builtin_bit_cast(bf16x8, b), c, 0, 0, 0);
}
DI float dot2(u32 a, u32 b, float c) {
  return __builtin_amdgcn_fdot2_f32_bf16(__builtin_bit_cast(bf16x2, a), __builtin_bit_cast(bf16x2, b), c, false);
}
DI void bar() { asm volatile("s_waitcnt lgkmcnt(0)\n\ts_barrier" ::: "memory"); }
DI void drain() { asm volatile("s_waitcnt vmcnt(0)" ::: "memory"); }
DI void cfence() { asm volatile("" ::: "memory"); }
DI u32x4 ldc(rsrc_t r, u32 off) { return __builtin_amdgcn_raw_buffer_load_b128(r, off, 0, 16); }
DI u32x4 ldcs(rsrc_t r, u32 off, u32 soff) { return __builtin_amdgcn_raw_buffer_load_b128(r, off, soff, 16); }
DI void stc(rsrc_t r, u32 off, u32x4 v) { __builtin_amdgcn_raw_buffer_store_b128(v, r, off, 0, 16); }
DI void stc2(rsrc_t r, u32 off, u32x2 v) { __builtin_amdgcn_raw_buffer_store_b64(v, r, off, 0, 16); }
DI u32x4 ldg16(const u16* p) { return *(const u32x4*)p; }
DI u32x2 ldg8(const u16* p) { return *(const u32x2*)p; }
DI u32x2 trread(unsigned char* lds, int off) {
  s16x4 r = __builtin_amdgcn_ds_read_tr16_b64_v4i16((lds_s16x4*)(lds + off));
  return __builtin_bit_cast(u32x2, r);
}
DI void cmul(float ar, float ai, float br, float bi, float& cr, float& ci) {
  float r = ar * br - ai * bi, i = ar * bi + ai * br;
  cr = r; ci = i;
}
DI float red32(float a, float b) {
  auto r = __builtin_amdgcn_permlane32_swap(__float_as_uint(a), __float_as_uint(b), false, false);
  return __uint_as_float(r[0]) + __uint_as_float(r[1]);
}
DI float red16(float a, float b) {
  auto r = __builtin_amdgcn_permlane16_swap(__float_as_uint(a), __float_as_uint(b), false, false);
  return __uint_as_float(r[0]) + __uint_as_float(r[1]);
}

DI void pair16(float a, float& e, float& o) {
  auto r = __builtin_amdgcn_permlane16_swap(__float_as_uint(a), __float_as_uint(a), false, false);
  e = __uint_as_float(r[0]); o = __uint_as_float(r[1]);
}
DI void pair32(float a, float& e, float& o) {
  auto r = __builtin_amdgcn_permlane32_swap(__float_as_uint(a), __float_as_uint(a), false, false);
  e = __uint_as_float(r[0]); o = __uint_as_float(r[1]);
}
DI float sum3(float a, int lane) {
  return a + __int_as_float(__builtin_amdgcn_ds_bpermute(lane * 4 + 80, __float_as_int(a))) +
         __int_as_float(__builtin_amdgcn_ds_bpermute(lane * 4 + 160, __float_as_int(a)));
}
typedef __attribute__((address_space(3))) void lds_void;
typedef __attribute__((address_space(1))) const void gvoid;
template <int O1, int O2, int O3>
DI void ltr(unsigned char* lds, int wa, int ra, u32x4& r0, u32x4& r1, u32x4& r2, u32x4& r3) {
  *(u32x4*)(lds + wa) = r0; *(u32x4*)(lds + wa + 1040) = r1; *(u32x4*)(lds + wa + 2080) = r2; *(u32x4*)(lds + wa + 3120) = r3;
  cfence();
  r0 = *(const u32x4*)(lds + ra); r1 = *(const u32x4*)(lds + ra + O1);
  r2 = *(const u32x4*)(lds + ra + O2); r3 = *(const u32x4*)(lds + ra + O3);
  cfence();
}
template <int OFF>
DI void dma_g(const u16* base, u32 voff, u32 ldsa) {
  asm volatile("s_mov_b32 m0, %2\n\ts_nop 0\n\tglobal_load_lds_dwordx4 %0, %1 offset:%3" :: "v"(voff), "s"(base), "s"(ldsa - OFF), "n"(OFF) : "memory");
}
DI void dma_c(rsrc_t r, u32 voff, u32 soff, u32 ldsa) {
  asm volatile("s_mov_b32 m0, %3\n\ts_nop 0\n\tbuffer_load_dwordx4 %0, %1, %2 offen sc1 lds" :: "v"(voff), "s"(r), "s"(soff), "s"(ldsa) : "memory");
}
DI float quad_sum(float a) {
  a += __int_as_float(__builtin_amdgcn_update_dpp(0, __float_as_int(a), 0xb1, 0xf, 0xf, true));
  a += __int_as_float(__builtin_amdgcn_update_dpp(0, __float_as_int(a), 0x4e, 0xf, 0xf, true));
  return a;
}
constexpr unsigned SPIN_MAX = 1u << 19;
DI bool spin_next(unsigned& budget) {
  if (budget == 0) return false;
  --budget;
  __builtin_amdgcn_s_sleep(4);
  return true;
}
template <int N, bool PRE = false>
DI void gather(rsrc_t ws, const u32 (&off)[N], const bool (&need)[N], u32x4 (&v)[N], u32 tA, u32 tB, unsigned& budget) {
  if (PRE) {
    bool ok = true;
#pragma unroll
    for (int i = 0; i < N; ++i) ok &= (!need[i]) | ((v[i].y == tA) & (v[i].w == tB));
    if (__all(ok)) return;
  }
  for (;;) {
    cfence();
    bool ok = true;
#pragma unroll
    for (int i = 0; i < N; ++i) v[i] = ldc(ws, off[i]);
#pragma unroll
    for (int i = 0; i < N; ++i) ok &= (!need[i]) | ((v[i].y == tA) & (v[i].w == tB));
    if (__all(ok)) break;
    if (!spin_next(budget)) break;
  }
  cfence();
}
DI u64 poll_flags(unsigned char* wsp, u32 off, int lane) {
  return __hip_atomic_load((gu64*)(wsp + off) + (lane < 9 ? lane : 0), __ATOMIC_RELAXED, __HIP_MEMORY_SCOPE_AGENT);
}
DI void wait_flags(unsigned char* wsp, u32 off, u64 tag, int lane, unsigned& budget, u64 pre) {
  gu64* f = (gu64*)(wsp + off) + (lane < 9 ? lane : 0);
  if (!__all(pre == tag)) for (;;) {
    u64 x = __hip_atomic_load(f, __ATOMIC_RELAXED, __HIP_MEMORY_SCOPE_AGENT);
    if (__all(x == tag)) break;
    if (!spin_next(budget)) break;
  }
  __builtin_amdgcn_fence(__ATOMIC_ACQUIRE, "wavefront");
  cfence();
}

#define STAMP(i) do {} while (0)

template <int V> struct IC { static constexpr int v = V; constexpr operator int() const { return V; } };
template <int N, int K = 0, class F>
DI void static_for(F f) { if constexpr (K < N) { f(IC<K>{}); static_for<N, K + 1>(f); } }
constexpr int NPIN = 7;
template <int N>
DI void vmw() { asm volatile("s_waitcnt vmcnt(%0)" :: "n"(N) : "memory"); }
DI void conv_stage_k(unsigned char* cv, u32x4 kx, u32 knext, int tid) {
  *(u32x4*)(cv + 32 + 16 * tid) = kx;
  u32x4 s;
  s.x = __builtin_amdgcn_alignbit(kx.y, kx.x, 16);
  s.y = __builtin_amdgcn_alignbit(kx.z, kx.y, 16);
  s.z = __builtin_amdgcn_alignbit(kx.w, kx.z, 16);
  s.w = __builtin_amdgcn_alignbit(knext, kx.w, 16);
  *(u32x4*)(cv + CV_K1 + 32 + 16 * tid) = s;
  u32x4 z = {0, 0, 0, 0};
  if (tid == 0) {
    *(u32x4*)(cv) = z; *(u32x4*)(cv + 16) = z; *(u32x4*)(cv + CV_K1) = z;
    u32x4 f = {0, 0, 0, kx.x << 16};
    *(u32x4*)(cv + CV_K1 + 16) = f;
  }
  if (tid >= 64 && tid < 96) *(u32x4*)(cv + CV_R + 4096 + 16 * (tid - 64)) = z;
}
template <bool PRE>
DI void conv_gather(unsigned char* cv, rsrc_t ws, int ch0, int tid, u32x4 (&v)[4], u32 tA, u32 tB, unsigned& budget) {
  u32 off[4]; bool need[4];
#pragma unroll
  for (int r = 0; r < 4; ++r) { off[r] = W_KVT + ch0 * 8192 + (tid + 256 * r) * 16; need[r] = true; }
  gather<4, PRE>(ws, off, need, v, tA, tB, budget);
#pragma unroll
  for (int r = 0; r < 4; ++r) {
    u32x2 w = {v[r].x, v[r].z};
    *(u32x2*)(cv + (r >> 1) * CVSZ + CV_R + ((r & 1) * 256 + tid) * 8) = w;
  }
}
template <int R0, int R1, class F>
DI void conv_mma(unsigned char* lds, int cvo, int w, int lane, f32x4 (&acc)[8], F hook) {
  const int m = lane & 15, kg = lane >> 4;
  const int cb = cvo + (w >> 1) * CVSZ + (w & 1) * 256;
  int ka[4];
  ka[0] = cb + ((m & 1) ? 0 : CV_K1) + 16 * kg + 4 * ((m + 1) >> 1);
  ka[1] = ka[0] + 1024; ka[2] = ka[0] + 2048; ka[3] = ka[0] + 3072;
  int rbp = cb + CV_R + 480 - 32 * m + 16 * kg;
  asm volatile("" : "+v"(ka[0]), "+v"(ka[1]), "+v"(ka[2]), "+v"(ka[3]), "+v"(rbp));
  u32x4 A[8], B[8];
  auto ldA = [&](int r, int a) {
    const u32* q = (const u32*)(lds + ka[a >> 1] + 64 * r + 512 * (a & 1));
    A[a].x = q[0]; A[a].y = q[1]; A[a].z = q[2]; A[a].w = q[3];
  };
  auto ldB = [&](int r, int b) { B[b] = *(const u32x4*)(lds + rbp + 64 * r + 512 * (7 - b)); };
  ldB(R0, 0);
#pragma unroll
  for (int a = 7; a >= 0; --a) ldA(R0, a);
#pragma unroll
  for (int b = 1; b < 8; ++b) ldB(R0, b);
  SB();
#pragma unroll
  for (int r = R0; r < R1; ++r) {
#pragma unroll
    for (int b = 0; b < 8; ++b) {
#pragma unroll
      for (int a = 7 - b; a >= 0; --a) acc[a + b] = mma(A[a], B[b], acc[a + b]);
      SB();
      if (r + 1 < R1) { ldB(r + 1, b); ldA(r + 1, 7 - b); }
      hook(r, b);
      SB();
    }
  }
}
DI void conv_finish(unsigned char* lds, unsigned char* cv, rsrc_t ws, int ch0, float sdv, int w, int lane,
                    f32x4 (&acc)[8], u32 tA, u32 tB) {
  const int n = lane & 15, kg = lane >> 4, odd = w & 1;
  unsigned char* box = lds + L_HI + (w ^ 1) * 5376;
#pragma unroll
  for (int j = 0; j < 4; ++j) {
    f32x4 lo = acc[j], hi = acc[4 + j], snd, kp;
#pragma unroll
    for (int e = 0; e < 4; ++e) { snd[e] = odd ? lo[e] : hi[e]; kp[e] = odd ? hi[e] : lo[e]; }
    *(f32x4*)(box + j * 1024 + lane * 16) = snd;
    acc[j] = kp;
  }
  bar();
  const unsigned char* mine = lds + L_HI + w * 5376;
  const unsigned char* cb = cv + (w >> 1) * CVSZ + CV_R;
#pragma unroll
  for (int j = 0; j < 4; ++j) {
    f32x4 y = acc[j] + *(const f32x4*)(mine + j * 1024 + lane * 16);
    const int X = 256 * (4 * odd + j) + 16 * n + 4 * kg;
    u32x2 rv = *(const u32x2*)(cb + (2044 - X) * 2);
    u32 y01 = pk(y.x, y.y), y23 = pk(y.z, y.w);
    u32 s01 = pk(hi16(rv.y) * sdv, lo16(rv.y) * sdv), s23 = pk(hi16(rv.x) * sdv, lo16(rv.x) * sdv);
    u32x4 o;
    o.x = pk(lo16(y01) + lo16(s01), hi16(y01) + hi16(s01)); o.y = tA;
    o.z = pk(lo16(y23) + lo16(s23), hi16(y23) + hi16(s23)); o.w = tB;
    stc(ws, W_GT + (ch0 + (w >> 1)) * 8192 + X * 4, o);
  }
}

struct Scan {
  float ar, ai, cr, ci, a20r, a20i, a40r, a40i, aqr, aqi, bqr, bqi, a320r, a320i;
};

template <bool RS, class F>
DI void gemm_bu(const unsigned char* lds, u32x4 (&Bw)[18][2], int lane, f32x4 (&acc)[5][2], float& rsr, float& rsi, F pre) {
#pragma unroll
  for (int i = 0; i < 5; ++i) { acc[i][0] = f32x4{0, 0, 0, 0}; acc[i][1] = f32x4{0, 0, 0, 0}; }
  int ab0, ab1;
  {
    const int n = lane & 15, kg = lane >> 4;
    ab0 = L_A + (n >> 2) * RB + kg * 256 + (n & 3) * 64;
    ab1 = L_A + (n >> 2) * RB + 20480 + (kg & 1) * 256 + (n & 3) * 64 + (kg >> 1) * 16;
  }
  asm volatile("" : "+v"(ab0), "+v"(ab1));
  auto rdA = [&](int ks, int i) {
    return *(const u32x4*)(lds + (ks < 16 ? ab0 + i * 4096 + (ks >> 2) * 1024 + (ks & 3) * 16 : ab1 + i * 512 + (ks & 1) * 32));
  };
  f32x4 r0 = {0, 0, 0, 0}, r1 = {0, 0, 0, 0};
  const u32x4 ones = {0x3f803f80u, 0x3f803f80u, 0x3f803f80u, 0x3f803f80u};
  u32x4 a[2][5];
#pragma unroll
  for (int i = 0; i < 5; ++i) a[0][i] = rdA(0, i);
  SB();
#pragma unroll
  for (int ks = 0; ks < 18; ++ks) {
    asm("" : "+a"(Bw[ks][0]), "+a"(Bw[ks][1]));
#pragma unroll
    for (int i = 0; i < 5; ++i) {
      if (ks + 1 < 18) a[(ks + 1) & 1][i] = rdA(ks + 1, i);
      acc[i][0] = mma(a[ks & 1][i], Bw[ks][0], acc[i][0]);
      acc[i][1] = mma(a[ks & 1][i], Bw[ks][1], acc[i][1]);
      pre(ks, i);
      SB();
    }
    if (RS) { r0 = mma(ones, Bw[ks][0], r0); r1 = mma(ones, Bw[ks][1], r1); SB(); }
  }
  if (RS) { rsr = r0.x; rsi = r1.x; }
}
DI void scan_a(const Scan& sc, const f32x4 (&acc)[5][2], rsrc_t ws, u32 eoff, int lane, u32 tA, u32 tB, float& inr, float& ini) {
  float er = 0.f, ei = 0.f;
#pragma unroll
  for (int i = 0; i < 5; ++i)
#pragma unroll
    for (int r = 0; r < 4; ++r) {
      float nr = sc.ar * er + (acc[i][0][r] - sc.ai * ei), ni = sc.ar * ei + (acc[i][1][r] + sc.ai * er);
      er = nr; ei = ni;
    }
  const int q = lane >> 4;
  float e0r, e1r, e0i, e1i, pr, pi, p0r, p1r, p0i, p1i, tr, ti;
  pair16(er, e0r, e1r); pair16(ei, e0i, e1i);
  cmul(sc.a20r, sc.a20i, e0r, e0i, pr, pi);
  pr += e1r; pi += e1i;
  pair32(pr, p0r, p1r); pair32(pi, p0i, p1i);
  cmul(sc.a40r, sc.a40i, p0r, p0i, tr, ti);
  stc(ws, q == 0 ? eoff : OOB, u32x4{__float_as_uint(tr + p1r), tA, __float_as_uint(ti + p1i), tB});
  cmul(sc.a20r, sc.a20i, p0r, p0i, tr, ti);
  inr = q == 0 ? 0.f : q == 1 ? e0r : q == 2 ? p0r : tr + e0r;
  ini = q == 0 ? 0.f : q == 1 ? e0i : q == 2 ? p0i : ti + e0i;
}
template <bool PRE>
DI void scan_b(const Scan& sc, const f32x4 (&acc)[5][2], unsigned char* lds, rsrc_t ws, u32 ebase, u32 hoff, int c, int w,
               int lane, u32 tA, u32 tB, float inr, float ini, u32x4 (&v)[7], unsigned& budget) {
  const int n = lane & 15, q = lane >> 4;
  u32 off[7]; bool need[7];
#pragma unroll
  for (int j = 0; j < 7; ++j) {
    int cp = c - 1 - q - 4 * j;
    need[j] = cp >= 0;
    off[j] = ebase + (cp >= 0 ? cp : 0) * (D * 16);
  }
  gather<7, PRE>(ws, off, need, v, tA, tB, budget);
  float hr = 0.f, hi = 0.f;
#pragma unroll
  for (int j = 6; j >= 0; --j) {
    cmul(sc.a320r, sc.a320i, hr, hi, hr, hi);
    hr += need[j] ? __uint_as_float(v[j].x) : 0.f;
    hi += need[j] ? __uint_as_float(v[j].z) : 0.f;
  }
  cmul(sc.bqr, sc.bqi, hr, hi, hr, hi);
  hr = red16(hr, hr); hi = red16(hi, hi);
  hr = red32(hr, hr); hi = red32(hi, hi);
  cmul(sc.aqr, sc.aqi, hr, hi, hr, hi);
  hr += inr; hi += ini;
  if (!PRE) { cmul(sc.cr, sc.ci, hr, hi, hr, hi); hi = -hi; }
  unsigned char* img = lds + L_HI + w * 5376;
#pragma unroll
  for (int i = 0; i < 5; ++i) {
    float o_r[4], o_i[4];
#pragma unroll
    for (int r = 0; r < 4; ++r) {
      if (PRE) {
        float nr = sc.ar * hr + (acc[i][0][r] - sc.ai * hi), ni = sc.ar * hi + (acc[i][1][r] + sc.ai * hr);
        hr = nr; hi = ni;
        o_r[r] = sc.cr * hr - sc.ci * hi;
        o_i[r] = -(sc.cr * hi) - sc.ci * hr;
      } else {
        float nr = sc.ar * hr + (acc[i][0][r] + sc.ai * hi), ni = sc.ar * hi + (acc[i][1][r] - sc.ai * hr);
        o_r[r] = hr = nr; o_i[r] = hi = ni;
      }
    }
    *(u32x2*)(img + n * 168 + 40 * q + 8 * i) = u32x2{pk(o_r[0], o_r[1]), pk(o_r[2], o_r[3])};
    *(u32x2*)(img + (16 + n) * 168 + 40 * q + 8 * i) = u32x2{pk(o_i[0], o_i[1]), pk(o_i[2], o_i[3])};
  }
  asm volatile("s_waitcnt lgkmcnt(0)" ::: "memory");
  const int tro = L_HI + w * 5376 + (8 * q + (n >> 2)) * 168 + 8 * (n & 3);
#pragma unroll
  for (int i = 0; i < 5; ++i) {
    u32x2 a = trread(lds, tro + 32 * i), b = trread(lds, tro + 4 * 168 + 32 * i);
    stc(ws, hoff + i * 1024 + lane * 16, u32x4{a.x, a.y, b.x, b.y});
  }
}
DI void dma_c4(rsrc_t r, u32 voff, u32 soff, u32 mb) {
  asm volatile("s_mov_b32 m0, %3\n\ts_nop 0\n\tbuffer_load_dwordx4 %0, %1, %2 offen sc1 lds\n\t"
               "buffer_load_dwordx4 %0, %1, %2 offen offset:1024 sc1 lds\n\tbuffer_load_dwordx4 %0, %1, %2 offen offset:2048 sc1 lds\n\t"
               "buffer_load_dwordx4 %0, %1, %2 offen offset:3072 sc1 lds" :: "v"(voff), "s"(r), "s"(soff), "s"(mb) : "memory");
}
template <int NX, class F>
DI void gemm_y(unsigned char* lds, u32 lb, rsrc_t ws, u32 hbase, u32x4 (&Cw)[9][4], int w, int g, int lane, f32x4 (&y)[5], F mid) {
  f32x4 acc[4][5];
#pragma unroll
  for (int d = 0; d < 4; ++d)
#pragma unroll
    for (int i = 0; i < 5; ++i) acc[d][i] = d ? f32x4{0, 0, 0, 0} : y[i];
  u32 ho[3];
  {
    const int n = lane & 15, kg = lane >> 4;
    const u32 hb = hbase + (w >> 1) * 18 * 5120 + (w & 1) * 512 + n * 16;
    ho[0] = hb + ((kg + g) % 9) * 10240;
    ho[1] = hb + ((4 + kg + g) % 9) * 10240;
    ho[2] = hb + ((8 + g) % 9) * 10240 + (kg >> 1) * 5120 + (kg & 1) * 256;
  }
  const u32 sa = lb + L_A + w * RB, sh = lb + L_HI + w * 5376;
  auto dmaH = [&](int j) {
    const u32 so = ((j >> 1) & 1) * 5120 + (j & 1) * 256, mb = j == 4 ? sh : sa + (j % 5) * 5120;
    dma_c4(ws, ho[j >> 2], so, mb); dma_c(ws, ho[j >> 2], so + 4096, mb + 4096);
  };
  int ra = L_A + w * RB + lane * 16, rh = L_HI + w * 5376 + lane * 16;
  asm volatile("" : "+v"(ra), "+v"(rh));
  auto rdH = [&](int j, int i) { return *(const u32x4*)(lds + (j == 4 ? rh : ra + (j % 5) * 5120) + i * 1024); };
#pragma unroll
  for (int j = 0; j < 4; ++j) { dmaH(j); mid(10 + j); }
  u32x4 hf[2][5];
  mid(-2);
  vmw<15>();
  mid(-1);
#pragma unroll
  for (int i = 0; i < 5; ++i) hf[0][i] = rdH(0, i);
  SB();
  static_for<45>([&](auto I) {
    constexpr int j = I / 5, i = I % 5;
    if (i == 0 && j < NPIN) asm("" : "+a"(Cw[j][0]));
    if (j < 8) {
      vmw<(j < 5 ? 14 : j < 8 ? 39 - I + NX : 0)>();
      hf[(j + 1) & 1][i] = rdH(j + 1, i);
    }
    acc[3][i] = mma(Cw[j][3], hf[j & 1][i], acc[3][i]);
    acc[2][i] = mma(Cw[j][2], hf[j & 1][i], acc[2][i]);
    SB();
    if constexpr (j < 5) {
      constexpr int J = j + 4;
      dma_c(ws, ho[J >> 2], ((J >> 1) & 1) * 5120 + (J & 1) * 256 + i * 1024, (J == 4 ? sh : sa + (J % 5) * 5120) + i * 1024);
    }
    if (j == 8 && i > 0) {
#pragma unroll
      for (int d = 1; d < 4; ++d) *(f32x4*)(lds + ra + ((d - 1) * 5 + i - 1) * 1024) = acc[d][i > 0 ? i - 1 : 0];
    }
    SB();
    acc[1][i] = mma(Cw[j][1], hf[j & 1][i], acc[1][i]);
    acc[0][i] = mma(Cw[j][0], hf[j & 1][i], acc[0][i]);
    SB();
    if (i == 4) mid(j);
    SB();
  });
#pragma unroll
  for (int d = 1; d < 4; ++d) *(f32x4*)(lds + ra + ((d - 1) * 5 + 4) * 1024) = acc[d][4];
  bar();
  const unsigned char* b1 = lds + L_A + ((w + 3) & 3) * RB + lane * 16;
  const unsigned char* b2 = lds + L_A + ((w + 2) & 3) * RB + 5120 + lane * 16;
  const unsigned char* b3 = lds + L_A + ((w + 1) & 3) * RB + 10240 + lane * 16;
#pragma unroll
  for (int i = 0; i < 5; ++i)
    y[i] = (acc[0][i] + *(const f32x4*)(b1 + i * 1024)) + (*(const f32x4*)(b2 + i * 1024) + *(const f32x4*)(b3 + i * 1024));
}
DI void rs_part(unsigned char* lds, const f32x4 (&z)[5], int w, int lane) {
  const int n = lane & 15, kg = lane >> 4;
#pragma unroll
  for (int i = 0; i < 5; ++i) {
    float s0 = (z[i].x + z[i].y) + (z[i].z + z[i].w);
    float s1 = (z[i].x * z[i].x + z[i].y * z[i].y) + (z[i].z * z[i].z + z[i].w * z[i].w);
    float s = red32(s0, s1);
    *(float*)(lds + L_SP + (w * 80 + 16 * i + n) * 8 + (kg >> 1) * 4) = red16(s, s);
  }
}
DI void rs_pub(unsigned char* lds, rsrc_t ws, u32 base, int tid, u32 tA, u32 tB) {
  const int r = tid < 80 ? tid : 0;
  f32x2 a = *(const f32x2*)(lds + L_SP + r * 8), b = *(const f32x2*)(lds + L_SP + (80 + r) * 8);
  f32x2 e = *(const f32x2*)(lds + L_SP + (160 + r) * 8), f = *(const f32x2*)(lds + L_SP + (240 + r) * 8);
  a = (a + b) + (e + f);
  stc(ws, tid < 80 ? base + tid * 16 : OOB, u32x4{__float_as_uint(a.x), tA, __float_as_uint(a.y), tB});
}
DI u32 rs_off(int w, int lane) {
  const int l3 = lane < 60 ? lane : 0, th = l3 / 20;
  return (240 * th + 20 * w + l3 - 20 * th) * 16;
}
template <bool PRE>
DI void rs_get(unsigned char* lds, int lout, rsrc_t ws, u32 base, int w, int lane, u32x4 (&v)[3], u32 tA, u32 tB, unsigned& budget) {
  u32 off[3]; bool need[3];
#pragma unroll
  for (int j = 0; j < 3; ++j) { off[j] = base + rs_off(w, lane) + j * 1280; need[j] = true; }
  gather<3, PRE>(ws, off, need, v, tA, tB, budget);
  float s0 = __uint_as_float(v[0].x) + __uint_as_float(v[1].x) + __uint_as_float(v[2].x);
  float s1 = __uint_as_float(v[0].z) + __uint_as_float(v[1].z) + __uint_as_float(v[2].z);
  s0 = sum3(s0, lane) * (1.f / D);
  s1 = sum3(s1, lane) * (1.f / D);
  if (lane < 20) *(f32x2*)(lds + lout + (20 * w + lane) * 8) = f32x2{__builtin_amdgcn_rsqf(fmaxf(s1 - s0 * s0, 0.f) + 1e-5f), s0};
}
DI void norm_acc(const unsigned char* lds, int lp, int kg, f32x4 (&acc)[5][2], float rsr, float rsi) {
#pragma unroll
  for (int i = 0; i < 5; ++i) {
    f32x4 pa = *(const f32x4*)(lds + lp + (20 * kg + 4 * i) * 8), pb = *(const f32x4*)(lds + lp + (20 * kg + 4 * i) * 8 + 16);
    acc[i][0].x = (acc[i][0].x - pa.y * rsr) * pa.x; acc[i][1].x = (acc[i][1].x - pa.y * rsi) * pa.x;
    acc[i][0].y = (acc[i][0].y - pa.w * rsr) * pa.z; acc[i][1].y = (acc[i][1].y - pa.w * rsi) * pa.z;
    acc[i][0].z = (acc[i][0].z - pb.y * rsr) * pb.x; acc[i][1].z = (acc[i][1].z - pb.y * rsi) * pb.x;
    acc[i][0].w = (acc[i][0].w - pb.w * rsr) * pb.z; acc[i][1].w = (acc[i][1].w - pb.w * rsi) * pb.z;
  }
}
__global__ void __launch_bounds__(256, 1) fused_kernel(Params p) {
  extern __shared__ __attribute__((aligned(16))) unsigned char lds[];
  int tid = threadIdx.x, lane = tid & 63;
  const int w = __builtin_amdgcn_readfirstlane(tid >> 6);
  const int xcd = blockIdx.x & 7, slot = blockIdx.x >> 3;
  int c, g, spare = -1;
  if (slot < 27) { c = xcd * 3 + slot / 9; g = slot % 9; }
  else {
    int e = (xcd & 3) * 5 + (slot - 27);
    if (xcd < 4) { int e2 = (xcd & 1) * 5 + (slot - 27); c = 24 + (xcd >> 1); g = e2; if (e2 >= 9) spare = 20 + (xcd >> 1); }
    else { spare = e; c = 0; g = e >> 1; }
  }
#define DUMP() do {} while (0)
  STAMP(0);
  {
    u64 pc; u32 len;
    asm volatile("s_getpc_b64 %0\n.Lpf%=:\n\ts_mov_b32 %1, .Lkend_mark-.Lpf%=" : "=s"(pc), "=s"(len));
    const u32 o = (((slot & 15) * 4 + w) * 64 + lane) * 16;
    asm volatile("s_mov_b32 m0, %2\n\ts_nop 0\n\tglobal_load_lds_dwordx4 %0, %1" :: "v"((int)o < (int)len ? o : 0u), "s"(pc & ~15ull),
                 "s"((u32)(size_t)(lds_void*)lds + L_TOT - 1024) : "memory");
  }
  const rsrc_t ws = __builtin_amdgcn_make_buffer_rsrc(p.ws, 0, W_END, 0x00020000);
  u32 ep = *(const __attribute__((address_space(4))) u32*)p.ws;
#define TAGS() asm volatile("" : "+s"(ep)); const u32 tA = ep + 1u; \
  const u32 tB = tA * 0x9E3779B1u + 0x7F4A7C15u
  unsigned budget = SPIN_MAX;
  int n = lane & 15, kg = lane >> 4;
#define PHASE() do { asm volatile("" : "+v"(tid)); lane = tid & 63; n = lane & 15; kg = lane >> 4; pch = 64 * g + 16 * w + n; } while (0)

  if (spare >= 0) {
    auto warm = [&]() {
      constexpr u32 NA = (W_ST + 3 * NC * 9 * 1280) / 128;
#pragma unroll
      for (int r = 0; r < 11; ++r) {
        const u32 L = (r * 4 + spare - 18) * 256 + tid, o = L < NA ? L * 128 : OOB;
        asm volatile("s_mov_b32 m0, %2\n\ts_nop 0\n\tbuffer_load_dword %0, %1, 0 offen sc1 lds"
                     :: "v"(o), "s"(ws), "s"((u32)(size_t)(lds_void*)lds + L_TOT - 256) : "memory");
      }
    };
    if (spare >= 18) { warm(); drain(); return; }
    const int ch0 = 64 * g + 52 + 6 * (spare & 1);
#pragma unroll
    for (int i = 0; i < 6; ++i) {
      const u16* kp = p.sk + (ch0 + i) * S + 8 * tid;
      u32x4 kx = ldg16(kp);
      u32 kn = tid < 255 ? kp[8] : 0;
      conv_stage_k(lds + i * CVSZ, kx, kn, tid);
    }
    u32 sd0 = p.sd[ch0 + (w >> 1)], sd1 = p.sd[ch0 + 2 + (w >> 1)], sd2 = p.sd[ch0 + 4 + (w >> 1)];
    TAGS();
    u32x4 kq[3][4];
    __builtin_amdgcn_s_sleep(127);
    conv_gather<false>(lds, ws, ch0, tid, kq[0], tA, tB, budget);
#pragma unroll
    for (int r = 4; r < 12; ++r) kq[r >> 2][r & 3] = ldc(ws, W_KVT + ch0 * 8192 + (tid + 256 * r) * 16);
    conv_gather<true>(lds + 2 * CVSZ, ws, ch0 + 2, tid, kq[1], tA, tB, budget);
    conv_gather<true>(lds + 4 * CVSZ, ws, ch0 + 4, tid, kq[2], tA, tB, budget);
#pragma unroll 1
    for (int r = 0; r < 3; ++r) {
      const float sdv = lo16(r == 0 ? sd0 : r == 1 ? sd1 : sd2);
      bar();
      f32x4 acc[8];
#pragma unroll
      for (int j = 0; j < 8; ++j) acc[j] = f32x4{0, 0, 0, 0};
      conv_mma<0, 4>(lds, 2 * r * CVSZ, w, lane, acc, [](int, int) {});
      conv_finish(lds, lds + 2 * r * CVSZ, ws, ch0 + 2 * r, sdv, w, lane, acc, tA, tB);
    }
    asm volatile(".Lkend_mark:");
    STAMP(15);
    DUMP();
    return;
  }

  const int t0 = 80 * c;
  const u32 lb = (u32)(size_t)(lds_void*)lds;
  u32 xo[8];
  {
    const int r = (lane >> 2) & 3, jj = lane & 3;
#pragma unroll
    for (int i = 0; i < 5; ++i) {
      int t = t0 + 20 * w + 4 * i + r; t = t > S - 1 ? S - 1 : t;
      xo[i] = (t * D + 32 * kg + 8 * jj) * 2;
    }
#pragma unroll
    for (int u = 0; u < 3; ++u) {
      int t = t0 + 20 * w + 8 * u + 4 * (kg >> 1) + r; t = t > S - 1 ? S - 1 : t;
      xo[5 + u] = (t * D + 512 + 32 * (kg & 1) + 8 * jj) * 2;
    }
  }
  auto xdma = [&](int pc) {
    const u32 la = lb + L_A + w * RB + (pc >> 1) * 4096;
    if (pc & 1) { dma_g<512>(p.x, xo[pc >> 1], la + 2048); dma_g<768>(p.x, xo[pc >> 1], la + 3072); }
    else { dma_g<0>(p.x, xo[pc >> 1], la); dma_g<256>(p.x, xo[pc >> 1], la + 1024); }
    if (pc < 3) dma_g<0>(p.x, xo[5 + pc], lb + L_A + w * RB + 20480 + pc * 1024);
  };
  STAMP(26);
  u32x2 kk[8], vv[5];
  u32x4 shw[2];
  {
    const int col = 64 * g + 4 * (tid & 15), r0 = t0 + 5 * (tid >> 4);
#pragma unroll
    for (int j = 0; j < 8; ++j) { int t = r0 + j - 3; t = t < 0 ? 0 : (t > S - 1 ? S - 1 : t); kk[j] = ldg8(p.k + t * D + col); }
#pragma unroll
    for (int j = 0; j < 5; ++j) { int t = r0 + j; t = t > S - 1 ? S - 1 : t; vv[j] = ldg8(p.v + t * D + col); }
    shw[0] = ldg16(p.shk + col * 4); shw[1] = ldg16(p.shk + col * 4 + 8);
  }
  const int ch0 = 64 * g + 2 * c;
  u32x4 kx0 = ldg16(p.sk + ch0 * S + 8 * tid), kx1 = ldg16(p.sk + (ch0 + 1) * S + 8 * tid);
  u32 kn0 = p.sk[ch0 * S + 8 * tid + (tid < 255 ? 8 : 0)], kn1 = p.sk[(ch0 + 1) * S + 8 * tid + (tid < 255 ? 8 : 0)];
  int pch = 64 * g + 16 * w + n;
  u32 lre_h = p.lre[pch], lim_h = p.lim[pch], ldt_h = p.ldt[pch];
  __builtin_amdgcn_sched_barrier(0);
  u32x4 Bw[18][2];
  const int bo = (64 * g + 16 * w + (n & 12)) * D + 8 * (lane & 3);
  const u16 *br = p.bre + bo + 32 * kg, *bi = p.bim + bo + 32 * kg;
  const u16* bt = (kg < 2 ? p.bre : p.bim) + bo + 512 + 32 * (kg & 1);
  auto bld = [&](int G) {
    SB();
#pragma unroll
    for (int j = 0; j < 4; ++j) {
      if (G < 8) Bw[4 * (G >> 1) + j][G & 1] = ldg16(((G & 1) ? bi : br) + j * D + 128 * (G >> 1));
      else Bw[16 + (j >> 1)][j & 1] = ldg16(bt + j * D);
    }
    SB();
  };
#pragma unroll
  for (int G = 0; G < 5; ++G) bld(G);
  int twa = L_HI + w * 5376 + lane * 16, tra = L_HI + w * 5376 + (n & 3) * 1040 + (n >> 2) * 64;
  auto bltr = [&](int G) {
    const int i = G >> 1, m = G & 1;
    if (G < 8) ltr<16, 32, 48>(lds, twa, tra + kg * 256, Bw[4 * i][m], Bw[4 * i + 1][m], Bw[4 * i + 2][m], Bw[4 * i + 3][m]);
    else ltr<512, 32, 544>(lds, twa, tra + (kg & 1) * 256 + (kg >> 1) * 16, Bw[16][0], Bw[16][1], Bw[17][0], Bw[17][1]);
  };
  STAMP(20);
  {
    const int r0 = 5 * (tid >> 4);
    const bool first = (t0 + r0 == 0);
    asm volatile("" : "+v"(kk[0]), "+v"(kk[1]), "+v"(kk[2]), "+v"(kk[3]), "+v"(kk[4]), "+v"(kk[5]), "+v"(kk[6]), "+v"(kk[7]),
                 "+v"(vv[0]), "+v"(vv[1]), "+v"(vv[2]), "+v"(vv[3]), "+v"(vv[4]), "+v"(shw[0]), "+v"(shw[1]));
    asm volatile("" : "+v"(kx0), "+v"(kx1), "+v"(kn0), "+v"(kn1), "+v"(lre_h), "+v"(lim_h), "+v"(ldt_h));
    u16* img = (u16*)(lds + L_GI) + 4 * (tid & 15) * 84 + r0;
#pragma unroll
    for (int h = 0; h < 2; ++h) {
      f32x2 sw[4], kf[8];
      sw[0] = f32x2{lo16(shw[h].x), lo16(shw[h].z)}; sw[1] = f32x2{hi16(shw[h].x), hi16(shw[h].z)};
      sw[2] = f32x2{lo16(shw[h].y), lo16(shw[h].w)}; sw[3] = f32x2{hi16(shw[h].y), hi16(shw[h].w)};
#pragma unroll
      for (int j = 0; j < 8; ++j) {
        u32 q = kk[j][h];
        if (j < 3) q = first ? 0u : q;
        kf[j] = f32x2{lo16(q), hi16(q)};
      }
#pragma unroll
      for (int r = 0; r < 5; ++r) {
        SB();
        if (h == 0) bltr(r);
        else { xdma(2 * r); xdma(2 * r + 1); }
        SB();
        f32x2 a = kf[r] * sw[3];
#pragma unroll
        for (int j = 1; j < 4; ++j) a = __builtin_elementwise_fma(kf[r + j], sw[3 - j], a);
        a *= f32x2{lo16(vv[r][h]), hi16(vv[r][h])};
        const u32 o = pk(a.x, a.y);
        img[(2 * h) * 84 + r] = (u16)o;
        img[(2 * h + 1) * 84 + r] = (u16)(o >> 16);
      }
    }
  }
  STAMP(21);
  bld(5);
  conv_stage_k(lds + L_CV, kx0, tid < 255 ? kn0 : 0, tid);
  conv_stage_k(lds + L_CV + CVSZ, kx1, tid < 255 ? kn1 : 0, tid);
  STAMP(30);
  bld(6);
  Scan sc;
  {
    float lr = lo16(lre_h), li = lo16(lim_h), dt = rb(expf(lo16(ldt_h)));
    float e = expf(dt * lr), sn, cs;
    sincosf(dt * li, &sn, &cs);
    bld(7);
    sc.ar = e * cs; sc.ai = e * sn;
    float nr = sc.ar - 1.f, ni = sc.ai, dn = lr * lr + li * li;
    sc.cr = 2.f * ((nr * lr + ni * li) / dn); sc.ci = 2.f * ((ni * lr - nr * li) / dn);
    bld(8);
    float r2, i2, r4, i4, r5, i5, r80, i80, r160, i160, r60, i60, r240, i240;
    cmul(sc.ar, sc.ai, sc.ar, sc.ai, r2, i2); cmul(r2, i2, r2, i2, r4, i4); cmul(r4, i4, sc.ar, sc.ai, r5, i5);
    cmul(r5, i5, r5, i5, r2, i2); cmul(r2, i2, r2, i2, sc.a20r, sc.a20i);
    cmul(sc.a20r, sc.a20i, sc.a20r, sc.a20i, sc.a40r, sc.a40i);
    cmul(sc.a40r, sc.a40i, sc.a20r, sc.a20i, r60, i60);
    cmul(sc.a40r, sc.a40i, sc.a40r, sc.a40i, r80, i80);
    cmul(r80, i80, r80, i80, r160, i160); cmul(r160, i160, r80, i80, r240, i240);
    cmul(r160, i160, r160, i160, sc.a320r, sc.a320i);
    sc.aqr = kg == 0 ? 1.f : kg == 1 ? sc.a20r : kg == 2 ? sc.a40r : r60;
    sc.aqi = kg == 0 ? 0.f : kg == 1 ? sc.a20i : kg == 2 ? sc.a40i : i60;
    sc.bqr = kg == 0 ? 1.f : kg == 1 ? r80 : kg == 2 ? r160 : r240;
    sc.bqi = kg == 0 ? 0.f : kg == 1 ? i80 : kg == 2 ? i160 : i240;
  }
  STAMP(28);
  asm volatile("s_waitcnt vmcnt(16)" ::: "memory");
  {
    const u32 ones = 0x3f803f80u;
#pragma unroll
    for (int i = 0; i < 5; ++i) {
      u32x4 xv[5];
#pragma unroll
      for (int q = 0; q < 4; ++q) xv[q] = *(const u32x4*)(lds + L_A + w * RB + (i * 4 + q) * 1024 + lane * 16);
      xv[4] = *(const u32x4*)(lds + L_A + w * RB + 20480 + i * 512 + (lane & 31) * 16);
      SB();
      if (i > 0) bltr(4 + i);
      SB();
      float s1 = 0.f, s2 = 0.f;
#pragma unroll
      for (int e = 0; e < 4; ++e) { s1 = dot2(xv[4][e], ones, s1); s2 = dot2(xv[4][e], xv[4][e], s2); }
      s1 *= 0.5f; s2 *= 0.5f;
#pragma unroll
      for (int q = 0; q < 4; ++q)
#pragma unroll
        for (int e = 0; e < 4; ++e) { s1 = dot2(xv[q][e], ones, s1); s2 = dot2(xv[q][e], xv[q][e], s2); }
      float sm = red32(s1, s2);
      sm = quad_sum(red16(sm, sm));
      *(float*)(lds + L_SP + (20 * w + 4 * i + (n >> 2)) * 8 + (kg >> 1) * 4) = sm;
      SB();
    }
    asm volatile("s_waitcnt lgkmcnt(0)" ::: "memory");
    if (lane < 20) {
      f32x2 v = *(const f32x2*)(lds + L_SP + (20 * w + lane) * 8);
      float m = v.x * (1.f / D), var = fmaxf(v.y * (1.f / D) - m * m, 0.f);
      float den = rb(sqrtf(rb(rb(var) + 1e-5f)));
      *(f32x2*)(lds + L_P1 + (20 * w + lane) * 8) = f32x2{1.f / den, rb(m)};
    }
  }
#pragma unroll
  for (int ks = 0; ks < 18; ++ks) asm volatile("" : "+a"(Bw[ks][0]), "+a"(Bw[ks][1]));
  bar();
  STAMP(1);
  TAGS();
  const u64 tF = ((u64)tB << 32) | tA;
  f32x4 acc[5][2];
  float rsr, rsi;
  u32x4 kq[4];
  u32x4 Cw[9][4];
  const rsrc_t crs = __builtin_amdgcn_make_buffer_rsrc((void*)((w & 1) ? p.cim : p.cre), 0, D * D * 2, 0x00020000);
  auto cld = [&](int L) {
    const int d = L >> 3, j = L & 3, cb = ((64 * g + (n & 12)) * D + 288 * (w >> 1) + 8 * (lane & 3)) * 2 + j * D * 2;
    if (L >= 32) Cw[8][j] = __builtin_amdgcn_raw_buffer_load_b128(crs, cb + 64 * ((8 + g) % 9) + ((w + kg) & 3) * 32 * D, 0, 0);
    else Cw[L & 7][d] = __builtin_amdgcn_raw_buffer_load_b128(crs, cb + 64 * (((L & 4) + kg + g) % 9), ((w + d) & 3) * 32 * D, 0);
  };
  {
    u32x2 kvq;
    u32 kvo;
    SB();
    STAMP(23);
    gemm_bu<true>(lds, Bw, lane, acc, rsr, rsi, [&](int ks, int i) {
      if (ks < 5 && i == 0) {
        const int it = tid + 256 * ks, ch = it / 20, tq = it - 20 * ch, gq = 20 * c + tq;
        kvq = *(const u32x2*)(lds + L_GI + ch * 168 + tq * 8);
        kvo = gq < 512 ? W_KVT + (64 * g + ch) * 8192 + (511 - gq) * 16 : OOB;
      }
      if (ks < 5 && i == 4)
        stc(ws, kvo, u32x4{__builtin_amdgcn_alignbit(kvq.y, kvq.y, 16), tA, __builtin_amdgcn_alignbit(kvq.x, kvq.x, 16), tB});
      if (ks >= 6 && i == 2) cld(ks - 6);
      if (ks == 16 && i < 4) kq[i] = ldc(ws, W_KVT + ch0 * 8192 + (tid + 256 * i) * 16);
    });
  }
  STAMP(2);
  norm_acc(lds, L_P1, kg, acc, rsr, rsi);
  *(f32x2*)(lds + L_RS + tid * 8) = f32x2{rsr, rsi};
  float inr, ini;
  scan_a(sc, acc, ws, W_E + (c * D + pch) * 16, lane, tA, tB, inr, ini);
  STAMP(3);
  u32x2 dsk_h = ldg8(p.dsk + 64 * g + 16 * w + 4 * kg);
  const u16 sd_h = p.sd[ch0 + (w >> 1)];
  conv_gather<true>(lds + L_CV, ws, ch0, tid, kq, tA, tB, budget);
  bar();
  STAMP(4);
  f32x4 cacc[8];
  u32x4 ev[7];
#pragma unroll
  for (int j = 0; j < 8; ++j) cacc[j] = f32x4{0, 0, 0, 0};
  conv_mma<0, 2>(lds, L_CV, w, lane, cacc, [&](int r, int b) {
    if (b < 6) cld(12 + 6 * r + b);
    if (r == 1 && b == 2) {
#pragma unroll
      for (int j = 0; j < 7; ++j) { const int cp = c - 1 - kg - 4 * j; ev[j] = ldc(ws, W_E + pch * 16 + (cp >= 0 ? cp : 0) * (D * 16)); }
    }
  });
  STAMP(5);
  scan_b<true>(sc, acc, lds, ws, W_E + pch * 16, W_H + (c * 36 + 4 * g + w) * 5120, c, w, lane, tA, tB, inr, ini, ev, budget);
  STAMP(6);
  {
    conv_mma<2, 4>(lds, L_CV, w, lane, cacc, [&](int r, int b) {
      if (b < 6) cld(12 + 6 * r + b);
      if (r == 3 && b == 7) {
        const u32 o = W_U3 + c * 94208 + w * 23552 + (lane >> 2) * (g < 8 ? 4096 : 512) + (g < 8 ? g * 512 : 20480) + (lane & 3) * 128;
        asm volatile("s_mov_b32 m0, %2\n\ts_nop 0\n\tbuffer_load_dword %0, %1, 0 offen sc1 lds"
                     :: "v"(lane < 20 ? o : OOB), "s"(ws), "s"(lb + L_TOT - 256) : "memory");
      }
      if (r == 2 && b == 7) {
        asm volatile("s_waitcnt vmcnt(6)\n\ts_barrier" ::: "memory");
        stc2(ws, tid == 0 ? W_FLG + c * 128 + g * 8 : OOB, u32x2{tA, tB});
      }
    });
  }
  float dk0 = lo16(dsk_h.x), dk1 = hi16(dsk_h.x), dk2 = lo16(dsk_h.y), dk3 = hi16(dsk_h.y);
  f32x4 z[5];
  auto frag_off = [&](int i, int rb) {
    const int tl = 16 * i + n, q = tl / 20, rem = tl - 20 * q;
    return q * rb + (rem >> 2) * (g < 8 ? 4096 : 512) + (g < 8 ? g * 512 : 20480) + (w >> 1) * 256 + (rem & 3) * 64 + (w & 1) * 32 + kg * 8;
  };
#pragma unroll
  for (int i = 0; i < 5; ++i) {
    u32x2 xq = *(const u32x2*)(lds + L_A + frag_off(i, RB));
    f32x2 pr = *(const f32x2*)(lds + L_P1 + (16 * i + n) * 8);
    const f32x4 d = f32x4{lo16(xq.x), hi16(xq.x), lo16(xq.y), hi16(xq.y)} - pr.y;
    z[i] = d + d * pr.x * f32x4{dk0, dk1, dk2, dk3};
    asm volatile("" : "+v"(z[i]));
  }
  conv_finish(lds, lds + L_CV, ws, ch0, b2f(sd_h), w, lane, cacc, tA, tB);
  *(f32x4*)(lds + L_CV + tid * 16) = f32x4{sc.ar, sc.ai, sc.cr, sc.ci};
  *(f32x4*)(lds + L_CV + 4096 + tid * 16) = f32x4{sc.a20r, sc.a20i, sc.a40r, sc.a40i};
  *(f32x4*)(lds + L_CV + CVSZ + tid * 16) = f32x4{sc.aqr, sc.aqi, sc.bqr, sc.bqi};
  *(f32x2*)(lds + L_CV + CVSZ + 4096 + tid * 8) = f32x2{sc.a320r, sc.a320i};
  *(u32x2*)(lds + L_CV + CVSZ + 6144 + tid * 8) = dsk_h;
  STAMP(7);
  PHASE();
  twa = L_HI + w * 5376 + lane * 16; tra = L_HI + w * 5376 + (n & 3) * 1040 + (n >> 2) * 64;
  auto cltr = [&](int G) {
    if (G < 8) ltr<16, 32, 48>(lds, twa, tra + kg * 256, Cw[4 * (G & 1)][G >> 1], Cw[4 * (G & 1) + 1][G >> 1], Cw[4 * (G & 1) + 2][G >> 1], Cw[4 * (G & 1) + 3][G >> 1]);
    else ltr<256, 512, 768>(lds, twa, tra + kg * 16, Cw[8][0], Cw[8][1], Cw[8][2], Cw[8][3]);
  };
  {
    const u64 pre = poll_flags(p.ws, W_FLG + c * 128, lane);
    SB();
    cltr(0); cltr(1); cltr(2);
    SB();
    STAMP(24);
    wait_flags(p.ws, W_FLG + c * 128, tF, lane, budget, pre);
  }
  STAMP(8);
  u32 gq[5][2];
  {
    u32 off[5]; bool need[5]; u32x4 v[5];
    auto goff = [&]() {
#pragma unroll
      for (int r = 0; r < 5; ++r) {
        const int it = lane + 64 * r, ch = it / 20, tq = it - 20 * ch, q = 20 * c + tq;
        need[r] = true;
        off[r] = W_GT + (64 * g + 16 * w + ch) * 8192 + (q < 512 ? q : 511) * 16;
      }
    };
    gemm_y<0>(lds, lb, ws, W_H + c * 36 * 5120, Cw, w, g, lane, z, [&](int j) {
      if (j >= 10) cltr(j - 7);
      if (j >= 12) cltr(j - 5);
      if (j == -2) STAMP(22);
      if (j == -1) STAMP(29);
      if (j == 4) STAMP(27);
      if (j == 8) STAMP(31);
      if (j != 7) return;
      goff();
#pragma unroll
      for (int r = 0; r < 5; ++r) v[r] = ldc(ws, off[r]);
    });
    STAMP(9);
    PHASE();
    rs_part(lds, z, w, lane);
    bar();
    rs_pub(lds, ws, W_ST + (c * 9 + g) * 1280, tid, tA, tB);
    goff();
    gather<5, true>(ws, off, need, v, tA, tB, budget);
#pragma unroll
    for (int r = 0; r < 5; ++r) {
      const int it = lane + 64 * r, ch = it / 20, tq = it - 20 * ch;
      *(u32x2*)(lds + L_GI + (16 * w + ch) * 168 + tq * 8) = u32x2{v[r].x, v[r].z};
    }
    asm volatile("s_waitcnt lgkmcnt(0)" ::: "memory");
#pragma unroll
    for (int i = 0; i < 5; ++i) {
      u32x2 t = trread(lds, L_GI + (16 * w + 4 * kg + (n >> 2)) * 168 + 8 * (n & 3) + 32 * i);
      gq[i][0] = t.x; gq[i][1] = t.y;
    }
  }
  STAMP(10);
  u32x4 sv[3];
  rs_get<false>(lds, L_PAR, ws, W_ST + c * 9 * 1280, w, lane, sv, tA, tB, budget);
  bar();
  STAMP(11);
#pragma unroll
  for (int i = 0; i < 5; ++i) {
    f32x2 pr = *(const f32x2*)(lds + L_PAR + (16 * i + n) * 8);
    float gv[4] = {lo16(gq[i][0]), hi16(gq[i][0]), lo16(gq[i][1]), hi16(gq[i][1])};
#pragma unroll
    for (int e = 0; e < 4; ++e) { float t = z[i][e] - pr.y; z[i][e] = t + t * pr.x * gv[e]; }
    stc2(ws, W_U3 + c * 94208 + frag_off(i, 23552), u32x2{pk(z[i].x, z[i].y), pk(z[i].z, z[i].w)});
  }
  SB();
  rs_part(lds, z, w, lane);
  drain();
  bar();
  stc2(ws, tid == 0 ? W_FLG + (NC + c) * 128 + g * 8 : OOB, u32x2{tA, tB});
  rs_pub(lds, ws, W_ST + ((NC + c) * 9 + g) * 1280, tid, tA, tB);
  __builtin_amdgcn_s_sleep(5);
  wait_flags(p.ws, W_FLG + (NC + c) * 128, tF, lane, budget, tF + 1);
  STAMP(12);
  PHASE();
  const u32 ub = W_U3 + c * 94208 + w * 23552 + lane * 16;
  auto udma = [&](int j) {
    const int o = j < 20 ? (j % 5) * 4096 + (j / 5) * 1024 : j * 1024;
    dma_c(ws, ub, o, lb + L_A + w * RB + o);
  };
#pragma unroll
  for (int j = 0; j < 10; ++j) udma(j);
  asm volatile("s_waitcnt vmcnt(5)" ::: "memory");
  bar();
  STAMP(13);
#pragma unroll
  for (int i = 0; i < 5; ++i) asm volatile("" : "+v"(gq[i][0]), "+v"(gq[i][1]));
  gemm_bu<false>(lds, Bw, lane, acc, rsr, rsi, [&](int ks, int i) {
    if (ks == 12 && i < 3) sv[i] = ldc(ws, W_ST + (NC + c) * 9 * 1280 + rs_off(w, lane) + i * 1280);
    if (5 * ks + i < 13) udma(10 + 5 * ks + i);
    if (i == 4 && ks == 2) asm volatile("s_waitcnt vmcnt(13)\n\ts_barrier" ::: "memory");
    if (i == 4 && ks == 6) asm volatile("s_waitcnt vmcnt(8)\n\ts_barrier" ::: "memory");
    if (i == 4 && (ks == 10 || ks == 14)) asm volatile("s_waitcnt vmcnt(3)\n\ts_barrier" ::: "memory");
  });
  STAMP(25);
  PHASE();
  rs_get<true>(lds, L_P3, ws, W_ST + (NC + c) * 9 * 1280, w, lane, sv, tA, tB, budget);
  bar();
  {
    const f32x2 q = *(const f32x2*)(lds + L_RS + tid * 8);
    norm_acc(lds, L_P3, kg, acc, q.x, q.y);
  }
  {
    f32x4 a = *(const f32x4*)(lds + L_CV + tid * 16), b = *(const f32x4*)(lds + L_CV + 4096 + tid * 16);
    f32x4 e = *(const f32x4*)(lds + L_CV + CVSZ + tid * 16);
    f32x2 f = *(const f32x2*)(lds + L_CV + CVSZ + 4096 + tid * 8);
    sc.ar = a.x; sc.ai = a.y; sc.cr = a.z; sc.ci = a.w; sc.a20r = b.x; sc.a20i = b.y; sc.a40r = b.z; sc.a40i = b.w;
    sc.aqr = e.x; sc.aqi = e.y; sc.bqr = e.z; sc.bqi = e.w; sc.a320r = f.x; sc.a320i = f.y;
  }
  scan_a(sc, acc, ws, W_E + ((NC + c) * D + pch) * 16, lane, tA, tB, inr, ini);
  STAMP(14);
  SB();
  dsk_h = *(const u32x2*)(lds + L_CV + CVSZ + 6144 + tid * 8);
#pragma unroll
  for (int i = 0; i < 5; ++i) {
    f32x2 pr = *(const f32x2*)(lds + L_P3 + (16 * i + n) * 8);
    const f32x4 t = z[i] - pr.y;
    z[i] = t + t * pr.x * f32x4{lo16(dsk_h.x), hi16(dsk_h.x), lo16(dsk_h.y), hi16(dsk_h.y)};
#pragma unroll
    for (int r = 0; r < 4; ++r) {
      const float a = acc[i][0][r], b = acc[i][1][r];
      acc[i][0][r] = sc.cr * a - sc.ci * b; acc[i][1][r] = -(sc.cr * b) - sc.ci * a;
    }
    asm volatile("" : "+v"(acc[i][0]), "+v"(acc[i][1]), "+v"(z[i]));
  }
  SB();
  __builtin_amdgcn_s_sleep(5);
  scan_b<false>(sc, acc, lds, ws, W_E + (NC * D + pch) * 16, W_H + ((NC + c) * 36 + 4 * g + w) * 5120, c, w, lane, tA, tB, inr, ini, ev, budget);
  drain();
  bar();
  stc2(ws, tid == 0 ? W_FLG + (2 * NC + c) * 128 + g * 8 : OOB, u32x2{tA, tB});
  STAMP(15);
  __builtin_amdgcn_s_sleep(5);
  wait_flags(p.ws, W_FLG + (2 * NC + c) * 128, tF, lane, budget, tF + 1);
  STAMP(16);
  {
    gemm_y<0>(lds, lb, ws, W_H + (NC + c) * 36 * 5120, Cw, w, g, lane, z, [](int) {});
    rs_part(lds, z, w, lane);
  }
  bar();
  STAMP(17);
  rs_pub(lds, ws, W_ST + ((2 * NC + c) * 9 + g) * 1280, tid, tA, tB);
  f32x4 zc[5], gf[5];
#pragma unroll
  for (int i = 0; i < 5; ++i) {
    zc[i] = z[i] + (*(const float*)(lds + L_P1 + (16 * i + n) * 8 + 4) + *(const float*)(lds + L_PAR + (16 * i + n) * 8 + 4) +
                    *(const float*)(lds + L_P3 + (16 * i + n) * 8 + 4));
    gf[i] = f32x4{lo16(gq[i][0]), hi16(gq[i][0]), lo16(gq[i][1]), hi16(gq[i][1])};
    asm volatile("" : "+v"(zc[i]), "+v"(gf[i]));
  }
  rs_get<false>(lds, L_P2, ws, W_ST + (2 * NC + c) * 9 * 1280, w, lane, sv, tA, tB, budget);
  bar();
  STAMP(18);
  const rsrc_t ors = __builtin_amdgcn_make_buffer_rsrc(p.out, 0, S * D * 4, 0x00020000);
#pragma unroll
  for (int i = 0; i < 5; ++i) {
    f32x2 pr = *(const f32x2*)(lds + L_P2 + (16 * i + n) * 8);
    const f32x4 o = zc[i] + (z[i] - pr.y) * pr.x * gf[i];
    const int t = t0 + 16 * i + n;
    __builtin_amdgcn_raw_buffer_store_b128(__builtin_bit_cast(u32x4, o), ors, (t * D + 64 * g + 16 * w + 4 * kg) * 4, 0, 19);
  }
  if (c == NC - 1 && g == 0 && tid == 0) __hip_atomic_store((gu32*)p.ws, tA, __ATOMIC_RELAXED, __HIP_MEMORY_SCOPE_AGENT);
  STAMP(19);
  DUMP();
}

extern "C" void kernel_launch(void* const* d_in, const int* in_sizes, int n_in, void* d_out, int out_size, void* d_ws,
                              size_t ws_size, hipStream_t stream) {
  Params p;
  p.x = (const u16*)d_in[0]; p.lre = (const u16*)d_in[1]; p.lim = (const u16*)d_in[2];
  p.bre = (const u16*)d_in[3]; p.bim = (const u16*)d_in[4]; p.cre = (const u16*)d_in[5]; p.cim = (const u16*)d_in[6];
  p.ldt = (const u16*)d_in[7]; p.dsk = (const u16*)d_in[8]; p.k = (const u16*)d_in[10]; p.v = (const u16*)d_in[11];
  p.shk = (const u16*)d_in[12]; p.sk = (const u16*)d_in[13]; p.sd = (const u16*)d_in[14];
  p.out = (float*)d_out; p.ws = (unsigned char*)d_ws;
  (void)hipFuncSetAttribute((const void*)fused_kernel, hipFuncAttributeMaxDynamicSharedMemorySize, L_TOT);
  hipLaunchKernelGGL(fused_kernel, dim3(256), dim3(256), L_TOT, stream, p);
}
```

```cpp
#include <hip/hip_runtime.h>
#include <stdint.h>

typedef unsigned short u16;
typedef unsigned int u32;
typedef unsigned long long u64;
typedef __attribute__((ext_vector_type(4))) unsigned int u32x4;
typedef __attribute__((ext_vector_type(2))) unsigned int u32x2;
typedef __attribute__((ext_vector_type(4))) float f32x4;
typedef __attribute__((ext_vector_type(2))) float f32x2;
typedef __attribute__((ext_vector_type(8))) __bf16 bf16x8;
typedef __attribute__((ext_vector_type(2))) __bf16 bf16x2;
typedef __attribute__((ext_vector_type(4))) short s16x4;
typedef __attribute__((address_space(1))) u64 gu64;
typedef __attribute__((address_space(1))) u32 gu32;
typedef __attribute__((address_space(3))) s16x4 lds_s16x4;
typedef __amdgpu_buffer_rsrc_t rsrc_t;

#define DI __device__ __forceinline__
#define SB() __builtin_amdgcn_sched_barrier(0)

constexpr int S = 2048, D = 576, NC = 26;
constexpr u32 W_FLG = 4096;
constexpr u32 W_E = 32768;
constexpr u32 W_ST = W_E + 2 * NC * D * 16;
constexpr u32 W_KVT = 4u << 20;
constexpr u32 W_GT = W_KVT + D * 8192;
constexpr u32 W_H = W_GT + D * 8192;
constexpr u32 W_U3 = W_H + 2 * NC * 36 * 5120;
constexpr u32 W_END = W_U3 + NC * 94208;
constexpr u32 W_DBG = 64u << 20;
constexpr int L_A = 0;
constexpr int CVSZ = 13056, CV_K1 = 4224, CV_R = 8448;
constexpr int RB = 23568;
constexpr u32 OOB = 0x7ffffff0u;
constexpr int L_CV = 4 * RB;
constexpr int L_HI = L_CV + 2 * CVSZ;
constexpr int L_GI = L_HI + 4 * 5376;
constexpr int L_SP = L_GI + 64 * 168;
constexpr int L_PAR = L_SP + 2560;
constexpr int L_P3 = L_PAR + 640;
constexpr int L_P1 = L_PAR + 1280;
constexpr int L_P2 = L_P1 + 640;
constexpr int L_RS = L_P2 + 640;
constexpr int L_TOT = L_RS + 2048 + 1024;

struct Params {
  const u16 *x, *lre, *lim, *bre, *bim, *cre, *cim, *ldt, *dsk, *k, *v, *shk, *sk, *sd;
  float* out;
  unsigned char* ws;
};

DI float lo16(u32 w) { return __uint_as_float(w << 16); }
DI float hi16(u32 w) { return __uint_as_float(w & 0xffff0000u); }
DI float b2f(u16 h) { return __uint_as_float(((u32)h) << 16); }
DI u32 pk(float a, float b) {
  f32x2 v = {a, b};
  return __builtin_bit_cast(u32, __builtin_convertvector(v, bf16x2));
}
DI float rb(float a) { return lo16(pk(a, a)); }
DI f32x4 mma(u32x4 a, u32x4 b, f32x4 c) {
  return __builtin_amdgcn_mfma_f32_16x16x32_bf16(__builtin_bit_cast(bf16x8, a), __builtin_bit_cast(bf16x8, b), c, 0, 0, 0);
}
DI float dot2(u32 a, u32 b, float c) {
  return __builtin_amdgcn_fdot2_f32_bf16(__builtin_bit_cast(bf16x2, a), __builtin_bit_cast(bf16x2, b), c, false);
}
DI void bar() { asm volatile("s_waitcnt lgkmcnt(0)\n\ts_barrier" ::: "memory"); }
DI void drain() { asm volatile("s_waitcnt vmcnt(0)" ::: "memory"); }
DI void cfence() { asm volatile("" ::: "memory"); }
DI u32x4 ldc(rsrc_t r, u32 off) { return __builtin_amdgcn_raw_buffer_load_b128(r, off, 0, 16); }
DI u32x4 ldcs(rsrc_t r, u32 off, u32 soff) { return __builtin_amdgcn_raw_buffer_load_b128(r, off, soff, 16); }
DI void stc(rsrc_t r, u32 off, u32x4 v) { __builtin_amdgcn_raw_buffer_store_b128(v, r, off, 0, 16); }
DI void stc2(rsrc_t r, u32 off, u32x2 v) { __builtin_amdgcn_raw_buffer_store_b64(v, r, off, 0, 16); }
DI u32x4 ldg16(const u16* p) { return *(const u32x4*)p; }
DI u32x2 ldg8(const u16* p) { return *(const u32x2*)p; }
DI u32x2 trread(unsigned char* lds, int off) {
  s16x4 r = __builtin_amdgcn_ds_read_tr16_b64_v4i16((lds_s16x4*)(lds + off));
  return __builtin_bit_cast(u32x2, r);
}
DI void cmul(float ar, float ai, float br, float bi, float& cr, float& ci) {
  float r = ar * br - ai * bi, i = ar * bi + ai * br;
  cr = r; ci = i;
}
DI float red32(float a, float b) {
  auto r = __builtin_amdgcn_permlane32_swap(__float_as_uint(a), __float_as_uint(b), false, false);
  return __uint_as_float(r[0]) + __uint_as_float(r[1]);
}
DI float red16(float a, float b) {
  auto r = __builtin_amdgcn_permlane16_swap(__float_as_uint(a), __float_as_uint(b), false, false);
  return __uint_as_float(r[0]) + __uint_as_float(r[1]);
}

DI void pair16(float a, float& e, float& o) {
  auto r = __builtin_amdgcn_permlane16_swap(__float_as_uint(a), __float_as_uint(a), false, false);
  e = __uint_as_float(r[0]); o = __uint_as_float(r[1]);
}
DI void pair32(float a, float& e, float& o) {
  auto r = __builtin_amdgcn_permlane32_swap(__float_as_uint(a), __float_as_uint(a), false, false);
  e = __uint_as_float(r[0]); o = __uint_as_float(r[1]);
}
DI float sum3(float a, int lane) {
  return a + __int_as_float(__builtin_amdgcn_ds_bpermute(lane * 4 + 80, __float_as_int(a))) +
         __int_as_float(__builtin_amdgcn_ds_bpermute(lane * 4 + 160, __float_as_int(a)));
}
typedef __attribute__((address_space(3))) void lds_void;
typedef __attribute__((address_space(1))) const void gvoid;
template <int O1, int O2, int O3>
DI void ltr(unsigned char* lds, int wa, int ra, u32x4& r0, u32x4& r1, u32x4& r2, u32x4& r3) {
  *(u32x4*)(lds + wa) = r0; *(u32x4*)(lds + wa + 1040) = r1; *(u32x4*)(lds + wa + 2080) = r2; *(u32x4*)(lds + wa + 3120) = r3;
  cfence();
  r0 = *(const u32x4*)(lds + ra); r1 = *(const u32x4*)(lds + ra + O1);
  r2 = *(const u32x4*)(lds + ra + O2); r3 = *(const u32x4*)(lds + ra + O3);
  cfence();
}
template <int OFF>
DI void dma_g(const u16* base, u32 voff, u32 ldsa) {
  asm volatile("s_mov_b32 m0, %2\n\ts_nop 0\n\tglobal_load_lds_dwordx4 %0, %1 offset:%3" :: "v"(voff), "s"(base), "s"(ldsa - OFF), "n"(OFF) : "memory");
}
DI void dma_c(rsrc_t r, u32 voff, u32 soff, u32 ldsa) {
  asm volatile("s_mov_b32 m0, %3\n\ts_nop 0\n\tbuffer_load_dwordx4 %0, %1, %2 offen sc1 lds" :: "v"(voff), "s"(r), "s"(soff), "s"(ldsa) : "memory");
}
DI float quad_sum(float a) {
  a += __int_as_float(__builtin_amdgcn_update_dpp(0, __float_as_int(a), 0xb1, 0xf, 0xf, true));
  a += __int_as_float(__builtin_amdgcn_update_dpp(0, __float_as_int(a), 0x4e, 0xf, 0xf, true));
  return a;
}
constexpr unsigned SPIN_MAX = 1u << 20;
DI bool spin_next(unsigned& budget) {
  if (budget == 0) return false;
  --budget;
  __builtin_amdgcn_s_sleep(1);
  return true;
}
template <int N, bool PRE = false>
DI void gather(rsrc_t ws, const u32 (&off)[N], const bool (&need)[N], u32x4 (&v)[N], u32 tA, u32 tB, unsigned& budget) {
  if (PRE) {
    bool ok = true;
#pragma unroll
    for (int i = 0; i < N; ++i) ok &= (!need[i]) | ((v[i].y == tA) & (v[i].w == tB));
    if (__all(ok)) return;
  }
  for (;;) {
    cfence();
    bool ok = true;
#pragma unroll
    for (int i = 0; i < N; ++i) v[i] = ldc(ws, off[i]);
#pragma unroll
    for (int i = 0; i < N; ++i) ok &= (!need[i]) | ((v[i].y == tA) & (v[i].w == tB));
    if (__all(ok)) break;
    if (!spin_next(budget)) break;
  }
  cfence();
}
DI u64 poll_flags(unsigned char* wsp, u32 off, int lane) {
  return __hip_atomic_load((gu64*)(wsp + off) + (lane < 9 ? lane : 0), __ATOMIC_RELAXED, __HIP_MEMORY_SCOPE_AGENT);
}
DI void wait_flags(unsigned char* wsp, u32 off, u64 tag, int lane, unsigned& budget, u64 pre) {
  gu64* f = (gu64*)(wsp + off) + (lane < 9 ? lane : 0);
  if (!__all(pre == tag)) for (;;) {
    u64 x = __hip_atomic_load(f, __ATOMIC_RELAXED, __HIP_MEMORY_SCOPE_AGENT);
    if (__all(x == tag)) break;
    if (!spin_next(budget)) break;
  }
  __builtin_amdgcn_fence(__ATOMIC_ACQUIRE, "wavefront");
  cfence();
}

#define STAMP(i) do {} while (0)

template <int V> struct IC { static constexpr int v = V; constexpr operator int() const { return V; } };
template <int N, int K = 0, class F>
DI void static_for(F f) { if constexpr (K < N) { f(IC<K>{}); static_for<N, K + 1>(f); } }
constexpr int NPIN = 7;
template <int N>
DI void vmw() { asm volatile("s_waitcnt vmcnt(%0)" :: "n"(N) : "memory"); }
DI void conv_stage_k(unsigned char* cv, u32x4 kx, u32 knext, int tid) {
  *(u32x4*)(cv + 32 + 16 * tid) = kx;
  u32x4 s;
  s.x = __builtin_amdgcn_alignbit(kx.y, kx.x, 16);
  s.y = __builtin_amdgcn_alignbit(kx.z, kx.y, 16);
  s.z = __builtin_amdgcn_alignbit(kx.w, kx.z, 16);
  s.w = __builtin_amdgcn_alignbit(knext, kx.w, 16);
  *(u32x4*)(cv + CV_K1 + 32 + 16 * tid) = s;
  u32x4 z = {0, 0, 0, 0};
  if (tid == 0) {
    *(u32x4*)(cv) = z; *(u32x4*)(cv + 16) = z; *(u32x4*)(cv + CV_K1) = z;
    u32x4 f = {0, 0, 0, kx.x << 16};
    *(u32x4*)(cv + CV_K1 + 16) = f;
  }
  if (tid >= 64 && tid < 96) *(u32x4*)(cv + CV_R + 4096 + 16 * (tid - 64)) = z;
}
template <bool PRE>
DI void conv_gather(unsigned char* cv, rsrc_t ws, int ch0, int tid, u32x4 (&v)[4], u32 tA, u32 tB, unsigned& budget) {
  u32 off[4]; bool need[4];
#pragma unroll
  for (int r = 0; r < 4; ++r) { off[r] = W_KVT + ch0 * 8192 + (tid + 256 * r) * 16; need[r] = true; }
  gather<4, PRE>(ws, off, need, v, tA, tB, budget);
#pragma unroll
  for (int r = 0; r < 4; ++r) {
    u32x2 w = {v[r].x, v[r].z};
    *(u32x2*)(cv + (r >> 1) * CVSZ + CV_R + ((r & 1) * 256 + tid) * 8) = w;
  }
}
template <int R0, int R1, class F>
DI void conv_mma(unsigned char* lds, int cvo, int w, int lane, f32x4 (&acc)[8], F hook) {
  const int m = lane & 15, kg = lane >> 4;
  const int cb = cvo + (w >> 1) * CVSZ + (w & 1) * 256;
  int ka[4];
  ka[0] = cb + ((m & 1) ? 0 : CV_K1) + 16 * kg + 4 * ((m + 1) >> 1);
  ka[1] = ka[0] + 1024; ka[2] = ka[0] + 2048; ka[3] = ka[0] + 3072;
  int rbp = cb + CV_R + 480 - 32 * m + 16 * kg;
  asm volatile("" : "+v"(ka[0]), "+v"(ka[1]), "+v"(ka[2]), "+v"(ka[3]), "+v"(rbp));
  u32x4 A[8], B[8];
  auto ldA = [&](int r, int a) {
    const u32* q = (const u32*)(lds + ka[a >> 1] + 64 * r + 512 * (a & 1));
    A[a].x = q[0]; A[a].y = q[1]; A[a].z = q[2]; A[a].w = q[3];
  };
  auto ldB = [&](int r, int b) { B[b] = *(const u32x4*)(lds + rbp + 64 * r + 512 * (7 - b)); };
  ldB(R0, 0);
#pragma unroll
  for (int a = 7; a >= 0; --a) ldA(R0, a);
#pragma unroll
  for (int b = 1; b < 8; ++b) ldB(R0, b);
  SB();
#pragma unroll
  for (int r = R0; r < R1; ++r) {
#pragma unroll
    for (int b = 0; b < 8; ++b) {
#pragma unroll
      for (int a = 7 - b; a >= 0; --a) acc[a + b] = mma(A[a], B[b], acc[a + b]);
      SB();
      if (r + 1 < R1) { ldB(r + 1, b); ldA(r + 1, 7 - b); }
      hook(r, b);
      SB();
    }
  }
}
DI void conv_finish(unsigned char* lds, unsigned char* cv, rsrc_t ws, int ch0, float sdv, int w, int lane,
                    f32x4 (&acc)[8], u32 tA, u32 tB) {
  const int n = lane & 15, kg = lane >> 4, odd = w & 1;
  unsigned char* box = lds + L_HI + (w ^ 1) * 5376;
#pragma unroll
  for (int j = 0; j < 4; ++j) {
    f32x4 lo = acc[j], hi = acc[4 + j], snd, kp;
#pragma unroll
    for (int e = 0; e < 4; ++e) { snd[e] = odd ? lo[e] : hi[e]; kp[e] = odd ? hi[e] : lo[e]; }
    *(f32x4*)(box + j * 1024 + lane * 16) = snd;
    acc[j] = kp;
  }
  bar();
  const unsigned char* mine = lds + L_HI + w * 5376;
  const unsigned char* cb = cv + (w >> 1) * CVSZ + CV_R;
#pragma unroll
  for (int j = 0; j < 4; ++j) {
    f32x4 y = acc[j] + *(const f32x4*)(mine + j * 1024 + lane * 16);
    const int X = 256 * (4 * odd + j) + 16 * n + 4 * kg;
    u32x2 rv = *(const u32x2*)(cb + (2044 - X) * 2);
    u32 y01 = pk(y.x, y.y), y23 = pk(y.z, y.w);
    u32 s01 = pk(hi16(rv.y) * sdv, lo16(rv.y) * sdv), s23 = pk(hi16(rv.x) * sdv, lo16(rv.x) * sdv);
    u32x4 o;
    o.x = pk(lo16(y01) + lo16(s01), hi16(y01) + hi16(s01)); o.y = tA;
    o.z = pk(lo16(y23) + lo16(s23), hi16(y23) + hi16(s23)); o.w = tB;
    stc(ws, W_GT + (ch0 + (w >> 1)) * 8192 + X * 4, o);
  }
}

struct Scan {
  float ar, ai, cr, ci, a20r, a20i, a40r, a40i, aqr, aqi, bqr, bqi, a320r, a320i;
};

template <bool RS, class F>
DI void gemm_bu(const unsigned char* lds, u32x4 (&Bw)[18][2], int lane, f32x4 (&acc)[5][2], float& rsr, float& rsi, F pre) {
#pragma unroll
  for (int i = 0; i < 5; ++i) { acc[i][0] = f32x4{0, 0, 0, 0}; acc[i][1] = f32x4{0, 0, 0, 0}; }
  int ab0, ab1;
  {
    const int n = lane & 15, kg = lane >> 4;
    ab0 = L_A + (n >> 2) * RB + kg * 256 + (n & 3) * 64;
    ab1 = L_A + (n >> 2) * RB + 20480 + (kg & 1) * 256 + (n & 3) * 64 + (kg >> 1) * 16;
  }
  asm volatile("" : "+v"(ab0), "+v"(ab1));
  auto rdA = [&](int ks, int i) {
    return *(const u32x4*)(lds + (ks < 16 ? ab0 + i * 4096 + (ks >> 2) * 1024 + (ks & 3) * 16 : ab1 + i * 512 + (ks & 1) * 32));
  };
  f32x4 r0 = {0, 0, 0, 0}, r1 = {0, 0, 0, 0};
  const u32x4 ones = {0x3f803f80u, 0x3f803f80u, 0x3f803f80u, 0x3f803f80u};
  u32x4 a[2][5];
#pragma unroll
  for (int i = 0; i < 5; ++i) a[0][i] = rdA(0, i);
  SB();
#pragma unroll
  for (int ks = 0; ks < 18; ++ks) {
    asm("" : "+a"(Bw[ks][0]), "+a"(Bw[ks][1]));
#pragma unroll
    for (int i = 0; i < 5; ++i) {
      if (ks + 1 < 18) a[(ks + 1) & 1][i] = rdA(ks + 1, i);
      acc[i][0] = mma(a[ks & 1][i], Bw[ks][0], acc[i][0]);
      acc[i][1] = mma(a[ks & 1][i], Bw[ks][1], acc[i][1]);
      pre(ks, i);
      SB();
    }
    if (RS) { r0 = mma(ones, Bw[ks][0], r0); r1 = mma(ones, Bw[ks][1], r1); SB(); }
  }
  if (RS) { rsr = r0.x; rsi = r1.x; }
}
DI void scan_a(const Scan& sc, const f32x4 (&acc)[5][2], rsrc_t ws, u32 eoff, int lane, u32 tA, u32 tB, float& inr, float& ini) {
  float er = 0.f, ei = 0.f;
#pragma unroll
  for (int i = 0; i < 5; ++i)
#pragma unroll
    for (int r = 0; r < 4; ++r) {
      float nr = sc.ar * er + (acc[i][0][r] - sc.ai * ei), ni = sc.ar * ei + (acc[i][1][r] + sc.ai * er);
      er = nr; ei = ni;
    }
  const int q = lane >> 4;
  float e0r, e1r, e0i, e1i, pr, pi, p0r, p1r, p0i, p1i, tr, ti;
  pair16(er, e0r, e1r); pair16(ei, e0i, e1i);
  cmul(sc.a20r, sc.a20i, e0r, e0i, pr, pi);
  pr += e1r; pi += e1i;
  pair32(pr, p0r, p1r); pair32(pi, p0i, p1i);
  cmul(sc.a40r, sc.a40i, p0r, p0i, tr, ti);
  stc(ws, q == 0 ? eoff : OOB, u32x4{__float_as_uint(tr + p1r), tA, __float_as_uint(ti + p1i), tB});
  cmul(sc.a20r, sc.a20i, p0r, p0i, tr, ti);
  inr = q == 0 ? 0.f : q == 1 ? e0r : q == 2 ? p0r : tr + e0r;
  ini = q == 0 ? 0.f : q == 1 ? e0i : q == 2 ? p0i : ti + e0i;
}
template <bool PRE>
DI void scan_b(const Scan& sc, const f32x4 (&acc)[5][2], unsigned char* lds, rsrc_t ws, u32 ebase, u32 hoff, int c, int w,
               int lane, u32 tA, u32 tB, float inr, float ini, u32x4 (&v)[7], unsigned& budget) {
  const int n = lane & 15, q = lane >> 4;
  u32 off[7]; bool need[7];
#pragma unroll
  for (int j = 0; j < 7; ++j) {
    int cp = c - 1 - q - 4 * j;
    need[j] = cp >= 0;
    off[j] = ebase + (cp >= 0 ? cp : 0) * (D * 16);
  }
  gather<7, PRE>(ws, off, need, v, tA, tB, budget);
  float hr = 0.f, hi = 0.f;
#pragma unroll
  for (int j = 6; j >= 0; --j) {
    cmul(sc.a320r, sc.a320i, hr, hi, hr, hi);
    hr += need[j] ? __uint_as_float(v[j].x) : 0.f;
    hi += need[j] ? __uint_as_float(v[j].z) : 0.f;
  }
  cmul(sc.bqr, sc.bqi, hr, hi, hr, hi);
  hr = red16(hr, hr); hi = red16(hi, hi);
  hr = red32(hr, hr); hi = red32(hi, hi);
  cmul(sc.aqr, sc.aqi, hr, hi, hr, hi);
  hr += inr; hi += ini;
  if (!PRE) { cmul(sc.cr, sc.ci, hr, hi, hr, hi); hi = -hi; }
  unsigned char* img = lds + L_HI + w * 5376;
#pragma unroll
  for (int i = 0; i < 5; ++i) {
    float o_r[4], o_i[4];
#pragma unroll
    for (int r = 0; r < 4; ++r) {
      if (PRE) {
        float nr = sc.ar * hr + (acc[i][0][r] - sc.ai * hi), ni = sc.ar * hi + (acc[i][1][r] + sc.ai * hr);
        hr = nr; hi = ni;
        o_r[r] = sc.cr * hr - sc.ci * hi;
        o_i[r] = -(sc.cr * hi) - sc.ci * hr;
      } else {
        float nr = sc.ar * hr + (acc[i][0][r] + sc.ai * hi), ni = sc.ar * hi + (acc[i][1][r] - sc.ai * hr);
        o_r[r] = hr = nr; o_i[r] = hi = ni;
      }
    }
    *(u32x2*)(img + n * 168 + 40 * q + 8 * i) = u32x2{pk(o_r[0], o_r[1]), pk(o_r[2], o_r[3])};
    *(u32x2*)(img + (16 + n) * 168 + 40 * q + 8 * i) = u32x2{pk(o_i[0], o_i[1]), pk(o_i[2], o_i[3])};
  }
  asm volatile("s_waitcnt lgkmcnt(0)" ::: "memory");
  const int tro = L_HI + w * 5376 + (8 * q + (n >> 2)) * 168 + 8 * (n & 3);
#pragma unroll
  for (int i = 0; i < 5; ++i) {
    u32x2 a = trread(lds, tro + 32 * i), b = trread(lds, tro + 4 * 168 + 32 * i);
    stc(ws, hoff + i * 1024 + lane * 16, u32x4{a.x, a.y, b.x, b.y});
  }
}
DI void dma_c4(rsrc_t r, u32 voff, u32 soff, u32 mb) {
  asm volatile("s_mov_b32 m0, %3\n\ts_nop 0\n\tbuffer_load_dwordx4 %0, %1, %2 offen sc1 lds\n\t"
               "buffer_load_dwordx4 %0, %1, %2 offen offset:1024 sc1 lds\n\tbuffer_load_dwordx4 %0, %1, %2 offen offset:2048 sc1 lds\n\t"
               "buffer_load_dwordx4 %0, %1, %2 offen offset:3072 sc1 lds" :: "v"(voff), "s"(r), "s"(soff), "s"(mb) : "memory");
}
template <int NX, class F>
DI void gemm_y(unsigned char* lds, u32 lb, rsrc_t ws, u32 hbase, u32x4 (&Cw)[9][4], int w, int g, int lane, f32x4 (&y)[5], F mid) {
  f32x4 acc[4][5];
#pragma unroll
  for (int d = 0; d < 4; ++d)
#pragma unroll
    for (int i = 0; i < 5; ++i) acc[d][i] = d ? f32x4{0, 0, 0, 0} : y[i];
  u32 ho[3];
  {
    const int n = lane & 15, kg = lane >> 4;
    const u32 hb = hbase + (w >> 1) * 18 * 5120 + (w & 1) * 512 + n * 16;
    ho[0] = hb + ((kg + g) % 9) * 10240;
    ho[1] = hb + ((4 + kg + g) % 9) * 10240;
    ho[2] = hb + ((8 + g) % 9) * 10240 + (kg >> 1) * 5120 + (kg & 1) * 256;
  }
  const u32 sa = lb + L_A + w * RB, sh = lb + L_HI + w * 5376;
  auto dmaH = [&](int j) {
    const u32 so = ((j >> 1) & 1) * 5120 + (j & 1) * 256, mb = j == 4 ? sh : sa + (j % 5) * 5120;
    dma_c4(ws, ho[j >> 2], so, mb); dma_c(ws, ho[j >> 2], so + 4096, mb + 4096);
  };
  int ra = L_A + w * RB + lane * 16, rh = L_HI + w * 5376 + lane * 16;
  asm volatile("" : "+v"(ra), "+v"(rh));
  auto rdH = [&](int j, int i) { return *(const u32x4*)(lds + (j == 4 ? rh : ra + (j % 5) * 5120) + i * 1024); };
#pragma unroll
  for (int j = 0; j < 4; ++j) { dmaH(j); mid(10 + j); }
  u32x4 hf[2][5];
  mid(-2);
  vmw<15>();
  mid(-1);
#pragma unroll
  for (int i = 0; i < 5; ++i) hf[0][i] = rdH(0, i);
  SB();
  static_for<45>([&](auto I) {
    constexpr int j = I / 5, i = I % 5;
    if (i == 0 && j < NPIN) asm("" : "+a"(Cw[j][0]));
    if (j < 8) {
      vmw<(j < 5 ? 14 : j < 8 ? 39 - I + NX : 0)>();
      hf[(j + 1) & 1][i] = rdH(j + 1, i);
    }
    acc[3][i] = mma(Cw[j][3], hf[j & 1][i], acc[3][i]);
    acc[2][i] = mma(Cw[j][2], hf[j & 1][i], acc[2][i]);
    SB();
    if constexpr (j < 5) {
      constexpr int J = j + 4;
      dma_c(ws, ho[J >> 2], ((J >> 1) & 1) * 5120 + (J & 1) * 256 + i * 1024, (J == 4 ? sh : sa + (J % 5) * 5120) + i * 1024);
    }
    if (j == 8 && i > 0) {
#pragma unroll
      for (int d = 1; d < 4; ++d) *(f32x4*)(lds + ra + ((d - 1) * 5 + i - 1) * 1024) = acc[d][i > 0 ? i - 1 : 0];
    }
    SB();
    acc[1][i] = mma(Cw[j][1], hf[j & 1][i], acc[1][i]);
    acc[0][i] = mma(Cw[j][0], hf[j & 1][i], acc[0][i]);
    SB();
    if (i == 4) mid(j);
    SB();
  });
#pragma unroll
  for (int d = 1; d < 4; ++d) *(f32x4*)(lds + ra + ((d - 1) * 5 + 4) * 1024) = acc[d][4];
  bar();
  const unsigned char* b1 = lds + L_A + ((w + 3) & 3) * RB + lane * 16;
  const unsigned char* b2 = lds + L_A + ((w + 2) & 3) * RB + 5120 + lane * 16;
  const unsigned char* b3 = lds + L_A + ((w + 1) & 3) * RB + 10240 + lane * 16;
#pragma unroll
  for (int i = 0; i < 5; ++i)
    y[i] = (acc[0][i] + *(const f32x4*)(b1 + i * 1024)) + (*(const f32x4*)(b2 + i * 1024) + *(const f32x4*)(b3 + i * 1024));
}
DI void rs_part(unsigned char* lds, const f32x4 (&z)[5], int w, int lane) {
  const int n = lane & 15, kg = lane >> 4;
#pragma unroll
  for (int i = 0; i < 5; ++i) {
    float s0 = (z[i].x + z[i].y) + (z[i].z + z[i].w);
    float s1 = (z[i].x * z[i].x + z[i].y * z[i].y) + (z[i].z * z[i].z + z[i].w * z[i].w);
    float s = red32(s0, s1);
    *(float*)(lds + L_SP + (w * 80 + 16 * i + n) * 8 + (kg >> 1) * 4) = red16(s, s);
  }
}
DI void rs_pub(unsigned char* lds, rsrc_t ws, u32 base, int tid, u32 tA, u32 tB) {
  const int r = tid < 80 ? tid : 0;
  f32x2 a = *(const f32x2*)(lds + L_SP + r * 8), b = *(const f32x2*)(lds + L_SP + (80 + r) * 8);
  f32x2 e = *(const f32x2*)(lds + L_SP + (160 + r) * 8), f = *(const f32x2*)(lds + L_SP + (240 + r) * 8);
  a = (a + b) + (e + f);
  stc(ws, tid < 80 ? base + tid * 16 : OOB, u32x4{__float_as_uint(a.x), tA, __float_as_uint(a.y), tB});
}
DI u32 rs_off(int w, int lane) {
  const int l3 = lane < 60 ? lane : 0, th = l3 / 20;
  return (240 * th + 20 * w + l3 - 20 * th) * 16;
}
template <bool PRE>
DI void rs_get(unsigned char* lds, int lout, rsrc_t ws, u32 base, int w, int lane, u32x4 (&v)[3], u32 tA, u32 tB, unsigned& budget) {
  u32 off[3]; bool need[3];
#pragma unroll
  for (int j = 0; j < 3; ++j) { off[j] = base + rs_off(w, lane) + j * 1280; need[j] = true; }
  gather<3, PRE>(ws, off, need, v, tA, tB, budget);
  float s0 = __uint_as_float(v[0].x) + __uint_as_float(v[1].x) + __uint_as_float(v[2].x);
  float s1 = __uint_as_float(v[0].z) + __uint_as_float(v[1].z) + __uint_as_float(v[2].z);
  s0 = sum3(s0, lane) * (1.f / D);
  s1 = sum3(s1, lane) * (1.f / D);
  if (lane < 20) *(f32x2*)(lds + lout + (20 * w + lane) * 8) = f32x2{__builtin_amdgcn_rsqf(fmaxf(s1 - s0 * s0, 0.f) + 1e-5f), s0};
}
DI void norm_acc(const unsigned char* lds, int lp, int kg, f32x4 (&acc)[5][2], float rsr, float rsi) {
#pragma unroll
  for (int i = 0; i < 5; ++i) {
    f32x4 pa = *(const f32x4*)(lds + lp + (20 * kg + 4 * i) * 8), pb = *(const f32x4*)(lds + lp + (20 * kg + 4 * i) * 8 + 16);
    acc[i][0].x = (acc[i][0].x - pa.y * rsr) * pa.x; acc[i][1].x = (acc[i][1].x - pa.y * rsi) * pa.x;
    acc[i][0].y = (acc[i][0].y - pa.w * rsr) * pa.z; acc[i][1].y = (acc[i][1].y - pa.w * rsi) * pa.z;
    acc[i][0].z = (acc[i][0].z - pb.y * rsr) * pb.x; acc[i][1].z = (acc[i][1].z - pb.y * rsi) * pb.x;
    acc[i][0].w = (acc[i][0].w - pb.w * rsr) * pb.z; acc[i][1].w = (acc[i][1].w - pb.w * rsi) * pb.z;
  }
}
__global__ void __launch_bounds__(256, 1) fused_kernel(Params p) {
  extern __shared__ __attribute__((aligned(16))) unsigned char lds[];
  int tid = threadIdx.x, lane = tid & 63;
  const int w = __builtin_amdgcn_readfirstlane(tid >> 6);
  const int xcd = blockIdx.x & 7, slot = blockIdx.x >> 3;
  int c, g, spare = -1;
  if (slot < 27) { c = xcd * 3 + slot / 9; g = slot % 9; }
  else {
    int e = (xcd & 3) * 5 + (slot - 27);
    if (xcd < 4) { int e2 = (xcd & 1) * 5 + (slot - 27); c = 24 + (xcd >> 1); g = e2; if (e2 >= 9) spare = 20 + (xcd >> 1); }
    else { spare = e; c = 0; g = e >> 1; }
  }
#define DUMP() do {} while (0)
  STAMP(0);
  {
    u64 pc; u32 len;
    asm volatile("s_getpc_b64 %0\n.Lpf%=:\n\ts_mov_b32 %1, .Lkend_mark-.Lpf%=" : "=s"(pc), "=s"(len));
    const u32 o = (((slot & 15) * 4 + w) * 64 + lane) * 16;
    asm volatile("s_mov_b32 m0, %2\n\ts_nop 0\n\tglobal_load_lds_dwordx4 %0, %1" :: "v"((int)o < (int)len ? o : 0u), "s"(pc & ~15ull),
                 "s"((u32)(size_t)(lds_void*)lds + L_TOT - 1024) : "memory");
  }
  const rsrc_t ws = __builtin_amdgcn_make_buffer_rsrc(p.ws, 0, W_END, 0x00020000);
  u32 ep = *(const __attribute__((address_space(4))) u32*)p.ws;
#define TAGS() asm volatile("" : "+s"(ep)); const u32 tA = ep + 1u; \
  const u32 tB = tA * 0x9E3779B1u + 0x7F4A7C15u
  unsigned budget = SPIN_MAX;
  int n = lane & 15, kg = lane >> 4;
#define PHASE() do { asm volatile("" : "+v"(tid)); lane = tid & 63; n = lane & 15; kg = lane >> 4; pch = 64 * g + 16 * w + n; } while (0)

  if (spare >= 0) {
    auto warm = [&]() {
      constexpr u32 NA = (W_ST + 3 * NC * 9 * 1280) / 128;
#pragma unroll
      for (int r = 0; r < 11; ++r) {
        const u32 L = (r * 4 + spare - 18) * 256 + tid, o = L < NA ? L * 128 : OOB;
        asm volatile("s_mov_b32 m0, %2\n\ts_nop 0\n\tbuffer_load_dword %0, %1, 0 offen sc1 lds"
                     :: "v"(o), "s"(ws), "s"((u32)(size_t)(lds_void*)lds + L_TOT - 256) : "memory");
      }
    };
    if (spare >= 18) { warm(); drain(); return; }
    const int ch0 = 64 * g + 52 + 6 * (spare & 1);
#pragma unroll
    for (int i = 0; i < 6; ++i) {
      const u16* kp = p.sk + (ch0 + i) * S + 8 * tid;
      u32x4 kx = ldg16(kp);
      u32 kn = tid < 255 ? kp[8] : 0;
      conv_stage_k(lds + i * CVSZ, kx, kn, tid);
    }
    u32 sd0 = p.sd[ch0 + (w >> 1)], sd1 = p.sd[ch0 + 2 + (w >> 1)], sd2 = p.sd[ch0 + 4 + (w >> 1)];
    {
      const u16* cb = spare < 9 ? p.cre : p.cim;
      const u32 o = (((spare < 9 ? spare : spare - 9) * 4 + w) * 18 * 64 + lane) * 16;
#pragma unroll 1
      for (int r = 0; r < 18; ++r) dma_g<0>(cb, o + r * 1024, (u32)(size_t)(lds_void*)lds + L_TOT - 1024);
    }
    TAGS();
    u32x4 kq[3][4];
    conv_gather<false>(lds, ws, ch0, tid, kq[0], tA, tB, budget);
#pragma unroll
    for (int r = 4; r < 12; ++r) kq[r >> 2][r & 3] = ldc(ws, W_KVT + ch0 * 8192 + (tid + 256 * r) * 16);
    conv_gather<true>(lds + 2 * CVSZ, ws, ch0 + 2, tid, kq[1], tA, tB, budget);
    conv_gather<true>(lds + 4 * CVSZ, ws, ch0 + 4, tid, kq[2], tA, tB, budget);
#pragma unroll 1
    for (int r = 0; r < 3; ++r) {
      const float sdv = lo16(r == 0 ? sd0 : r == 1 ? sd1 : sd2);
      bar();
      f32x4 acc[8];
#pragma unroll
      for (int j = 0; j < 8; ++j) acc[j] = f32x4{0, 0, 0, 0};
      conv_mma<0, 4>(lds, 2 * r * CVSZ, w, lane, acc, [](int, int) {});
      conv_finish(lds, lds + 2 * r * CVSZ, ws, ch0 + 2 * r, sdv, w, lane, acc, tA, tB);
    }
    asm volatile(".Lkend_mark:");
    STAMP(15);
    DUMP();
    return;
  }

  const int t0 = 80 * c;
  const u32 lb = (u32)(size_t)(lds_void*)lds;
  u32 xo[8];
  {
    const int r = (lane >> 2) & 3, jj = lane & 3;
#pragma unroll
    for (int i = 0; i < 5; ++i) {
      int t = t0 + 20 * w + 4 * i + r; t = t > S - 1 ? S - 1 : t;
      xo[i] = (t * D + 32 * kg + 8 * jj) * 2;
    }
#pragma unroll
    for (int u = 0; u < 3; ++u) {
      int t = t0 + 20 * w + 8 * u + 4 * (kg >> 1) + r; t = t > S - 1 ? S - 1 : t;
      xo[5 + u] = (t * D + 512 + 32 * (kg & 1) + 8 * jj) * 2;
    }
  }
  auto xdma = [&](int pc) {
    const u32 la = lb + L_A + w * RB + (pc >> 1) * 4096;
    if (pc & 1) { dma_g<512>(p.x, xo[pc >> 1], la + 2048); dma_g<768>(p.x, xo[pc >> 1], la + 3072); }
    else { dma_g<0>(p.x, xo[pc >> 1], la); dma_g<256>(p.x, xo[pc >> 1], la + 1024); }
    if (pc < 3) dma_g<0>(p.x, xo[5 + pc], lb + L_A + w * RB + 20480 + pc * 1024);
  };
  STAMP(26);
  u32x2 kk[8], vv[5];
  u32x4 shw[2];
  {
    const int col = 64 * g + 4 * (tid & 15), r0 = t0 + 5 * (tid >> 4);
#pragma unroll
    for (int j = 0; j < 8; ++j) { int t = r0 + j - 3; t = t < 0 ? 0 : (t > S - 1 ? S - 1 : t); kk[j] = ldg8(p.k + t * D + col); }
#pragma unroll
    for (int j = 0; j < 5; ++j) { int t = r0 + j; t = t > S - 1 ? S - 1 : t; vv[j] = ldg8(p.v + t * D + col); }
    shw[0] = ldg16(p.shk + col * 4); shw[1] = ldg16(p.shk + col * 4 + 8);
  }
  const int ch0 = 64 * g + 2 * c;
  u32x4 kx0 = ldg16(p.sk + ch0 * S + 8 * tid), kx1 = ldg16(p.sk + (ch0 + 1) * S + 8 * tid);
  u32 kn0 = p.sk[ch0 * S + 8 * tid + (tid < 255 ? 8 : 0)], kn1 = p.sk[(ch0 + 1) * S + 8 * tid + (tid < 255 ? 8 : 0)];
  int pch = 64 * g + 16 * w + n;
  u32 lre_h = p.lre[pch], lim_h = p.lim[pch], ldt_h = p.ldt[pch];
  __builtin_amdgcn_sched_barrier(0);
  u32x4 Bw[18][2];
  const int bo = (64 * g + 16 * w + (n & 12)) * D + 8 * (lane & 3);
  const u16 *br = p.bre + bo + 32 * kg, *bi = p.bim + bo + 32 * kg;
  const u16* bt = (kg < 2 ? p.bre : p.bim) + bo + 512 + 32 * (kg & 1);
  auto bld = [&](int G) {
    SB();
#pragma unroll
    for (int j = 0; j < 4; ++j) {
      if (G < 8) Bw[4 * (G >> 1) + j][G & 1] = ldg16(((G & 1) ? bi : br) + j * D + 128 * (G >> 1));
      else Bw[16 + (j >> 1)][j & 1] = ldg16(bt + j * D);
    }
    SB();
  };
#pragma unroll
  for (int G = 0; G < 5; ++G) bld(G);
  int twa = L_HI + w * 5376 + lane * 16, tra = L_HI + w * 5376 + (n & 3) * 1040 + (n >> 2) * 64;
  auto bltr = [&](int G) {
    const int i = G >> 1, m = G & 1;
    if (G < 8) ltr<16, 32, 48>(lds, twa, tra + kg * 256, Bw[4 * i][m], Bw[4 * i + 1][m], Bw[4 * i + 2][m], Bw[4 * i + 3][m]);
    else ltr<512, 32, 544>(lds, twa, tra + (kg & 1) * 256 + (kg >> 1) * 16, Bw[16][0], Bw[16][1], Bw[17][0], Bw[17][1]);
  };
  STAMP(20);
  {
    const int r0 = 5 * (tid >> 4);
    const bool first = (t0 + r0 == 0);
    asm volatile("" : "+v"(kk[0]), "+v"(kk[1]), "+v"(kk[2]), "+v"(kk[3]), "+v"(kk[4]), "+v"(kk[5]), "+v"(kk[6]), "+v"(kk[7]),
                 "+v"(vv[0]), "+v"(vv[1]), "+v"(vv[2]), "+v"(vv[3]), "+v"(vv[4]), "+v"(shw[0]), "+v"(shw[1]));
    asm volatile("" : "+v"(kx0), "+v"(kx1), "+v"(kn0), "+v"(kn1), "+v"(lre_h), "+v"(lim_h), "+v"(ldt_h));
    u16* img = (u16*)(lds + L_GI) + 4 * (tid & 15) * 84 + r0;
#pragma unroll
    for (int h = 0; h < 2; ++h) {
      f32x2 sw[4], kf[8];
      sw[0] = f32x2{lo16(shw[h].x), lo16(shw[h].z)}; sw[1] = f32x2{hi16(shw[h].x), hi16(shw[h].z)};
      sw[2] = f32x2{lo16(shw[h].y), lo16(shw[h].w)}; sw[3] = f32x2{hi16(shw[h].y), hi16(shw[h].w)};
#pragma unroll
      for (int j = 0; j < 8; ++j) {
        u32 q = kk[j][h];
        if (j < 3) q = first ? 0u : q;
        kf[j] = f32x2{lo16(q), hi16(q)};
      }
#pragma unroll
      for (int r = 0; r < 5; ++r) {
        SB();
        if (h == 0) bltr(r);
        else { xdma(2 * r); xdma(2 * r + 1); }
        SB();
        f32x2 a = kf[r] * sw[3];
#pragma unroll
        for (int j = 1; j < 4; ++j) a = __builtin_elementwise_fma(kf[r + j], sw[3 - j], a);
        a *= f32x2{lo16(vv[r][h]), hi16(vv[r][h])};
        const u32 o = pk(a.x, a.y);
        img[(2 * h) * 84 + r] = (u16)o;
        img[(2 * h + 1) * 84 + r] = (u16)(o >> 16);
      }
    }
  }
  STAMP(21);
  bld(5);
  conv_stage_k(lds + L_CV, kx0, tid < 255 ? kn0 : 0, tid);
  conv_stage_k(lds + L_CV + CVSZ, kx1, tid < 255 ? kn1 : 0, tid);
  STAMP(30);
  bld(6);
  Scan sc;
  {
    float lr = lo16(lre_h), li = lo16(lim_h), dt = rb(expf(lo16(ldt_h)));
    float e = expf(dt * lr), sn, cs;
    sincosf(dt * li, &sn, &cs);
    bld(7);
    sc.ar = e * cs; sc.ai = e * sn;
    float nr = sc.ar - 1.f, ni = sc.ai, dn = lr * lr + li * li;
    sc.cr = 2.f * ((nr * lr + ni * li) / dn); sc.ci = 2.f * ((ni * lr - nr * li) / dn);
    bld(8);
    float r2, i2, r4, i4, r5, i5, r80, i80, r160, i160, r60, i60, r240, i240;
    cmul(sc.ar, sc.ai, sc.ar, sc.ai, r2, i2); cmul(r2, i2, r2, i2, r4, i4); cmul(r4, i4, sc.ar, sc.ai, r5, i5);
    cmul(r5, i5, r5, i5, r2, i2); cmul(r2, i2, r2, i2, sc.a20r, sc.a20i);
    cmul(sc.a20r, sc.a20i, sc.a20r, sc.a20i, sc.a40r, sc.a40i);
    cmul(sc.a40r, sc.a40i, sc.a20r, sc.a20i, r60, i60);
    cmul(sc.a40r, sc.a40i, sc.a40r, sc.a40i, r80, i80);
    cmul(r80, i80, r80, i80, r160, i160); cmul(r160, i160, r80, i80, r240, i240);
    cmul(r160, i160, r160, i160, sc.a320r, sc.a320i);
    sc.aqr = kg == 0 ? 1.f : kg == 1 ? sc.a20r : kg == 2 ? sc.a40r : r60;
    sc.aqi = kg == 0 ? 0.f : kg == 1 ? sc.a20i : kg == 2 ? sc.a40i : i60;
    sc.bqr = kg == 0 ? 1.f : kg == 1 ? r80 : kg == 2 ? r160 : r240;
    sc.bqi = kg == 0 ? 0.f : kg == 1 ? i80 : kg == 2 ? i160 : i240;
  }
  STAMP(28);
  asm volatile("s_waitcnt vmcnt(16)" ::: "memory");
  {
    const u32 ones = 0x3f803f80u;
#pragma unroll
    for (int i = 0; i < 5; ++i) {
      u32x4 xv[5];
#pragma unroll
      for (int q = 0; q < 4; ++q) xv[q] = *(const u32x4*)(lds + L_A + w * RB + (i * 4 + q) * 1024 + lane * 16);
      xv[4] = *(const u32x4*)(lds + L_A + w * RB + 20480 + i * 512 + (lane & 31) * 16);
      SB();
      if (i > 0) bltr(4 + i);
      SB();
      float s1 = 0.f, s2 = 0.f;
#pragma unroll
      for (int e = 0; e < 4; ++e) { s1 = dot2(xv[4][e], ones, s1); s2 = dot2(xv[4][e], xv[4][e], s2); }
      s1 *= 0.5f; s2 *= 0.5f;
#pragma unroll
      for (int q = 0; q < 4; ++q)
#pragma unroll
        for (int e = 0; e < 4; ++e) { s1 = dot2(xv[q][e], ones, s1); s2 = dot2(xv[q][e], xv[q][e], s2); }
      float sm = red32(s1, s2);
      sm = quad_sum(red16(sm, sm));
      *(float*)(lds + L_SP + (20 * w + 4 * i + (n >> 2)) * 8 + (kg >> 1) * 4) = sm;
      SB();
    }
    asm volatile("s_waitcnt lgkmcnt(0)" ::: "memory");
    if (lane < 20) {
      f32x2 v = *(const f32x2*)(lds + L_SP + (20 * w + lane) * 8);
      float m = v.x * (1.f / D), var = fmaxf(v.y * (1.f / D) - m * m, 0.f);
      float den = rb(sqrtf(rb(rb(var) + 1e-5f)));
      *(f32x2*)(lds + L_P1 + (20 * w + lane) * 8) = f32x2{1.f / den, rb(m)};
    }
  }
#pragma unroll
  for (int ks = 0; ks < 18; ++ks) asm volatile("" : "+a"(Bw[ks][0]), "+a"(Bw[ks][1]));
  bar();
  STAMP(1);
  TAGS();
  const u64 tF = ((u64)tB << 32) | tA;
  f32x4 acc[5][2];
  float rsr, rsi;
  u32x4 kq[4];
  u32x4 Cw[9][4];
  const rsrc_t crs = __builtin_amdgcn_make_buffer_rsrc((void*)((w & 1) ? p.cim : p.cre), 0, D * D * 2, 0x00020000);
  auto cld = [&](int L) {
    const int d = L >> 3, j = L & 3, cb = ((64 * g + (n & 12)) * D + 288 * (w >> 1) + 8 * (lane & 3)) * 2 + j * D * 2;
    if (L >= 32) Cw[8][j] = __builtin_amdgcn_raw_buffer_load_b128(crs, cb + 64 * ((8 + g) % 9) + ((w + kg) & 3) * 32 * D, 0, 0);
    else Cw[L & 7][d] = __builtin_amdgcn_raw_buffer_load_b128(crs, cb + 64 * (((L & 4) + kg + g) % 9), ((w + d) & 3) * 32 * D, 0);
  };
  {
    u32x2 kvq;
    u32 kvo;
    SB();
    STAMP(23);
    gemm_bu<true>(lds, Bw, lane, acc, rsr, rsi, [&](int ks, int i) {
      if (ks < 5 && i == 0) {
        const int it = tid + 256 * ks, ch = it / 20, tq = it - 20 * ch, gq = 20 * c + tq;
        kvq = *(const u32x2*)(lds + L_GI + ch * 168 + tq * 8);
        kvo = gq < 512 ? W_KVT + (64 * g + ch) * 8192 + (511 - gq) * 16 : OOB;
      }
      if (ks < 5 && i == 4)
        stc(ws, kvo, u32x4{__builtin_amdgcn_alignbit(kvq.y, kvq.y, 16), tA, __builtin_amdgcn_alignbit(kvq.x, kvq.x, 16), tB});
      if (ks >= 6 && i == 2) cld(ks - 6);
      if (ks == 16 && i < 4) kq[i] = ldc(ws, W_KVT + ch0 * 8192 + (tid + 256 * i) * 16);
    });
  }
  STAMP(2);
  norm_acc(lds, L_P1, kg, acc, rsr, rsi);
  *(f32x2*)(lds + L_RS + tid * 8) = f32x2{rsr, rsi};
  float inr, ini;
  scan_a(sc, acc, ws, W_E + (c * D + pch) * 16, lane, tA, tB, inr, ini);
  STAMP(3);
  u32x2 dsk_h = ldg8(p.dsk + 64 * g + 16 * w + 4 * kg);
  const u16 sd_h = p.sd[ch0 + (w >> 1)];
  conv_gather<true>(lds + L_CV, ws, ch0, tid, kq, tA, tB, budget);
  bar();
  STAMP(4);
  f32x4 cacc[8];
  u32x4 ev[7];
#pragma unroll
  for (int j = 0; j < 8; ++j) cacc[j] = f32x4{0, 0, 0, 0};
  conv_mma<0, 2>(lds, L_CV, w, lane, cacc, [&](int r, int b) {
    if (b < 6) cld(12 + 6 * r + b);
    if (r == 1 && b == 2) {
#pragma unroll
      for (int j = 0; j < 7; ++j) { const int cp = c - 1 - kg - 4 * j; ev[j] = ldc(ws, W_E + pch * 16 + (cp >= 0 ? cp : 0) * (D * 16)); }
    }
  });
  STAMP(5);
  scan_b<true>(sc, acc, lds, ws, W_E + pch * 16, W_H + (c * 36 + 4 * g + w) * 5120, c, w, lane, tA, tB, inr, ini, ev, budget);
  STAMP(6);
  {
    conv_mma<2, 4>(lds, L_CV, w, lane, cacc, [&](int r, int b) {
      if (b < 6) cld(12 + 6 * r + b);
      if (r == 3 && b == 7) {
        const u32 o = W_U3 + c * 94208 + w * 23552 + (lane >> 2) * (g < 8 ? 4096 : 512) + (g < 8 ? g * 512 : 20480) + (lane & 3) * 128;
        asm volatile("s_mov_b32 m0, %2\n\ts_nop 0\n\tbuffer_load_dword %0, %1, 0 offen sc1 lds"
                     :: "v"(lane < 20 ? o : OOB), "s"(ws), "s"(lb + L_TOT - 256) : "memory");
      }
      if (r == 2 && b == 7) {
        asm volatile("s_waitcnt vmcnt(6)\n\ts_barrier" ::: "memory");
        stc2(ws, tid == 0 ? W_FLG + c * 128 + g * 8 : OOB, u32x2{tA, tB});
      }
    });
  }
  float dk0 = lo16(dsk_h.x), dk1 = hi16(dsk_h.x), dk2 = lo16(dsk_h.y), dk3 = hi16(dsk_h.y);
  f32x4 z[5];
  auto frag_off = [&](int i, int rb) {
    const int tl = 16 * i + n, q = tl / 20, rem = tl - 20 * q;
    return q * rb + (rem >> 2) * (g < 8 ? 4096 : 512) + (g < 8 ? g * 512 : 20480) + (w >> 1) * 256 + (rem & 3) * 64 + (w & 1) * 32 + kg * 8;
  };
#pragma unroll
  for (int i = 0; i < 5; ++i) {
    u32x2 xq = *(const u32x2*)(lds + L_A + frag_off(i, RB));
    f32x2 pr = *(const f32x2*)(lds + L_P1 + (16 * i + n) * 8);
    const f32x4 d = f32x4{lo16(xq.x), hi16(xq.x), lo16(xq.y), hi16(xq.y)} - pr.y;
    z[i] = d + d * pr.x * f32x4{dk0, dk1, dk2, dk3};
    asm volatile("" : "+v"(z[i]));
  }
  conv_finish(lds, lds + L_CV, ws, ch0, b2f(sd_h), w, lane, cacc, tA, tB);
  *(f32x4*)(lds + L_CV + tid * 16) = f32x4{sc.ar, sc.ai, sc.cr, sc.ci};
  *(f32x4*)(lds + L_CV + 4096 + tid * 16) = f32x4{sc.a20r, sc.a20i, sc.a40r, sc.a40i};
  *(f32x4*)(lds + L_CV + CVSZ + tid * 16) = f32x4{sc.aqr, sc.aqi, sc.bqr, sc.bqi};
  *(f32x2*)(lds + L_CV + CVSZ + 4096 + tid * 8) = f32x2{sc.a320r, sc.a320i};
  *(u32x2*)(lds + L_CV + CVSZ + 6144 + tid * 8) = dsk_h;
  STAMP(7);
  PHASE();
  twa = L_HI + w * 5376 + lane * 16; tra = L_HI + w * 5376 + (n & 3) * 1040 + (n >> 2) * 64;
  auto cltr = [&](int G) {
    if (G < 8) ltr<16, 32, 48>(lds, twa, tra + kg * 256, Cw[4 * (G & 1)][G >> 1], Cw[4 * (G & 1) + 1][G >> 1], Cw[4 * (G & 1) + 2][G >> 1], Cw[4 * (G & 1) + 3][G >> 1]);
    else ltr<256, 512, 768>(lds, twa, tra + kg * 16, Cw[8][0], Cw[8][1], Cw[8][2], Cw[8][3]);
  };
  {
    const u64 pre = poll_flags(p.ws, W_FLG + c * 128, lane);
    SB();
    cltr(0); cltr(1); cltr(2);
    SB();
    STAMP(24);
    wait_flags(p.ws, W_FLG + c * 128, tF, lane, budget, pre);
  }
  STAMP(8);
  u32 gq[5][2];
  {
    u32 off[5]; bool need[5]; u32x4 v[5];
    auto goff = [&]() {
#pragma unroll
      for (int r = 0; r < 5; ++r) {
        const int it = lane + 64 * r, ch = it / 20, tq = it - 20 * ch, q = 20 * c + tq;
        need[r] = true;
        off[r] = W_GT + (64 * g + 16 * w + ch) * 8192 + (q < 512 ? q : 511) * 16;
      }
    };
    gemm_y<0>(lds, lb, ws, W_H + c * 36 * 5120, Cw, w, g, lane, z, [&](int j) {
      if (j >= 10) cltr(j - 7);
      if (j >= 12) cltr(j - 5);
      if (j == -2) STAMP(22);
      if (j == -1) STAMP(29);
      if (j == 4) STAMP(27);
      if (j == 8) STAMP(31);
      if (j != 7) return;
      goff();
#pragma unroll
      for (int r = 0; r < 5; ++r) v[r] = ldc(ws, off[r]);
    });
    STAMP(9);
    PHASE();
    rs_part(lds, z, w, lane);
    bar();
    rs_pub(lds, ws, W_ST + (c * 9 + g) * 1280, tid, tA, tB);
    goff();
    gather<5, true>(ws, off, need, v, tA, tB, budget);
#pragma unroll
    for (int r = 0; r < 5; ++r) {
      const int it = lane + 64 * r, ch = it / 20, tq = it - 20 * ch;
      *(u32x2*)(lds + L_GI + (16 * w + ch) * 168 + tq * 8) = u32x2{v[r].x, v[r].z};
    }
    asm volatile("s_waitcnt lgkmcnt(0)" ::: "memory");
#pragma unroll
    for (int i = 0; i < 5; ++i) {
      u32x2 t = trread(lds, L_GI + (16 * w + 4 * kg + (n >> 2)) * 168 + 8 * (n & 3) + 32 * i);
      gq[i][0] = t.x; gq[i][1] = t.y;
    }
  }
  STAMP(10);
  u32x4 sv[3];
  rs_get<false>(lds, L_PAR, ws, W_ST + c * 9 * 1280, w, lane, sv, tA, tB, budget);
  bar();
  STAMP(11);
#pragma unroll
  for (int i = 0; i < 5; ++i) {
    f32x2 pr = *(const f32x2*)(lds + L_PAR + (16 * i + n) * 8);
    float gv[4] = {lo16(gq[i][0]), hi16(gq[i][0]), lo16(gq[i][1]), hi16(gq[i][1])};
#pragma unroll
    for (int e = 0; e < 4; ++e) { float t = z[i][e] - pr.y; z[i][e] = t + t * pr.x * gv[e]; }
    stc2(ws, W_U3 + c * 94208 + frag_off(i, 23552), u32x2{pk(z[i].x, z[i].y), pk(z[i].z, z[i].w)});
  }
  SB();
  rs_part(lds, z, w, lane);
  drain();
  bar();
  stc2(ws, tid == 0 ? W_FLG + (NC + c) * 128 + g * 8 : OOB, u32x2{tA, tB});
  rs_pub(lds, ws, W_ST + ((NC + c) * 9 + g) * 1280, tid, tA, tB);
  __builtin_amdgcn_s_sleep(5);
  wait_flags(p.ws, W_FLG + (NC + c) * 128, tF, lane, budget, tF + 1);
  STAMP(12);
  PHASE();
  const u32 ub = W_U3 + c * 94208 + w * 23552 + lane * 16;
  auto udma = [&](int j) {
    const int o = j < 20 ? (j % 5) * 4096 + (j / 5) * 1024 : j * 1024;
    dma_c(ws, ub, o, lb + L_A + w * RB + o);
  };
#pragma unroll
  for (int j = 0; j < 10; ++j) udma(j);
  asm volatile("s_waitcnt vmcnt(5)" ::: "memory");
  bar();
  STAMP(13);
#pragma unroll
  for (int i = 0; i < 5; ++i) asm volatile("" : "+v"(gq[i][0]), "+v"(gq[i][1]));
  gemm_bu<false>(lds, Bw, lane, acc, rsr, rsi, [&](int ks, int i) {
    if (ks == 12 && i < 3) sv[i] = ldc(ws, W_ST + (NC + c) * 9 * 1280 + rs_off(w, lane) + i * 1280);
    if (5 * ks + i < 13) udma(10 + 5 * ks + i);
    if (i == 4 && ks == 2) asm volatile("s_waitcnt vmcnt(13)\n\ts_barrier" ::: "memory");
    if (i == 4 && ks == 6) asm volatile("s_waitcnt vmcnt(8)\n\ts_barrier" ::: "memory");
    if (i == 4 && (ks == 10 || ks == 14)) asm volatile("s_waitcnt vmcnt(3)\n\ts_barrier" ::: "memory");
  });
  STAMP(25);
  PHASE();
  rs_get<true>(lds, L_P3, ws, W_ST + (NC + c) * 9 * 1280, w, lane, sv, tA, tB, budget);
  bar();
  {
    const f32x2 q = *(const f32x2*)(lds + L_RS + tid * 8);
    norm_acc(lds, L_P3, kg, acc, q.x, q.y);
  }
  {
    f32x4 a = *(const f32x4*)(lds + L_CV + tid * 16), b = *(const f32x4*)(lds + L_CV + 4096 + tid * 16);
    f32x4 e = *(const f32x4*)(lds + L_CV + CVSZ + tid * 16);
    f32x2 f = *(const f32x2*)(lds + L_CV + CVSZ + 4096 + tid * 8);
    sc.ar = a.x; sc.ai = a.y; sc.cr = a.z; sc.ci = a.w; sc.a20r = b.x; sc.a20i = b.y; sc.a40r = b.z; sc.a40i = b.w;
    sc.aqr = e.x; sc.aqi = e.y; sc.bqr = e.z; sc.bqi = e.w; sc.a320r = f.x; sc.a320i = f.y;
  }
  scan_a(sc, acc, ws, W_E + ((NC + c) * D + pch) * 16, lane, tA, tB, inr, ini);
  STAMP(14);
  SB();
  dsk_h = *(const u32x2*)(lds + L_CV + CVSZ + 6144 + tid * 8);
#pragma unroll
  for (int i = 0; i < 5; ++i) {
    f32x2 pr = *(const f32x2*)(lds + L_P3 + (16 * i + n) * 8);
    const f32x4 t = z[i] - pr.y;
    z[i] = t + t * pr.x * f32x4{lo16(dsk_h.x), hi16(dsk_h.x), lo16(dsk_h.y), hi16(dsk_h.y)};
#pragma unroll
    for (int r = 0; r < 4; ++r) {
      const float a = acc[i][0][r], b = acc[i][1][r];
      acc[i][0][r] = sc.cr * a - sc.ci * b; acc[i][1][r] = -(sc.cr * b) - sc.ci * a;
    }
    asm volatile("" : "+v"(acc[i][0]), "+v"(acc[i][1]), "+v"(z[i]));
  }
  SB();
  scan_b<false>(sc, acc, lds, ws, W_E + (NC * D + pch) * 16, W_H + ((NC + c) * 36 + 4 * g + w) * 5120, c, w, lane, tA, tB, inr, ini, ev, budget);
  drain();
  bar();
  stc2(ws, tid == 0 ? W_FLG + (2 * NC + c) * 128 + g * 8 : OOB, u32x2{tA, tB});
  STAMP(15);
  __builtin_amdgcn_s_sleep(5);
  wait_flags(p.ws, W_FLG + (2 * NC + c) * 128, tF, lane, budget, tF + 1);
  STAMP(16);
  {
    gemm_y<0>(lds, lb, ws, W_H + (NC + c) * 36 * 5120, Cw, w, g, lane, z, [](int) {});
    rs_part(lds, z, w, lane);
  }
  bar();
  STAMP(17);
  rs_pub(lds, ws, W_ST + ((2 * NC + c) * 9 + g) * 1280, tid, tA, tB);
  f32x4 zc[5], gf[5];
#pragma unroll
  for (int i = 0; i < 5; ++i) {
    zc[i] = z[i] + (*(const float*)(lds + L_P1 + (16 * i + n) * 8 + 4) + *(const float*)(lds + L_PAR + (16 * i + n) * 8 + 4) +
                    *(const float*)(lds + L_P3 + (16 * i + n) * 8 + 4));
    gf[i] = f32x4{lo16(gq[i][0]), hi16(gq[i][0]), lo16(gq[i][1]), hi16(gq[i][1])};
    asm volatile("" : "+v"(zc[i]), "+v"(gf[i]));
  }
  rs_get<false>(lds, L_P2, ws, W_ST + (2 * NC + c) * 9 * 1280, w, lane, sv, tA, tB, budget);
  bar();
  STAMP(18);
  const rsrc_t ors = __builtin_amdgcn_make_buffer_rsrc(p.out, 0, S * D * 4, 0x00020000);
#pragma unroll
  for (int i = 0; i < 5; ++i) {
    f32x2 pr = *(const f32x2*)(lds + L_P2 + (16 * i + n) * 8);
    const f32x4 o = zc[i] + (z[i] - pr.y) * pr.x * gf[i];
    const int t = t0 + 16 * i + n;
    __builtin_amdgcn_raw_buffer_store_b128(__builtin_bit_cast(u32x4, o), ors, (t * D + 64 * g + 16 * w + 4 * kg) * 4, 0, 19);
  }
  if (c == NC - 1 && g == 0 && tid == 0) __hip_atomic_store((gu32*)p.ws, tA, __ATOMIC_RELAXED, __HIP_MEMORY_SCOPE_AGENT);
  STAMP(19);
  DUMP();
}

extern "C" void kernel_launch(void* const* d_in, const int* in_sizes, int n_in, void* d_out, int out_size, void* d_ws,
                              size_t ws_size, hipStream_t stream) {
  Params p;
  p.x = (const u16*)d_in[0]; p.lre = (const u16*)d_in[1]; p.lim = (const u16*)d_in[2];
  p.bre = (const u16*)d_in[3]; p.bim = (const u16*)d_in[4]; p.cre = (const u16*)d_in[5]; p.cim = (const u16*)d_in[6];
  p.ldt = (const u16*)d_in[7]; p.dsk = (const u16*)d_in[8]; p.k = (const u16*)d_in[10]; p.v = (const u16*)d_in[11];
  p.shk = (const u16*)d_in[12]; p.sk = (const u16*)d_in[13]; p.sd = (const u16*)d_in[14];
  p.out = (float*)d_out; p.ws = (unsigned char*)d_ws;
  (void)hipFuncSetAttribute((const void*)fused_kernel, hipFuncAttributeMaxDynamicSharedMemorySize, L_TOT);
  hipLaunchKernelGGL(fused_kernel, dim3(256), dim3(256), L_TOT, stream, p);
}
```

```cpp
#include <hip/hip_runtime.h>
#include <stdint.h>

typedef unsigned short u16;
typedef unsigned int u32;
typedef unsigned long long u64;
typedef __attribute__((ext_vector_type(4))) unsigned int u32x4;
typedef __attribute__((ext_vector_type(2))) unsigned int u32x2;
typedef __attribute__((ext_vector_type(4))) float f32x4;
typedef __attribute__((ext_vector_type(2))) float f32x2;
typedef __attribute__((ext_vector_type(8))) __bf16 bf16x8;
typedef __attribute__((ext_vector_type(2))) __bf16 bf16x2;
typedef __attribute__((ext_vector_type(4))) short s16x4;
typedef __attribute__((address_space(1))) u64 gu64;
typedef __attribute__((address_space(1))) u32 gu32;
typedef __attribute__((address_space(3))) s16x4 lds_s16x4;
typedef __amdgpu_buffer_rsrc_t rsrc_t;

#define DI __device__ __forceinline__
#define SB() __builtin_amdgcn_sched_barrier(0)

constexpr int S = 2048, D = 576, NC = 26;
constexpr u32 W_FLG = 4096;
constexpr u32 W_E = 32768;
constexpr u32 W_ST = W_E + 2 * NC * D * 16;
constexpr u32 W_KVT = 4u << 20;
constexpr u32 W_GT = W_KVT + D * 8192;
constexpr u32 W_H = W_GT + D * 8192;
constexpr u32 W_U3 = W_H + 2 * NC * 36 * 5120;
constexpr u32 W_END = W_U3 + NC * 94208;
constexpr u32 W_DBG = 64u << 20;
constexpr int L_A = 0;
constexpr int CVSZ = 13056, CV_K1 = 4224, CV_R = 8448;
constexpr int RB = 23568;
constexpr u32 OOB = 0x7ffffff0u;
constexpr int L_CV = 4 * RB;
constexpr int L_HI = L_CV + 2 * CVSZ;
constexpr int L_GI = L_HI + 4 * 5376;
constexpr int L_SP = L_GI + 64 * 168;
constexpr int L_PAR = L_SP + 2560;
constexpr int L_P3 = L_PAR + 640;
constexpr int L_P1 = L_PAR + 1280;
constexpr int L_P2 = L_P1 + 640;
constexpr int L_RS = L_P2 + 640;
constexpr int L_TOT = L_RS + 2048 + 1024;

struct Params {
  const u16 *x, *lre, *lim, *bre, *bim, *cre, *cim, *ldt, *dsk, *k, *v, *shk, *sk, *sd;
  float* out;
  unsigned char* ws;
};

DI float lo16(u32 w) { return __uint_as_float(w << 16); }
DI float hi16(u32 w) { return __uint_as_float(w & 0xffff0000u); }
DI float b2f(u16 h) { return __uint_as_float(((u32)h) << 16); }
DI u32 pk(float a, float b) {
  f32x2 v = {a, b};
  return __builtin_bit_cast(u32, __builtin_convertvector(v, bf16x2));
}
DI float rb(float a) { return lo16(pk(a, a)); }
DI f32x4 mma(u32x4 a, u32x4 b, f32x4 c) {
  return __builtin_amdgcn_mfma_f32_16x16x32_bf16(__builtin_bit_cast(bf16x8, a), __builtin_bit_cast(bf16x8, b), c, 0, 0, 0);
}
DI float dot2(u32 a, u32 b, float c) {
  return __builtin_amdgcn_fdot2_f32_bf16(__builtin_bit_cast(bf16x2, a), __builtin_bit_cast(bf16x2, b), c, false);
}
DI void bar() { asm volatile("s_waitcnt lgkmcnt(0)\n\ts_barrier" ::: "memory"); }
DI void drain() { asm volatile("s_waitcnt vmcnt(0)" ::: "memory"); }
DI void cfence() { asm volatile("" ::: "memory"); }
DI u32x4 ldc(rsrc_t r, u32 off) { return __builtin_amdgcn_raw_buffer_load_b128(r, off, 0, 16); }
DI u32x4 ldcs(rsrc_t r, u32 off, u32 soff) { return __builtin_amdgcn_raw_buffer_load_b128(r, off, soff, 16); }
DI void stc(rsrc_t r, u32 off, u32x4 v) { __builtin_amdgcn_raw_buffer_store_b128(v, r, off, 0, 16); }
DI void stc2(rsrc_t r, u32 off, u32x2 v) { __builtin_amdgcn_raw_buffer_store_b64(v, r, off, 0, 16); }
DI u32x4 ldg16(const u16* p) { return *(const u32x4*)p; }
DI u32x2 ldg8(const u16* p) { return *(const u32x2*)p; }
DI u32x2 trread(unsigned char* lds, int off) {
  s16x4 r = __builtin_amdgcn_ds_read_tr16_b64_v4i16((lds_s16x4*)(lds + off));
  return __builtin_bit_cast(u32x2, r);
}
DI void cmul(float ar, float ai, float br, float bi, float& cr, float& ci) {
  float r = ar * br - ai * bi, i = ar * bi + ai * br;
  cr = r; ci = i;
}
DI float red32(float a, float b) {
  auto r = __builtin_amdgcn_permlane32_swap(__float_as_uint(a), __float_as_uint(b), false, false);
  return __uint_as_float(r[0]) + __uint_as_float(r[1]);
}
DI float red16(float a, float b) {
  auto r = __builtin_amdgcn_permlane16_swap(__float_as_uint(a), __float_as_uint(b), false, false);
  return __uint_as_float(r[0]) + __uint_as_float(r[1]);
}

DI void pair16(float a, float& e, float& o) {
  auto r = __builtin_amdgcn_permlane16_swap(__float_as_uint(a), __float_as_uint(a), false, false);
  e = __uint_as_float(r[0]); o = __uint_as_float(r[1]);
}
DI void pair32(float a, float& e, float& o) {
  auto r = __builtin_amdgcn_permlane32_swap(__float_as_uint(a), __float_as_uint(a), false, false);
  e = __uint_as_float(r[0]); o = __uint_as_float(r[1]);
}
DI float sum3(float a, int lane) {
  return a + __int_as_float(__builtin_amdgcn_ds_bpermute(lane * 4 + 80, __float_as_int(a))) +
         __int_as_float(__builtin_amdgcn_ds_bpermute(lane * 4 + 160, __float_as_int(a)));
}
typedef __attribute__((address_space(3))) void lds_void;
typedef __attribute__((address_space(1))) const void gvoid;
template <int O1, int O2, int O3>
DI void ltr(unsigned char* lds, int wa, int ra, u32x4& r0, u32x4& r1, u32x4& r2, u32x4& r3) {
  *(u32x4*)(lds + wa) = r0; *(u32x4*)(lds + wa + 1040) = r1; *(u32x4*)(lds + wa + 2080) = r2; *(u32x4*)(lds + wa + 3120) = r3;
  cfence();
  r0 = *(const u32x4*)(lds + ra); r1 = *(const u32x4*)(lds + ra + O1);
  r2 = *(const u32x4*)(lds + ra + O2); r3 = *(const u32x4*)(lds + ra + O3);
  cfence();
}
template <int OFF>
DI void dma_g(const u16* base, u32 voff, u32 ldsa) {
  asm volatile("s_mov_b32 m0, %2\n\ts_nop 0\n\tglobal_load_lds_dwordx4 %0, %1 offset:%3" :: "v"(voff), "s"(base), "s"(ldsa - OFF), "n"(OFF) : "memory");
}
DI void dma_c(rsrc_t r, u32 voff, u32 soff, u32 ldsa) {
  asm volatile("s_mov_b32 m0, %3\n\ts_nop 0\n\tbuffer_load_dwordx4 %0, %1, %2 offen sc1 lds" :: "v"(voff), "s"(r), "s"(soff), "s"(ldsa) : "memory");
}
DI float quad_sum(float a) {
  a += __int_as_float(__builtin_amdgcn_update_dpp(0, __float_as_int(a), 0xb1, 0xf, 0xf, true));
  a += __int_as_float(__builtin_amdgcn_update_dpp(0, __float_as_int(a), 0x4e, 0xf, 0xf, true));
  return a;
}
constexpr unsigned SPIN_MAX = 1u << 20;
DI bool spin_next(unsigned& budget) {
  if (budget == 0) return false;
  --budget;
  __builtin_amdgcn_s_sleep(1);
  return true;
}
template <int N, bool PRE = false>
DI void gather(rsrc_t ws, const u32 (&off)[N], const bool (&need)[N], u32x4 (&v)[N], u32 tA, u32 tB, unsigned& budget) {
  if (PRE) {
    bool ok = true;
#pragma unroll
    for (int i = 0; i < N; ++i) ok &= (!need[i]) | ((v[i].y == tA) & (v[i].w == tB));
    if (__all(ok)) return;
  }
  for (;;) {
    cfence();
    bool ok = true;
#pragma unroll
    for (int i = 0; i < N; ++i) v[i] = ldc(ws, off[i]);
#pragma unroll
    for (int i = 0; i < N; ++i) ok &= (!need[i]) | ((v[i].y == tA) & (v[i].w == tB));
    if (__all(ok)) break;
    if (!spin_next(budget)) break;
  }
  cfence();
}
DI u64 poll_flags(unsigned char* wsp, u32 off, int lane) {
  return __hip_atomic_load((gu64*)(wsp + off) + (lane < 9 ? lane : 0), __ATOMIC_RELAXED, __HIP_MEMORY_SCOPE_AGENT);
}
DI void wait_flags(unsigned char* wsp, u32 off, u64 tag, int lane, unsigned& budget, u64 pre) {
  gu64* f = (gu64*)(wsp + off) + (lane < 9 ? lane : 0);
  if (!__all(pre == tag)) for (;;) {
    u64 x = __hip_atomic_load(f, __ATOMIC_RELAXED, __HIP_MEMORY_SCOPE_AGENT);
    if (__all(x == tag)) break;
    if (!spin_next(budget)) break;
  }
  __builtin_amdgcn_fence(__ATOMIC_ACQUIRE, "wavefront");
  cfence();
}

#define STAMP(i) do {} while (0)

template <int V> struct IC { static constexpr int v = V; constexpr operator int() const { return V; } };
template <int N, int K = 0, class F>
DI void static_for(F f) { if constexpr (K < N) { f(IC<K>{}); static_for<N, K + 1>(f); } }
constexpr int NPIN = 7;
template <int N>
DI void vmw() { asm volatile("s_waitcnt vmcnt(%0)" :: "n"(N) : "memory"); }
DI void conv_stage_k(unsigned char* cv, u32x4 kx, u32 knext, int tid) {
  *(u32x4*)(cv + 32 + 16 * tid) = kx;
  u32x4 s;
  s.x = __builtin_amdgcn_alignbit(kx.y, kx.x, 16);
  s.y = __builtin_amdgcn_alignbit(kx.z, kx.y, 16);
  s.z = __builtin_amdgcn_alignbit(kx.w, kx.z, 16);
  s.w = __builtin_amdgcn_alignbit(knext, kx.w, 16);
  *(u32x4*)(cv + CV_K1 + 32 + 16 * tid) = s;
  u32x4 z = {0, 0, 0, 0};
  if (tid == 0) {
    *(u32x4*)(cv) = z; *(u32x4*)(cv + 16) = z; *(u32x4*)(cv + CV_K1) = z;
    u32x4 f = {0, 0, 0, kx.x << 16};
    *(u32x4*)(cv + CV_K1 + 16) = f;
  }
  if (tid >= 64 && tid < 96) *(u32x4*)(cv + CV_R + 4096 + 16 * (tid - 64)) = z;
}
template <bool PRE>
DI void conv_gather(unsigned char* cv, rsrc_t ws, int ch0, int tid, u32x4 (&v)[4], u32 tA, u32 tB, unsigned& budget) {
  u32 off[4]; bool need[4];
#pragma unroll
  for (int r = 0; r < 4; ++r) { off[r] = W_KVT + ch0 * 8192 + (tid + 256 * r) * 16; need[r] = true; }
  gather<4, PRE>(ws, off, need, v, tA, tB, budget);
#pragma unroll
  for (int r = 0; r < 4; ++r) {
    u32x2 w = {v[r].x, v[r].z};
    *(u32x2*)(cv + (r >> 1) * CVSZ + CV_R + ((r & 1) * 256 + tid) * 8) = w;
  }
}
template <int R0, int R1, class F>
DI void conv_mma(unsigned char* lds, int cvo, int w, int lane, f32x4 (&acc)[8], F hook) {
  const int m = lane & 15, kg = lane >> 4;
  const int cb = cvo + (w >> 1) * CVSZ + (w & 1) * 256;
  int ka[4];
  ka[0] = cb + ((m & 1) ? 0 : CV_K1) + 16 * kg + 4 * ((m + 1) >> 1);
  ka[1] = ka[0] + 1024; ka[2] = ka[0] + 2048; ka[3] = ka[0] + 3072;
  int rbp = cb + CV_R + 480 - 32 * m + 16 * kg;
  asm volatile("" : "+v"(ka[0]), "+v"(ka[1]), "+v"(ka[2]), "+v"(ka[3]), "+v"(rbp));
  u32x4 A[8], B[8];
  auto ldA = [&](int r, int a) {
    const u32* q = (const u32*)(lds + ka[a >> 1] + 64 * r + 512 * (a & 1));
    A[a].x = q[0]; A[a].y = q[1]; A[a].z = q[2]; A[a].w = q[3];
  };
  auto ldB = [&](int r, int b) { B[b] = *(const u32x4*)(lds + rbp + 64 * r + 512 * (7 - b)); };
  ldB(R0, 0);
#pragma unroll
  for (int a = 7; a >= 0; --a) ldA(R0, a);
#pragma unroll
  for (int b = 1; b < 8; ++b) ldB(R0, b);
  SB();
#pragma unroll
  for (int r = R0; r < R1; ++r) {
#pragma unroll
    for (int b = 0; b < 8; ++b) {
#pragma unroll
      for (int a = 7 - b; a >= 0; --a) acc[a + b] = mma(A[a], B[b], acc[a + b]);
      SB();
      if (r + 1 < R1) { ldB(r + 1, b); ldA(r + 1, 7 - b); }
      hook(r, b);
      SB();
    }
  }
}
DI void conv_finish(unsigned char* lds, unsigned char* cv, rsrc_t ws, int ch0, float sdv, int w, int lane,
                    f32x4 (&acc)[8], u32 tA, u32 tB) {
  const int n = lane & 15, kg = lane >> 4, odd = w & 1;
  unsigned char* box = lds + L_HI + (w ^ 1) * 5376;
#pragma unroll
  for (int j = 0; j < 4; ++j) {
    f32x4 lo = acc[j], hi = acc[4 + j], snd, kp;
#pragma unroll
    for (int e = 0; e < 4; ++e) { snd[e] = odd ? lo[e] : hi[e]; kp[e] = odd ? hi[e] : lo[e]; }
    *(f32x4*)(box + j * 1024 + lane * 16) = snd;
    acc[j] = kp;
  }
  bar();
  const unsigned char* mine = lds + L_HI + w * 5376;
  const unsigned char* cb = cv + (w >> 1) * CVSZ + CV_R;
#pragma unroll
  for (int j = 0; j < 4; ++j) {
    f32x4 y = acc[j] + *(const f32x4*)(mine + j * 1024 + lane * 16);
    const int X = 256 * (4 * odd + j) + 16 * n + 4 * kg;
    u32x2 rv = *(const u32x2*)(cb + (2044 - X) * 2);
    u32 y01 = pk(y.x, y.y), y23 = pk(y.z, y.w);
    u32 s01 = pk(hi16(rv.y) * sdv, lo16(rv.y) * sdv), s23 = pk(hi16(rv.x) * sdv, lo16(rv.x) * sdv);
    u32x4 o;
    o.x = pk(lo16(y01) + lo16(s01), hi16(y01) + hi16(s01)); o.y = tA;
    o.z = pk(lo16(y23) + lo16(s23), hi16(y23) + hi16(s23)); o.w = tB;
    stc(ws, W_GT + (ch0 + (w >> 1)) * 8192 + X * 4, o);
  }
}

struct Scan {
  float ar, ai, cr, ci, a20r, a20i, a40r, a40i, aqr, aqi, bqr, bqi, a320r, a320i;
};

template <bool RS, class F>
DI void gemm_bu(const unsigned char* lds, u32x4 (&Bw)[18][2], int lane, f32x4 (&acc)[5][2], float& rsr, float& rsi, F pre) {
#pragma unroll
  for (int i = 0; i < 5; ++i) { acc[i][0] = f32x4{0, 0, 0, 0}; acc[i][1] = f32x4{0, 0, 0, 0}; }
  int ab0, ab1;
  {
    const int n = lane & 15, kg = lane >> 4;
    ab0 = L_A + (n >> 2) * RB + kg * 256 + (n & 3) * 64;
    ab1 = L_A + (n >> 2) * RB + 20480 + (kg & 1) * 256 + (n & 3) * 64 + (kg >> 1) * 16;
  }
  asm volatile("" : "+v"(ab0), "+v"(ab1));
  auto rdA = [&](int ks, int i) {
    return *(const u32x4*)(lds + (ks < 16 ? ab0 + i * 4096 + (ks >> 2) * 1024 + (ks & 3) * 16 : ab1 + i * 512 + (ks & 1) * 32));
  };
  f32x4 r0 = {0, 0, 0, 0}, r1 = {0, 0, 0, 0};
  const u32x4 ones = {0x3f803f80u, 0x3f803f80u, 0x3f803f80u, 0x3f803f80u};
  u32x4 a[2][5];
#pragma unroll
  for (int i = 0; i < 5; ++i) a[0][i] = rdA(0, i);
  SB();
#pragma unroll
  for (int ks = 0; ks < 18; ++ks) {
    asm("" : "+a"(Bw[ks][0]), "+a"(Bw[ks][1]));
#pragma unroll
    for (int i = 0; i < 5; ++i) {
      if (ks + 1 < 18) a[(ks + 1) & 1][i] = rdA(ks + 1, i);
      acc[i][0] = mma(a[ks & 1][i], Bw[ks][0], acc[i][0]);
      acc[i][1] = mma(a[ks & 1][i], Bw[ks][1], acc[i][1]);
      pre(ks, i);
      SB();
    }
    if (RS) { r0 = mma(ones, Bw[ks][0], r0); r1 = mma(ones, Bw[ks][1], r1); SB(); }
  }
  if (RS) { rsr = r0.x; rsi = r1.x; }
}
DI void scan_a(const Scan& sc, const f32x4 (&acc)[5][2], rsrc_t ws, u32 eoff, int lane, u32 tA, u32 tB, float& inr, float& ini) {
  float er = 0.f, ei = 0.f;
#pragma unroll
  for (int i = 0; i < 5; ++i)
#pragma unroll
    for (int r = 0; r < 4; ++r) {
      float nr = sc.ar * er + (acc[i][0][r] - sc.ai * ei), ni = sc.ar * ei + (acc[i][1][r] + sc.ai * er);
      er = nr; ei = ni;
    }
  const int q = lane >> 4;
  float e0r, e1r, e0i, e1i, pr, pi, p0r, p1r, p0i, p1i, tr, ti;
  pair16(er, e0r, e1r); pair16(ei, e0i, e1i);
  cmul(sc.a20r, sc.a20i, e0r, e0i, pr, pi);
  pr += e1r; pi += e1i;
  pair32(pr, p0r, p1r); pair32(pi, p0i, p1i);
  cmul(sc.a40r, sc.a40i, p0r, p0i, tr, ti);
  stc(ws, q == 0 ? eoff : OOB, u32x4{__float_as_uint(tr + p1r), tA, __float_as_uint(ti + p1i), tB});
  cmul(sc.a20r, sc.a20i, p0r, p0i, tr, ti);
  inr = q == 0 ? 0.f : q == 1 ? e0r : q == 2 ? p0r : tr + e0r;
  ini = q == 0 ? 0.f : q == 1 ? e0i : q == 2 ? p0i : ti + e0i;
}
template <bool PRE>
DI void scan_b(const Scan& sc, const f32x4 (&acc)[5][2], unsigned char* lds, rsrc_t ws, u32 ebase, u32 hoff, int c, int w,
               int lane, u32 tA, u32 tB, float inr, float ini, u32x4 (&v)[7], unsigned& budget) {
  const int n = lane & 15, q = lane >> 4;
  u32 off[7]; bool need[7];
#pragma unroll
  for (int j = 0; j < 7; ++j) {
    int cp = c - 1 - q - 4 * j;
    need[j] = cp >= 0;
    off[j] = ebase + (cp >= 0 ? cp : 0) * (D * 16);
  }
  gather<7, PRE>(ws, off, need, v, tA, tB, budget);
  float hr = 0.f, hi = 0.f;
#pragma unroll
  for (int j = 6; j >= 0; --j) {
    cmul(sc.a320r, sc.a320i, hr, hi, hr, hi);
    hr += need[j] ? __uint_as_float(v[j].x) : 0.f;
    hi += need[j] ? __uint_as_float(v[j].z) : 0.f;
  }
  cmul(sc.bqr, sc.bqi, hr, hi, hr, hi);
  hr = red16(hr, hr); hi = red16(hi, hi);
  hr = red32(hr, hr); hi = red32(hi, hi);
  cmul(sc.aqr, sc.aqi, hr, hi, hr, hi);
  hr += inr; hi += ini;
  if (!PRE) { cmul(sc.cr, sc.ci, hr, hi, hr, hi); hi = -hi; }
  unsigned char* img = lds + L_HI + w * 5376;
#pragma unroll
  for (int i = 0; i < 5; ++i) {
    float o_r[4], o_i[4];
#pragma unroll
    for (int r = 0; r < 4; ++r) {
      if (PRE) {
        float nr = sc.ar * hr + (acc[i][0][r] - sc.ai * hi), ni = sc.ar * hi + (acc[i][1][r] + sc.ai * hr);
        hr = nr; hi = ni;
        o_r[r] = sc.cr * hr - sc.ci * hi;
        o_i[r] = -(sc.cr * hi) - sc.ci * hr;
      } else {
        float nr = sc.ar * hr + (acc[i][0][r] + sc.ai * hi), ni = sc.ar * hi + (acc[i][1][r] - sc.ai * hr);
        o_r[r] = hr = nr; o_i[r] = hi = ni;
      }
    }
    *(u32x2*)(img + n * 168 + 40 * q + 8 * i) = u32x2{pk(o_r[0], o_r[1]), pk(o_r[2], o_r[3])};
    *(u32x2*)(img + (16 + n) * 168 + 40 * q + 8 * i) = u32x2{pk(o_i[0], o_i[1]), pk(o_i[2], o_i[3])};
  }
  asm volatile("s_waitcnt lgkmcnt(0)" ::: "memory");
  const int tro = L_HI + w * 5376 + (8 * q + (n >> 2)) * 168 + 8 * (n & 3);
#pragma unroll
  for (int i = 0; i < 5; ++i) {
    u32x2 a = trread(lds, tro + 32 * i), b = trread(lds, tro + 4 * 168 + 32 * i);
    stc(ws, hoff + i * 1024 + lane * 16, u32x4{a.x, a.y, b.x, b.y});
  }
}
DI void dma_c4(rsrc_t r, u32 voff, u32 soff, u32 mb) {
  asm volatile("s_mov_b32 m0, %3\n\ts_nop 0\n\tbuffer_load_dwordx4 %0, %1, %2 offen sc1 lds\n\t"
               "buffer_load_dwordx4 %0, %1, %2 offen offset:1024 sc1 lds\n\tbuffer_load_dwordx4 %0, %1, %2 offen offset:2048 sc1 lds\n\t"
               "buffer_load_dwordx4 %0, %1, %2 offen offset:3072 sc1 lds" :: "v"(voff), "s"(r), "s"(soff), "s"(mb) : "memory");
}
template <int NX, class F>
DI void gemm_y(unsigned char* lds, u32 lb, rsrc_t ws, u32 hbase, u32x4 (&Cw)[9][4], int w, int g, int lane, f32x4 (&y)[5], F mid) {
  f32x4 acc[4][5];
#pragma unroll
  for (int d = 0; d < 4; ++d)
#pragma unroll
    for (int i = 0; i < 5; ++i) acc[d][i] = d ? f32x4{0, 0, 0, 0} : y[i];
  u32 ho[3];
  {
    const int n = lane & 15, kg = lane >> 4;
    const u32 hb = hbase + (w >> 1) * 18 * 5120 + (w & 1) * 512 + n * 16;
    ho[0] = hb + ((kg + g) % 9) * 10240;
    ho[1] = hb + ((4 + kg + g) % 9) * 10240;
    ho[2] = hb + ((8 + g) % 9) * 10240 + (kg >> 1) * 5120 + (kg & 1) * 256;
  }
  const u32 sa = lb + L_A + w * RB, sh = lb + L_HI + w * 5376;
  auto dmaH = [&](int j) {
    const u32 so = ((j >> 1) & 1) * 5120 + (j & 1) * 256, mb = j == 4 ? sh : sa + (j % 5) * 5120;
    dma_c4(ws, ho[j >> 2], so, mb); dma_c(ws, ho[j >> 2], so + 4096, mb + 4096);
  };
  int ra = L_A + w * RB + lane * 16, rh = L_HI + w * 5376 + lane * 16;
  asm volatile("" : "+v"(ra), "+v"(rh));
  auto rdH = [&](int j, int i) { return *(const u32x4*)(lds + (j == 4 ? rh : ra + (j % 5) * 5120) + i * 1024); };
#pragma unroll
  for (int j = 0; j < 4; ++j) { dmaH(j); mid(10 + j); }
  u32x4 hf[2][5];
  mid(-2);
  vmw<15>();
  mid(-1);
#pragma unroll
  for (int i = 0; i < 5; ++i) hf[0][i] = rdH(0, i);
  SB();
  static_for<45>([&](auto I) {
    constexpr int j = I / 5, i = I % 5;
    if (i == 0 && j < NPIN) asm("" : "+a"(Cw[j][0]));
    if (j < 8) {
      vmw<(j < 5 ? 14 : j < 8 ? 39 - I + NX : 0)>();
      hf[(j + 1) & 1][i] = rdH(j + 1, i);
    }
    acc[3][i] = mma(Cw[j][3], hf[j & 1][i], acc[3][i]);
    acc[2][i] = mma(Cw[j][2], hf[j & 1][i], acc[2][i]);
    SB();
    if constexpr (j < 5) {
      constexpr int J = j + 4;
      dma_c(ws, ho[J >> 2], ((J >> 1) & 1) * 5120 + (J & 1) * 256 + i * 1024, (J == 4 ? sh : sa + (J % 5) * 5120) + i * 1024);
    }
    if (j == 8 && i > 0) {
#pragma unroll
      for (int d = 1; d < 4; ++d) *(f32x4*)(lds + ra + ((d - 1) * 5 + i - 1) * 1024) = acc[d][i > 0 ? i - 1 : 0];
    }
    SB();
    acc[1][i] = mma(Cw[j][1], hf[j & 1][i], acc[1][i]);
    acc[0][i] = mma(Cw[j][0], hf[j & 1][i], acc[0][i]);
    SB();
    if (i == 4) mid(j);
    SB();
  });
#pragma unroll
  for (int d = 1; d < 4; ++d) *(f32x4*)(lds + ra + ((d - 1) * 5 + 4) * 1024) = acc[d][4];
  bar();
  const unsigned char* b1 = lds + L_A + ((w + 3) & 3) * RB + lane * 16;
  const unsigned char* b2 = lds + L_A + ((w + 2) & 3) * RB + 5120 + lane * 16;
  const unsigned char* b3 = lds + L_A + ((w + 1) & 3) * RB + 10240 + lane * 16;
#pragma unroll
  for (int i = 0; i < 5; ++i)
    y[i] = (acc[0][i] + *(const f32x4*)(b1 + i * 1024)) + (*(const f32x4*)(b2 + i * 1024) + *(const f32x4*)(b3 + i * 1024));
}
DI void rs_part(unsigned char* lds, const f32x4 (&z)[5], int w, int lane) {
  const int n = lane & 15, kg = lane >> 4;
#pragma unroll
  for (int i = 0; i < 5; ++i) {
    float s0 = (z[i].x + z[i].y) + (z[i].z + z[i].w);
    float s1 = (z[i].x * z[i].x + z[i].y * z[i].y) + (z[i].z * z[i].z + z[i].w * z[i].w);
    float s = red32(s0, s1);
    *(float*)(lds + L_SP + (w * 80 + 16 * i + n) * 8 + (kg >> 1) * 4) = red16(s, s);
  }
}
DI void rs_pub(unsigned char* lds, rsrc_t ws, u32 base, int tid, u32 tA, u32 tB) {
  const int r = tid < 80 ? tid : 0;
  f32x2 a = *(const f32x2*)(lds + L_SP + r * 8), b = *(const f32x2*)(lds + L_SP + (80 + r) * 8);
  f32x2 e = *(const f32x2*)(lds + L_SP + (160 + r) * 8), f = *(const f32x2*)(lds + L_SP + (240 + r) * 8);
  a = (a + b) + (e + f);
  stc(ws, tid < 80 ? base + tid * 16 : OOB, u32x4{__float_as_uint(a.x), tA, __float_as_uint(a.y), tB});
}
DI u32 rs_off(int w, int lane) {
  const int l3 = lane < 60 ? lane : 0, th = l3 / 20;
  return (240 * th + 20 * w + l3 - 20 * th) * 16;
}
template <bool PRE>
DI void rs_get(unsigned char* lds, int lout, rsrc_t ws, u32 base, int w, int lane, u32x4 (&v)[3], u32 tA, u32 tB, unsigned& budget) {
  u32 off[3]; bool need[3];
#pragma unroll
  for (int j = 0; j < 3; ++j) { off[j] = base + rs_off(w, lane) + j * 1280; need[j] = true; }
  gather<3, PRE>(ws, off, need, v, tA, tB, budget);
  float s0 = __uint_as_float(v[0].x) + __uint_as_float(v[1].x) + __uint_as_float(v[2].x);
  float s1 = __uint_as_float(v[0].z) + __uint_as_float(v[1].z) + __uint_as_float(v[2].z);
  s0 = sum3(s0, lane) * (1.f / D);
  s1 = sum3(s1, lane) * (1.f / D);
  if (lane < 20) *(f32x2*)(lds + lout + (20 * w + lane) * 8) = f32x2{__builtin_amdgcn_rsqf(fmaxf(s1 - s0 * s0, 0.f) + 1e-5f), s0};
}
DI void norm_acc(const unsigned char* lds, int lp, int kg, f32x4 (&acc)[5][2], float rsr, float rsi) {
#pragma unroll
  for (int i = 0; i < 5; ++i) {
    f32x4 pa = *(const f32x4*)(lds + lp + (20 * kg + 4 * i) * 8), pb = *(const f32x4*)(lds + lp + (20 * kg + 4 * i) * 8 + 16);
    acc[i][0].x = (acc[i][0].x - pa.y * rsr) * pa.x; acc[i][1].x = (acc[i][1].x - pa.y * rsi) * pa.x;
    acc[i][0].y = (acc[i][0].y - pa.w * rsr) * pa.z; acc[i][1].y = (acc[i][1].y - pa.w * rsi) * pa.z;
    acc[i][0].z = (acc[i][0].z - pb.y * rsr) * pb.x; acc[i][1].z = (acc[i][1].z - pb.y * rsi) * pb.x;
    acc[i][0].w = (acc[i][0].w - pb.w * rsr) * pb.z; acc[i][1].w = (acc[i][1].w - pb.w * rsi) * pb.z;
  }
}
__global__ void __launch_bounds__(256, 1) fused_kernel(Params p) {
  extern __shared__ __attribute__((aligned(16))) unsigned char lds[];
  int tid = threadIdx.x, lane = tid & 63;
  const int w = __builtin_amdgcn_readfirstlane(tid >> 6);
  const int xcd = blockIdx.x & 7, slot = blockIdx.x >> 3;
  {
    u64 pc; u32 len;
    asm volatile("s_getpc_b64 %0\n.Lpf%=:\n\ts_mov_b32 %1, .Lkend_mark-.Lpf%=" : "=s"(pc), "=s"(len));
    const u32 o = (((slot & 15) * 4 + w) * 64 + lane) * 16;
    asm volatile("s_mov_b32 m0, %2\n\ts_nop 0\n\tglobal_load_lds_dwordx4 %0, %1" :: "v"((int)o < (int)len ? o : 0u), "s"(pc & ~15ull),
                 "s"((u32)(size_t)(lds_void*)lds + L_TOT - 1024) : "memory");
  }
  asm volatile("" :: "s"(p.x), "s"(p.lre), "s"(p.lim), "s"(p.bre), "s"(p.bim), "s"(p.cre), "s"(p.cim), "s"(p.ldt),
               "s"(p.dsk), "s"(p.k), "s"(p.v), "s"(p.shk), "s"(p.sk), "s"(p.sd), "s"(p.out), "s"(p.ws));
  int c, g, spare = -1;
  if (slot < 27) { c = xcd * 3 + slot / 9; g = slot % 9; }
  else {
    int e = (xcd & 3) * 5 + (slot - 27);
    if (xcd < 4) { int e2 = (xcd & 1) * 5 + (slot - 27); c = 24 + (xcd >> 1); g = e2; if (e2 >= 9) spare = 20 + (xcd >> 1); }
    else { spare = e; c = 0; g = e >> 1; }
  }
#define DUMP() do {} while (0)
  STAMP(0);
  const rsrc_t ws = __builtin_amdgcn_make_buffer_rsrc(p.ws, 0, W_END, 0x00020000);
  u32 ep = *(const __attribute__((address_space(4))) u32*)p.ws;
#define TAGS() asm volatile("" : "+s"(ep)); const u32 tA = ep + 1u; \
  const u32 tB = tA * 0x9E3779B1u + 0x7F4A7C15u
  unsigned budget = SPIN_MAX;
  int n = lane & 15, kg = lane >> 4;
#define PHASE() do { asm volatile("" : "+v"(tid)); lane = tid & 63; n = lane & 15; kg = lane >> 4; pch = 64 * g + 16 * w + n; } while (0)

  if (spare >= 0) {
    auto warm = [&]() {
      constexpr u32 NA = (W_ST + 3 * NC * 9 * 1280) / 128;
#pragma unroll
      for (int r = 0; r < 11; ++r) {
        const u32 L = (r * 4 + spare - 18) * 256 + tid, o = L < NA ? L * 128 : OOB;
        asm volatile("s_mov_b32 m0, %2\n\ts_nop 0\n\tbuffer_load_dword %0, %1, 0 offen sc1 lds"
                     :: "v"(o), "s"(ws), "s"((u32)(size_t)(lds_void*)lds + L_TOT - 256) : "memory");
      }
    };
    if (spare >= 18) { warm(); drain(); return; }
    const int ch0 = 64 * g + 52 + 6 * (spare & 1);
#pragma unroll
    for (int i = 0; i < 6; ++i) {
      const u16* kp = p.sk + (ch0 + i) * S + 8 * tid;
      u32x4 kx = ldg16(kp);
      u32 kn = tid < 255 ? kp[8] : 0;
      conv_stage_k(lds + i * CVSZ, kx, kn, tid);
    }
    u32 sd0 = p.sd[ch0 + (w >> 1)], sd1 = p.sd[ch0 + 2 + (w >> 1)], sd2 = p.sd[ch0 + 4 + (w >> 1)];
    TAGS();
    u32x4 kq[3][4];
    conv_gather<false>(lds, ws, ch0, tid, kq[0], tA, tB, budget);
#pragma unroll
    for (int r = 4; r < 12; ++r) kq[r >> 2][r & 3] = ldc(ws, W_KVT + ch0 * 8192 + (tid + 256 * r) * 16);
    conv_gather<true>(lds + 2 * CVSZ, ws, ch0 + 2, tid, kq[1], tA, tB, budget);
    conv_gather<true>(lds + 4 * CVSZ, ws, ch0 + 4, tid, kq[2], tA, tB, budget);
#pragma unroll 1
    for (int r = 0; r < 3; ++r) {
      const float sdv = lo16(r == 0 ? sd0 : r == 1 ? sd1 : sd2);
      bar();
      f32x4 acc[8];
#pragma unroll
      for (int j = 0; j < 8; ++j) acc[j] = f32x4{0, 0, 0, 0};
      conv_mma<0, 4>(lds, 2 * r * CVSZ, w, lane, acc, [](int, int) {});
      conv_finish(lds, lds + 2 * r * CVSZ, ws, ch0 + 2 * r, sdv, w, lane, acc, tA, tB);
    }
    asm volatile(".Lkend_mark:");
    STAMP(15);
    DUMP();
    return;
  }

  const int t0 = 80 * c;
  const u32 lb = (u32)(size_t)(lds_void*)lds;
  u32 xo[8];
  {
    const int r = (lane >> 2) & 3, jj = lane & 3;
#pragma unroll
    for (int i = 0; i < 5; ++i) {
      int t = t0 + 20 * w + 4 * i + r; t = t > S - 1 ? S - 1 : t;
      xo[i] = (t * D + 32 * kg + 8 * jj) * 2;
    }
#pragma unroll
    for (int u = 0; u < 3; ++u) {
      int t = t0 + 20 * w + 8 * u + 4 * (kg >> 1) + r; t = t > S - 1 ? S - 1 : t;
      xo[5 + u] = (t * D + 512 + 32 * (kg & 1) + 8 * jj) * 2;
    }
  }
  auto xdma = [&](int pc) {
    const u32 la = lb + L_A + w * RB + (pc >> 1) * 4096;
    if (pc & 1) { dma_g<512>(p.x, xo[pc >> 1], la + 2048); dma_g<768>(p.x, xo[pc >> 1], la + 3072); }
    else { dma_g<0>(p.x, xo[pc >> 1], la); dma_g<256>(p.x, xo[pc >> 1], la + 1024); }
    if (pc < 3) dma_g<0>(p.x, xo[5 + pc], lb + L_A + w * RB + 20480 + pc * 1024);
  };
  STAMP(26);
  u32x2 kk[8], vv[5];
  u32x4 shw[2];
  {
    const int col = 64 * g + 4 * (tid & 15), r0 = t0 + 5 * (tid >> 4);
#pragma unroll
    for (int j = 0; j < 8; ++j) { int t = r0 + j - 3; t = t < 0 ? 0 : (t > S - 1 ? S - 1 : t); kk[j] = ldg8(p.k + t * D + col); }
#pragma unroll
    for (int j = 0; j < 5; ++j) { int t = r0 + j; t = t > S - 1 ? S - 1 : t; vv[j] = ldg8(p.v + t * D + col); }
    shw[0] = ldg16(p.shk + col * 4); shw[1] = ldg16(p.shk + col * 4 + 8);
  }
  const int ch0 = 64 * g + 2 * c;
  u32x4 kx0 = ldg16(p.sk + ch0 * S + 8 * tid), kx1 = ldg16(p.sk + (ch0 + 1) * S + 8 * tid);
  u32 kn0 = p.sk[ch0 * S + 8 * tid + (tid < 255 ? 8 : 0)], kn1 = p.sk[(ch0 + 1) * S + 8 * tid + (tid < 255 ? 8 : 0)];
  int pch = 64 * g + 16 * w + n;
  u32 lre_h = p.lre[pch], lim_h = p.lim[pch], ldt_h = p.ldt[pch];
  __builtin_amdgcn_sched_barrier(0);
  u32x4 Bw[18][2];
  const int bo = (64 * g + 16 * w + (n & 12)) * D + 8 * (lane & 3);
  const u16 *br = p.bre + bo + 32 * kg, *bi = p.bim + bo + 32 * kg;
  const u16* bt = (kg < 2 ? p.bre : p.bim) + bo + 512 + 32 * (kg & 1);
  auto bld = [&](int G) {
    SB();
#pragma unroll
    for (int j = 0; j < 4; ++j) {
      if (G < 8) Bw[4 * (G >> 1) + j][G & 1] = ldg16(((G & 1) ? bi : br) + j * D + 128 * (G >> 1));
      else Bw[16 + (j >> 1)][j & 1] = ldg16(bt + j * D);
    }
    SB();
  };
#pragma unroll
  for (int G = 0; G < 5; ++G) bld(G);
  int twa = L_HI + w * 5376 + lane * 16, tra = L_HI + w * 5376 + (n & 3) * 1040 + (n >> 2) * 64;
  auto bltr = [&](int G) {
    const int i = G >> 1, m = G & 1;
    if (G < 8) ltr<16, 32, 48>(lds, twa, tra + kg * 256, Bw[4 * i][m], Bw[4 * i + 1][m], Bw[4 * i + 2][m], Bw[4 * i + 3][m]);
    else ltr<512, 32, 544>(lds, twa, tra + (kg & 1) * 256 + (kg >> 1) * 16, Bw[16][0], Bw[16][1], Bw[17][0], Bw[17][1]);
  };
  STAMP(20);
  {
    const int r0 = 5 * (tid >> 4);
    const bool first = (t0 + r0 == 0);
    asm volatile("" : "+v"(kk[0]), "+v"(kk[1]), "+v"(kk[2]), "+v"(kk[3]), "+v"(kk[4]), "+v"(kk[5]), "+v"(kk[6]), "+v"(kk[7]),
                 "+v"(vv[0]), "+v"(vv[1]), "+v"(vv[2]), "+v"(vv[3]), "+v"(vv[4]), "+v"(shw[0]), "+v"(shw[1]));
    asm volatile("" : "+v"(kx0), "+v"(kx1), "+v"(kn0), "+v"(kn1), "+v"(lre_h), "+v"(lim_h), "+v"(ldt_h));
    u16* img = (u16*)(lds + L_GI) + 4 * (tid & 15) * 84 + r0;
#pragma unroll
    for (int h = 0; h < 2; ++h) {
      f32x2 sw[4], kf[8];
      sw[0] = f32x2{lo16(shw[h].x), lo16(shw[h].z)}; sw[1] = f32x2{hi16(shw[h].x), hi16(shw[h].z)};
      sw[2] = f32x2{lo16(shw[h].y), lo16(shw[h].w)}; sw[3] = f32x2{hi16(shw[h].y), hi16(shw[h].w)};
#pragma unroll
      for (int j = 0; j < 8; ++j) {
        u32 q = kk[j][h];
        if (j < 3) q = first ? 0u : q;
        kf[j] = f32x2{lo16(q), hi16(q)};
      }
#pragma unroll
      for (int r = 0; r < 5; ++r) {
        SB();
        if (h == 0) bltr(r);
        else { xdma(2 * r); xdma(2 * r + 1); }
        SB();
        f32x2 a = kf[r] * sw[3];
#pragma unroll
        for (int j = 1; j < 4; ++j) a = __builtin_elementwise_fma(kf[r + j], sw[3 - j], a);
        a *= f32x2{lo16(vv[r][h]), hi16(vv[r][h])};
        const u32 o = pk(a.x, a.y);
        img[(2 * h) * 84 + r] = (u16)o;
        img[(2 * h + 1) * 84 + r] = (u16)(o >> 16);
      }
    }
  }
  STAMP(21);
  bld(5);
  conv_stage_k(lds + L_CV, kx0, tid < 255 ? kn0 : 0, tid);
  conv_stage_k(lds + L_CV + CVSZ, kx1, tid < 255 ? kn1 : 0, tid);
  STAMP(30);
  bld(6);
  Scan sc;
  {
    float lr = lo16(lre_h), li = lo16(lim_h), dt = rb(expf(lo16(ldt_h)));
    float e = expf(dt * lr), sn, cs;
    sincosf(dt * li, &sn, &cs);
    bld(7);
    sc.ar = e * cs; sc.ai = e * sn;
    float nr = sc.ar - 1.f, ni = sc.ai, dn = lr * lr + li * li;
    sc.cr = 2.f * ((nr * lr + ni * li) / dn); sc.ci = 2.f * ((ni * lr - nr * li) / dn);
    bld(8);
    float r2, i2, r4, i4, r5, i5, r80, i80, r160, i160, r60, i60, r240, i240;
    cmul(sc.ar, sc.ai, sc.ar, sc.ai, r2, i2); cmul(r2, i2, r2, i2, r4, i4); cmul(r4, i4, sc.ar, sc.ai, r5, i5);
    cmul(r5, i5, r5, i5, r2, i2); cmul(r2, i2, r2, i2, sc.a20r, sc.a20i);
    cmul(sc.a20r, sc.a20i, sc.a20r, sc.a20i, sc.a40r, sc.a40i);
    cmul(sc.a40r, sc.a40i, sc.a20r, sc.a20i, r60, i60);
    cmul(sc.a40r, sc.a40i, sc.a40r, sc.a40i, r80, i80);
    cmul(r80, i80, r80, i80, r160, i160); cmul(r160, i160, r80, i80, r240, i240);
    cmul(r160, i160, r160, i160, sc.a320r, sc.a320i);
    sc.aqr = kg == 0 ? 1.f : kg == 1 ? sc.a20r : kg == 2 ? sc.a40r : r60;
    sc.aqi = kg == 0 ? 0.f : kg == 1 ? sc.a20i : kg == 2 ? sc.a40i : i60;
    sc.bqr = kg == 0 ? 1.f : kg == 1 ? r80 : kg == 2 ? r160 : r240;
    sc.bqi = kg == 0 ? 0.f : kg == 1 ? i80 : kg == 2 ? i160 : i240;
  }
  STAMP(28);
  asm volatile("s_waitcnt vmcnt(16)" ::: "memory");
  {
    const u32 ones = 0x3f803f80u;
#pragma unroll
    for (int i = 0; i < 5; ++i) {
      u32x4 xv[5];
#pragma unroll
      for (int q = 0; q < 4; ++q) xv[q] = *(const u32x4*)(lds + L_A + w * RB + (i * 4 + q) * 1024 + lane * 16);
      xv[4] = *(const u32x4*)(lds + L_A + w * RB + 20480 + i * 512 + (lane & 31) * 16);
      SB();
      if (i > 0) bltr(4 + i);
      SB();
      float s1 = 0.f, s2 = 0.f;
#pragma unroll
      for (int e = 0; e < 4; ++e) { s1 = dot2(xv[4][e], ones, s1); s2 = dot2(xv[4][e], xv[4][e], s2); }
      s1 *= 0.5f; s2 *= 0.5f;
#pragma unroll
      for (int q = 0; q < 4; ++q)
#pragma unroll
        for (int e = 0; e < 4; ++e) { s1 = dot2(xv[q][e], ones, s1); s2 = dot2(xv[q][e], xv[q][e], s2); }
      float sm = red32(s1, s2);
      sm = quad_sum(red16(sm, sm));
      *(float*)(lds + L_SP + (20 * w + 4 * i + (n >> 2)) * 8 + (kg >> 1) * 4) = sm;
      SB();
    }
    asm volatile("s_waitcnt lgkmcnt(0)" ::: "memory");
    if (lane < 20) {
      f32x2 v = *(const f32x2*)(lds + L_SP + (20 * w + lane) * 8);
      float m = v.x * (1.f / D), var = fmaxf(v.y * (1.f / D) - m * m, 0.f);
      float den = rb(__builtin_amdgcn_sqrtf(rb(rb(var) + 1e-5f)));
      *(f32x2*)(lds + L_P1 + (20 * w + lane) * 8) = f32x2{__builtin_amdgcn_rcpf(den), rb(m)};
    }
  }
#pragma unroll
  for (int ks = 0; ks < 18; ++ks) asm volatile("" : "+a"(Bw[ks][0]), "+a"(Bw[ks][1]));
  bar();
  STAMP(1);
  TAGS();
  const u64 tF = ((u64)tB << 32) | tA;
  f32x4 acc[5][2];
  float rsr, rsi;
  u32x4 kq[4];
  u32x4 Cw[9][4];
  const rsrc_t crs = __builtin_amdgcn_make_buffer_rsrc((void*)((w & 1) ? p.cim : p.cre), 0, D * D * 2, 0x00020000);
  auto cld = [&](int L) {
    const int d = L >> 3, j = L & 3, cb = ((64 * g + (n & 12)) * D + 288 * (w >> 1) + 8 * (lane & 3)) * 2 + j * D * 2;
    if (L >= 32) Cw[8][j] = __builtin_amdgcn_raw_buffer_load_b128(crs, cb + 64 * ((8 + g) % 9) + ((w + kg) & 3) * 32 * D, 0, 0);
    else Cw[L & 7][d] = __builtin_amdgcn_raw_buffer_load_b128(crs, cb + 64 * (((L & 4) + kg + g) % 9), ((w + d) & 3) * 32 * D, 0);
  };
  {
    u32x2 kvq;
    u32 kvo;
    SB();
    STAMP(23);
    gemm_bu<true>(lds, Bw, lane, acc, rsr, rsi, [&](int ks, int i) {
      if (ks < 5 && i == 0) {
        const int it = tid + 256 * ks, ch = it / 20, tq = it - 20 * ch, gq = 20 * c + tq;
        kvq = *(const u32x2*)(lds + L_GI + ch * 168 + tq * 8);
        kvo = gq < 512 ? W_KVT + (64 * g + ch) * 8192 + (511 - gq) * 16 : OOB;
      }
      if (ks < 5 && i == 4)
        stc(ws, kvo, u32x4{__builtin_amdgcn_alignbit(kvq.y, kvq.y, 16), tA, __builtin_amdgcn_alignbit(kvq.x, kvq.x, 16), tB});
      if (ks >= 6 && i == 2) cld(ks - 6);
      if (ks == 16 && i < 4) kq[i] = ldc(ws, W_KVT + ch0 * 8192 + (tid + 256 * i) * 16);
    });
  }
  STAMP(2);
  norm_acc(lds, L_P1, kg, acc, rsr, rsi);
  *(f32x2*)(lds + L_RS + tid * 8) = f32x2{rsr, rsi};
  float inr, ini;
  scan_a(sc, acc, ws, W_E + (c * D + pch) * 16, lane, tA, tB, inr, ini);
  STAMP(3);
  u32x2 dsk_h = ldg8(p.dsk + 64 * g + 16 * w + 4 * kg);
  const u16 sd_h = p.sd[ch0 + (w >> 1)];
  conv_gather<true>(lds + L_CV, ws, ch0, tid, kq, tA, tB, budget);
  bar();
  STAMP(4);
  f32x4 cacc[8];
  u32x4 ev[7];
#pragma unroll
  for (int j = 0; j < 8; ++j) cacc[j] = f32x4{0, 0, 0, 0};
  conv_mma<0, 2>(lds, L_CV, w, lane, cacc, [&](int r, int b) {
    if (b < 6) cld(12 + 6 * r + b);
    if (r == 1 && b == 2) {
#pragma unroll
      for (int j = 0; j < 7; ++j) { const int cp = c - 1 - kg - 4 * j; ev[j] = ldc(ws, W_E + pch * 16 + (cp >= 0 ? cp : 0) * (D * 16)); }
    }
  });
  STAMP(5);
  scan_b<true>(sc, acc, lds, ws, W_E + pch * 16, W_H + (c * 36 + 4 * g + w) * 5120, c, w, lane, tA, tB, inr, ini, ev, budget);
  STAMP(6);
  {
    conv_mma<2, 4>(lds, L_CV, w, lane, cacc, [&](int r, int b) {
      if (b < 6) cld(12 + 6 * r + b);
      if (r == 3 && b == 7) {
        const u32 o = W_U3 + c * 94208 + w * 23552 + (lane >> 2) * (g < 8 ? 4096 : 512) + (g < 8 ? g * 512 : 20480) + (lane & 3) * 128;
        asm volatile("s_mov_b32 m0, %2\n\ts_nop 0\n\tbuffer_load_dword %0, %1, 0 offen sc1 lds"
                     :: "v"(lane < 20 ? o : OOB), "s"(ws), "s"(lb + L_TOT - 256) : "memory");
      }
      if (r == 2 && b == 7) {
        asm volatile("s_waitcnt vmcnt(6)\n\ts_barrier" ::: "memory");
        stc2(ws, tid == 0 ? W_FLG + c * 128 + g * 8 : OOB, u32x2{tA, tB});
      }
    });
  }
  float dk0 = lo16(dsk_h.x), dk1 = hi16(dsk_h.x), dk2 = lo16(dsk_h.y), dk3 = hi16(dsk_h.y);
  f32x4 z[5];
  auto frag_off = [&](int i, int rb) {
    const int tl = 16 * i + n, q = tl / 20, rem = tl - 20 * q;
    return q * rb + (rem >> 2) * (g < 8 ? 4096 : 512) + (g < 8 ? g * 512 : 20480) + (w >> 1) * 256 + (rem & 3) * 64 + (w & 1) * 32 + kg * 8;
  };
#pragma unroll
  for (int i = 0; i < 5; ++i) {
    u32x2 xq = *(const u32x2*)(lds + L_A + frag_off(i, RB));
    f32x2 pr = *(const f32x2*)(lds + L_P1 + (16 * i + n) * 8);
    const f32x4 d = f32x4{lo16(xq.x), hi16(xq.x), lo16(xq.y), hi16(xq.y)} - pr.y;
    z[i] = d + d * pr.x * f32x4{dk0, dk1, dk2, dk3};
    asm volatile("" : "+v"(z[i]));
  }
  conv_finish(lds, lds + L_CV, ws, ch0, b2f(sd_h), w, lane, cacc, tA, tB);
  *(f32x4*)(lds + L_CV + tid * 16) = f32x4{sc.ar, sc.ai, sc.cr, sc.ci};
  *(f32x4*)(lds + L_CV + 4096 + tid * 16) = f32x4{sc.a20r, sc.a20i, sc.a40r, sc.a40i};
  *(f32x4*)(lds + L_CV + CVSZ + tid * 16) = f32x4{sc.aqr, sc.aqi, sc.bqr, sc.bqi};
  *(f32x2*)(lds + L_CV + CVSZ + 4096 + tid * 8) = f32x2{sc.a320r, sc.a320i};
  *(u32x2*)(lds + L_CV + CVSZ + 6144 + tid * 8) = dsk_h;
  STAMP(7);
  PHASE();
  twa = L_HI + w * 5376 + lane * 16; tra = L_HI + w * 5376 + (n & 3) * 1040 + (n >> 2) * 64;
  auto cltr = [&](int G) {
    if (G < 8) ltr<16, 32, 48>(lds, twa, tra + kg * 256, Cw[4 * (G & 1)][G >> 1], Cw[4 * (G & 1) + 1][G >> 1], Cw[4 * (G & 1) + 2][G >> 1], Cw[4 * (G & 1) + 3][G >> 1]);
    else ltr<256, 512, 768>(lds, twa, tra + kg * 16, Cw[8][0], Cw[8][1], Cw[8][2], Cw[8][3]);
  };
  {
    const u64 pre = poll_flags(p.ws, W_FLG + c * 128, lane);
    SB();
    cltr(0); cltr(1); cltr(2);
    SB();
    STAMP(24);
    wait_flags(p.ws, W_FLG + c * 128, tF, lane, budget, pre);
  }
  STAMP(8);
  u32 gq[5][2];
  {
    u32 off[5]; bool need[5]; u32x4 v[5];
    auto goff = [&]() {
#pragma unroll
      for (int r = 0; r < 5; ++r) {
        const int it = lane + 64 * r, ch = it / 20, tq = it - 20 * ch, q = 20 * c + tq;
        need[r] = true;
        off[r] = W_GT + (64 * g + 16 * w + ch) * 8192 + (q < 512 ? q : 511) * 16;
      }
    };
    gemm_y<0>(lds, lb, ws, W_H + c * 36 * 5120, Cw, w, g, lane, z, [&](int j) {
      if (j >= 10) cltr(j - 7);
      if (j >= 12) cltr(j - 5);
      if (j == -2) STAMP(22);
      if (j == -1) STAMP(29);
      if (j == 4) STAMP(27);
      if (j == 8) STAMP(31);
      if (j != 7) return;
      goff();
#pragma unroll
      for (int r = 0; r < 5; ++r) v[r] = ldc(ws, off[r]);
    });
    STAMP(9);
    PHASE();
    rs_part(lds, z, w, lane);
    bar();
    rs_pub(lds, ws, W_ST + (c * 9 + g) * 1280, tid, tA, tB);
    goff();
    gather<5, true>(ws, off, need, v, tA, tB, budget);
#pragma unroll
    for (int r = 0; r < 5; ++r) {
      const int it = lane + 64 * r, ch = it / 20, tq = it - 20 * ch;
      *(u32x2*)(lds + L_GI + (16 * w + ch) * 168 + tq * 8) = u32x2{v[r].x, v[r].z};
    }
    asm volatile("s_waitcnt lgkmcnt(0)" ::: "memory");
#pragma unroll
    for (int i = 0; i < 5; ++i) {
      u32x2 t = trread(lds, L_GI + (16 * w + 4 * kg + (n >> 2)) * 168 + 8 * (n & 3) + 32 * i);
      gq[i][0] = t.x; gq[i][1] = t.y;
    }
  }
  STAMP(10);
  u32x4 sv[3];
  rs_get<false>(lds, L_PAR, ws, W_ST + c * 9 * 1280, w, lane, sv, tA, tB, budget);
  bar();
  STAMP(11);
#pragma unroll
  for (int i = 0; i < 5; ++i) {
    f32x2 pr = *(const f32x2*)(lds + L_PAR + (16 * i + n) * 8);
    float gv[4] = {lo16(gq[i][0]), hi16(gq[i][0]), lo16(gq[i][1]), hi16(gq[i][1])};
#pragma unroll
    for (int e = 0; e < 4; ++e) { float t = z[i][e] - pr.y; z[i][e] = t + t * pr.x * gv[e]; }
    stc2(ws, W_U3 + c * 94208 + frag_off(i, 23552), u32x2{pk(z[i].x, z[i].y), pk(z[i].z, z[i].w)});
  }
  SB();
  rs_part(lds, z, w, lane);
  drain();
  bar();
  stc2(ws, tid == 0 ? W_FLG + (NC + c) * 128 + g * 8 : OOB, u32x2{tA, tB});
  rs_pub(lds, ws, W_ST + ((NC + c) * 9 + g) * 1280, tid, tA, tB);
  __builtin_amdgcn_s_sleep(5);
  wait_flags(p.ws, W_FLG + (NC + c) * 128, tF, lane, budget, tF + 1);
  STAMP(12);
  PHASE();
  const u32 ub = W_U3 + c * 94208 + w * 23552 + lane * 16;
  auto udma = [&](int j) {
    const int o = j < 20 ? (j % 5) * 4096 + (j / 5) * 1024 : j * 1024;
    dma_c(ws, ub, o, lb + L_A + w * RB + o);
  };
#pragma unroll
  for (int j = 0; j < 10; ++j) udma(j);
  asm volatile("s_waitcnt vmcnt(5)" ::: "memory");
  bar();
  STAMP(13);
#pragma unroll
  for (int i = 0; i < 5; ++i) asm volatile("" : "+v"(gq[i][0]), "+v"(gq[i][1]));
  gemm_bu<false>(lds, Bw, lane, acc, rsr, rsi, [&](int ks, int i) {
    if (ks == 12 && i < 3) sv[i] = ldc(ws, W_ST + (NC + c) * 9 * 1280 + rs_off(w, lane) + i * 1280);
    if (5 * ks + i < 13) udma(10 + 5 * ks + i);
    if (i == 4 && ks == 2) asm volatile("s_waitcnt vmcnt(13)\n\ts_barrier" ::: "memory");
    if (i == 4 && ks == 6) asm volatile("s_waitcnt vmcnt(8)\n\ts_barrier" ::: "memory");
    if (i == 4 && (ks == 10 || ks == 14)) asm volatile("s_waitcnt vmcnt(3)\n\ts_barrier" ::: "memory");
  });
  STAMP(25);
  PHASE();
  rs_get<true>(lds, L_P3, ws, W_ST + (NC + c) * 9 * 1280, w, lane, sv, tA, tB, budget);
  bar();
  {
    const f32x2 q = *(const f32x2*)(lds + L_RS + tid * 8);
    norm_acc(lds, L_P3, kg, acc, q.x, q.y);
  }
  {
    f32x4 a = *(const f32x4*)(lds + L_CV + tid * 16), b = *(const f32x4*)(lds + L_CV + 4096 + tid * 16);
    f32x4 e = *(const f32x4*)(lds + L_CV + CVSZ + tid * 16);
    f32x2 f = *(const f32x2*)(lds + L_CV + CVSZ + 4096 + tid * 8);
    sc.ar = a.x; sc.ai = a.y; sc.cr = a.z; sc.ci = a.w; sc.a20r = b.x; sc.a20i = b.y; sc.a40r = b.z; sc.a40i = b.w;
    sc.aqr = e.x; sc.aqi = e.y; sc.bqr = e.z; sc.bqi = e.w; sc.a320r = f.x; sc.a320i = f.y;
  }
  scan_a(sc, acc, ws, W_E + ((NC + c) * D + pch) * 16, lane, tA, tB, inr, ini);
  STAMP(14);
  SB();
  dsk_h = *(const u32x2*)(lds + L_CV + CVSZ + 6144 + tid * 8);
#pragma unroll
  for (int i = 0; i < 5; ++i) {
    f32x2 pr = *(const f32x2*)(lds + L_P3 + (16 * i + n) * 8);
    const f32x4 t = z[i] - pr.y;
    z[i] = t + t * pr.x * f32x4{lo16(dsk_h.x), hi16(dsk_h.x), lo16(dsk_h.y), hi16(dsk_h.y)};
#pragma unroll
    for (int r = 0; r < 4; ++r) {
      const float a = acc[i][0][r], b = acc[i][1][r];
      acc[i][0][r] = sc.cr * a - sc.ci * b; acc[i][1][r] = -(sc.cr * b) - sc.ci * a;
    }
    asm volatile("" : "+v"(acc[i][0]), "+v"(acc[i][1]), "+v"(z[i]));
  }
  SB();
  scan_b<false>(sc, acc, lds, ws, W_E + (NC * D + pch) * 16, W_H + ((NC + c) * 36 + 4 * g + w) * 5120, c, w, lane, tA, tB, inr, ini, ev, budget);
  drain();
  bar();
  stc2(ws, tid == 0 ? W_FLG + (2 * NC + c) * 128 + g * 8 : OOB, u32x2{tA, tB});
  STAMP(15);
  __builtin_amdgcn_s_sleep(5);
  wait_flags(p.ws, W_FLG + (2 * NC + c) * 128, tF, lane, budget, tF + 1);
  STAMP(16);
  {
    gemm_y<0>(lds, lb, ws, W_H + (NC + c) * 36 * 5120, Cw, w, g, lane, z, [](int) {});
    rs_part(lds, z, w, lane);
  }
  bar();
  STAMP(17);
  rs_pub(lds, ws, W_ST + ((2 * NC + c) * 9 + g) * 1280, tid, tA, tB);
  f32x4 zc[5], gf[5];
#pragma unroll
  for (int i = 0; i < 5; ++i) {
    zc[i] = z[i] + (*(const float*)(lds + L_P1 + (16 * i + n) * 8 + 4) + *(const float*)(lds + L_PAR + (16 * i + n) * 8 + 4) +
                    *(const float*)(lds + L_P3 + (16 * i + n) * 8 + 4));
    gf[i] = f32x4{lo16(gq[i][0]), hi16(gq[i][0]), lo16(gq[i][1]), hi16(gq[i][1])};
    asm volatile("" : "+v"(zc[i]), "+v"(gf[i]));
  }
  rs_get<false>(lds, L_P2, ws, W_ST + (2 * NC + c) * 9 * 1280, w, lane, sv, tA, tB, budget);
  bar();
  STAMP(18);
  const rsrc_t ors = __builtin_amdgcn_make_buffer_rsrc(p.out, 0, S * D * 4, 0x00020000);
#pragma unroll
  for (int i = 0; i < 5; ++i) {
    f32x2 pr = *(const f32x2*)(lds + L_P2 + (16 * i + n) * 8);
    const f32x4 o = zc[i] + (z[i] - pr.y) * pr.x * gf[i];
    const int t = t0 + 16 * i + n;
    __builtin_amdgcn_raw_buffer_store_b128(__builtin_bit_cast(u32x4, o), ors, (t * D + 64 * g + 16 * w + 4 * kg) * 4, 0, 19);
  }
  if (c == NC - 1 && g == 0 && tid == 0) __hip_atomic_store((gu32*)p.ws, tA, __ATOMIC_RELAXED, __HIP_MEMORY_SCOPE_AGENT);
  STAMP(19);
  DUMP();
}

extern "C" void kernel_launch(void* const* d_in, const int* in_sizes, int n_in, void* d_out, int out_size, void* d_ws,
                              size_t ws_size, hipStream_t stream) {
  Params p;
  p.x = (const u16*)d_in[0]; p.lre = (const u16*)d_in[1]; p.lim = (const u16*)d_in[2];
  p.bre = (const u16*)d_in[3]; p.bim = (const u16*)d_in[4]; p.cre = (const u16*)d_in[5]; p.cim = (const u16*)d_in[6];
  p.ldt = (const u16*)d_in[7]; p.dsk = (const u16*)d_in[8]; p.k = (const u16*)d_in[10]; p.v = (const u16*)d_in[11];
  p.shk = (const u16*)d_in[12]; p.sk = (const u16*)d_in[13]; p.sd = (const u16*)d_in[14];
  p.out = (float*)d_out; p.ws = (unsigned char*)d_ws;
  (void)hipFuncSetAttribute((const void*)fused_kernel, hipFuncAttributeMaxDynamicSharedMemorySize, L_TOT);
  hipLaunchKernelGGL(fused_kernel, dim3(256), dim3(256), L_TOT, stream, p);
}
```

```cpp
#include <hip/hip_runtime.h>
#include <stdint.h>

typedef unsigned short u16;
typedef unsigned int u32;
typedef unsigned long long u64;
typedef __attribute__((ext_vector_type(4))) unsigned int u32x4;
typedef __attribute__((ext_vector_type(2))) unsigned int u32x2;
typedef __attribute__((ext_vector_type(4))) float f32x4;
typedef __attribute__((ext_vector_type(2))) float f32x2;
typedef __attribute__((ext_vector_type(8))) __bf16 bf16x8;
typedef __attribute__((ext_vector_type(2))) __bf16 bf16x2;
typedef __attribute__((ext_vector_type(4))) short s16x4;
typedef __attribute__((address_space(1))) u64 gu64;
typedef __attribute__((address_space(1))) u32 gu32;
typedef __attribute__((address_space(3))) s16x4 lds_s16x4;
typedef __amdgpu_buffer_rsrc_t rsrc_t;

#define DI __device__ __forceinline__
#define SB() __builtin_amdgcn_sched_barrier(0)

constexpr int S = 2048, D = 576, NC = 26;
constexpr u32 W_FLG = 4096;
constexpr u32 W_E = 32768;
constexpr u32 W_ST = W_E + 2 * NC * D * 16;
constexpr u32 W_KVT = 4u << 20;
constexpr u32 W_GT = W_KVT + D * 8192;
constexpr u32 W_H = W_GT + D * 8192;
constexpr u32 W_U3 = W_H + 2 * NC * 36 * 5120;
constexpr u32 W_END = W_U3 + NC * 94208;
constexpr u32 W_DBG = 64u << 20;
constexpr int L_A = 0;
constexpr int CVSZ = 13056, CV_K1 = 4224, CV_R = 8448;
constexpr int RB = 23568;
constexpr u32 OOB = 0x7ffffff0u;
constexpr int L_CV = 4 * RB;
constexpr int L_HI = L_CV + 2 * CVSZ;
constexpr int L_GI = L_HI + 4 * 5376;
constexpr int L_SP = L_GI + 64 * 168;
constexpr int L_PAR = L_SP + 2560;
constexpr int L_P3 = L_PAR + 640;
constexpr int L_P1 = L_PAR + 1280;
constexpr int L_P2 = L_P1 + 640;
constexpr int L_RS = L_P2 + 640;
constexpr int L_TOT = L_RS + 2048 + 1024;

struct Params {
  const u16 *x, *lre, *lim, *bre, *bim, *cre, *cim, *ldt, *dsk, *k, *v, *shk, *sk, *sd;
  float* out;
  unsigned char* ws;
};

DI float lo16(u32 w) { return __uint_as_float(w << 16); }
DI float hi16(u32 w) { return __uint_as_float(w & 0xffff0000u); }
DI float b2f(u16 h) { return __uint_as_float(((u32)h) << 16); }
DI u32 pk(float a, float b) {
  f32x2 v = {a, b};
  return __builtin_bit_cast(u32, __builtin_convertvector(v, bf16x2));
}
DI float rb(float a) { return lo16(pk(a, a)); }
DI f32x4 mma(u32x4 a, u32x4 b, f32x4 c) {
  return __builtin_amdgcn_mfma_f32_16x16x32_bf16(__builtin_bit_cast(bf16x8, a), __builtin_bit_cast(bf16x8, b), c, 0, 0, 0);
}
DI float dot2(u32 a, u32 b, float c) {
  return __builtin_amdgcn_fdot2_f32_bf16(__builtin_bit_cast(bf16x2, a), __builtin_bit_cast(bf16x2, b), c, false);
}
DI void bar() { asm volatile("s_waitcnt lgkmcnt(0)\n\ts_barrier" ::: "memory"); }
DI void drain() { asm volatile("s_waitcnt vmcnt(0)" ::: "memory"); }
DI void cfence() { asm volatile("" ::: "memory"); }
DI u32x4 ldc(rsrc_t r, u32 off) { return __builtin_amdgcn_raw_buffer_load_b128(r, off, 0, 16); }
DI u32x4 ldcs(rsrc_t r, u32 off, u32 soff) { return __builtin_amdgcn_raw_buffer_load_b128(r, off, soff, 16); }
DI void stc(rsrc_t r, u32 off, u32x4 v) { __builtin_amdgcn_raw_buffer_store_b128(v, r, off, 0, 16); }
DI void stc2(rsrc_t r, u32 off, u32x2 v) { __builtin_amdgcn_raw_buffer_store_b64(v, r, off, 0, 16); }
DI u32x4 ldg16(const u16* p) { return *(const u32x4*)p; }
DI u32x2 ldg8(const u16* p) { return *(const u32x2*)p; }
DI u32x2 trread(unsigned char* lds, int off) {
  s16x4 r = __builtin_amdgcn_ds_read_tr16_b64_v4i16((lds_s16x4*)(lds + off));
  return __builtin_bit_cast(u32x2, r);
}
DI void cmul(float ar, float ai, float br, float bi, float& cr, float& ci) {
  float r = ar * br - ai * bi, i = ar * bi + ai * br;
  cr = r; ci = i;
}
DI float red32(float a, float b) {
  auto r = __builtin_amdgcn_permlane32_swap(__float_as_uint(a), __float_as_uint(b), false, false);
  return __uint_as_float(r[0]) + __uint_as_float(r[1]);
}
DI float red16(float a, float b) {
  auto r = __builtin_amdgcn_permlane16_swap(__float_as_uint(a), __float_as_uint(b), false, false);
  return __uint_as_float(r[0]) + __uint_as_float(r[1]);
}

DI void pair16(float a, float& e, float& o) {
  auto r = __builtin_amdgcn_permlane16_swap(__float_as_uint(a), __float_as_uint(a), false, false);
  e = __uint_as_float(r[0]); o = __uint_as_float(r[1]);
}
DI void pair32(float a, float& e, float& o) {
  auto r = __builtin_amdgcn_permlane32_swap(__float_as_uint(a), __float_as_uint(a), false, false);
  e = __uint_as_float(r[0]); o = __uint_as_float(r[1]);
}
DI float sum3(float a, int lane) {
  return a + __int_as_float(__builtin_amdgcn_ds_bpermute(lane * 4 + 80, __float_as_int(a))) +
         __int_as_float(__builtin_amdgcn_ds_bpermute(lane * 4 + 160, __float_as_int(a)));
}
typedef __attribute__((address_space(3))) void lds_void;
typedef __attribute__((address_space(1))) const void gvoid;
template <int O1, int O2, int O3>
DI void ltr(unsigned char* lds, int wa, int ra, u32x4& r0, u32x4& r1, u32x4& r2, u32x4& r3) {
  *(u32x4*)(lds + wa) = r0; *(u32x4*)(lds + wa + 1040) = r1; *(u32x4*)(lds + wa + 2080) = r2; *(u32x4*)(lds + wa + 3120) = r3;
  cfence();
  r0 = *(const u32x4*)(lds + ra); r1 = *(const u32x4*)(lds + ra + O1);
  r2 = *(const u32x4*)(lds + ra + O2); r3 = *(const u32x4*)(lds + ra + O3);
  cfence();
}
template <int OFF>
DI void dma_g(const u16* base, u32 voff, u32 ldsa) {
  asm volatile("s_mov_b32 m0, %2\n\ts_nop 0\n\tglobal_load_lds_dwordx4 %0, %1 offset:%3" :: "v"(voff), "s"(base), "s"(ldsa - OFF), "n"(OFF) : "memory");
}
DI void dma_c(rsrc_t r, u32 voff, u32 soff, u32 ldsa) {
  asm volatile("s_mov_b32 m0, %3\n\ts_nop 0\n\tbuffer_load_dwordx4 %0, %1, %2 offen sc1 lds" :: "v"(voff), "s"(r), "s"(soff), "s"(ldsa) : "memory");
}
DI float quad_sum(float a) {
  a += __int_as_float(__builtin_amdgcn_update_dpp(0, __float_as_int(a), 0xb1, 0xf, 0xf, true));
  a += __int_as_float(__builtin_amdgcn_update_dpp(0, __float_as_int(a), 0x4e, 0xf, 0xf, true));
  return a;
}
constexpr unsigned SPIN_MAX = 1u << 20;
DI bool spin_next(unsigned& budget) {
  if (budget == 0) return false;
  --budget;
  __builtin_amdgcn_s_sleep(1);
  return true;
}
template <int N, bool PRE = false>
DI void gather(rsrc_t ws, const u32 (&off)[N], const bool (&need)[N], u32x4 (&v)[N], u32 tA, u32 tB, unsigned& budget) {
  if (PRE) {
    bool ok = true;
#pragma unroll
    for (int i = 0; i < N; ++i) ok &= (!need[i]) | ((v[i].y == tA) & (v[i].w == tB));
    if (__all(ok)) return;
  }
  for (;;) {
    cfence();
    bool ok = true;
#pragma unroll
    for (int i = 0; i < N; ++i) v[i] = ldc(ws, off[i]);
#pragma unroll
    for (int i = 0; i < N; ++i) ok &= (!need[i]) | ((v[i].y == tA) & (v[i].w == tB));
    if (__all(ok)) break;
    if (!spin_next(budget)) break;
  }
  cfence();
}
DI u64 poll_flags(unsigned char* wsp, u32 off, int lane) {
  return __hip_atomic_load((gu64*)(wsp + off) + (lane < 9 ? lane : 0), __ATOMIC_RELAXED, __HIP_MEMORY_SCOPE_AGENT);
}
DI void wait_flags(unsigned char* wsp, u32 off, u64 tag, int lane, unsigned& budget, u64 pre) {
  gu64* f = (gu64*)(wsp + off) + (lane < 9 ? lane : 0);
  if (!__all(pre == tag)) for (;;) {
    u64 x = __hip_atomic_load(f, __ATOMIC_RELAXED, __HIP_MEMORY_SCOPE_AGENT);
    if (__all(x == tag)) break;
    if (!spin_next(budget)) break;
  }
  __builtin_amdgcn_fence(__ATOMIC_ACQUIRE, "wavefront");
  cfence();
}

#define STAMP(i) do {} while (0)

template <int V> struct IC { static constexpr int v = V; constexpr operator int() const { return V; } };
template <int N, int K = 0, class F>
DI void static_for(F f) { if constexpr (K < N) { f(IC<K>{}); static_for<N, K + 1>(f); } }
constexpr int NPIN = 7;
template <int N>
DI void vmw() { asm volatile("s_waitcnt vmcnt(%0)" :: "n"(N) : "memory"); }
DI void conv_stage_k(unsigned char* cv, u32x4 kx, u32 knext, int tid) {
  *(u32x4*)(cv + 32 + 16 * tid) = kx;
  u32x4 s;
  s.x = __builtin_amdgcn_alignbit(kx.y, kx.x, 16);
  s.y = __builtin_amdgcn_alignbit(kx.z, kx.y, 16);
  s.z = __builtin_amdgcn_alignbit(kx.w, kx.z, 16);
  s.w = __builtin_amdgcn_alignbit(knext, kx.w, 16);
  *(u32x4*)(cv + CV_K1 + 32 + 16 * tid) = s;
  u32x4 z = {0, 0, 0, 0};
  if (tid == 0) {
    *(u32x4*)(cv) = z; *(u32x4*)(cv + 16) = z; *(u32x4*)(cv + CV_K1) = z;
    u32x4 f = {0, 0, 0, kx.x << 16};
    *(u32x4*)(cv + CV_K1 + 16) = f;
  }
  if (tid >= 64 && tid < 96) *(u32x4*)(cv + CV_R + 4096 + 16 * (tid - 64)) = z;
}
template <bool PRE>
DI void conv_gather(unsigned char* cv, rsrc_t ws, int ch0, int tid, u32x4 (&v)[4], u32 tA, u32 tB, unsigned& budget) {
  u32 off[4]; bool need[4];
#pragma unroll
  for (int r = 0; r < 4; ++r) { off[r] = W_KVT + ch0 * 8192 + (tid + 256 * r) * 16; need[r] = true; }
  gather<4, PRE>(ws, off, need, v, tA, tB, budget);
#pragma unroll
  for (int r = 0; r < 4; ++r) {
    u32x2 w = {v[r].x, v[r].z};
    *(u32x2*)(cv + (r >> 1) * CVSZ + CV_R + ((r & 1) * 256 + tid) * 8) = w;
  }
}
template <int R0, int R1, class F>
DI void conv_mma(unsigned char* lds, int cvo, int w, int lane, f32x4 (&acc)[8], F hook) {
  const int m = lane & 15, kg = lane >> 4;
  const int cb = cvo + (w >> 1) * CVSZ + (w & 1) * 256;
  int ka[4];
  ka[0] = cb + ((m & 1) ? 0 : CV_K1) + 16 * kg + 4 * ((m + 1) >> 1);
  ka[1] = ka[0] + 1024; ka[2] = ka[0] + 2048; ka[3] = ka[0] + 3072;
  int rbp = cb + CV_R + 480 - 32 * m + 16 * kg;
  asm volatile("" : "+v"(ka[0]), "+v"(ka[1]), "+v"(ka[2]), "+v"(ka[3]), "+v"(rbp));
  u32x4 A[8], B[8];
  auto ldA = [&](int r, int a) {
    const u32* q = (const u32*)(lds + ka[a >> 1] + 64 * r + 512 * (a & 1));
    A[a].x = q[0]; A[a].y = q[1]; A[a].z = q[2]; A[a].w = q[3];
  };
  auto ldB = [&](int r, int b) { B[b] = *(const u32x4*)(lds + rbp + 64 * r + 512 * (7 - b)); };
  ldB(R0, 0);
#pragma unroll
  for (int a = 7; a >= 0; --a) ldA(R0, a);
#pragma unroll
  for (int b = 1; b < 8; ++b) ldB(R0, b);
  SB();
#pragma unroll
  for (int r = R0; r < R1; ++r) {
#pragma unroll
    for (int b = 0; b < 8; ++b) {
#pragma unroll
      for (int a = 7 - b; a >= 0; --a) acc[a + b] = mma(A[a], B[b], acc[a + b]);
      SB();
      if (r + 1 < R1) { ldB(r + 1, b); ldA(r + 1, 7 - b); }
      hook(r, b);
      SB();
    }
  }
}
DI void conv_finish(unsigned char* lds, unsigned char* cv, rsrc_t ws, int ch0, float sdv, int w, int lane,
                    f32x4 (&acc)[8], u32 tA, u32 tB) {
  const int n = lane & 15, kg = lane >> 4, odd = w & 1;
  unsigned char* box = lds + L_HI + (w ^ 1) * 5376;
#pragma unroll
  for (int j = 0; j < 4; ++j) {
    f32x4 lo = acc[j], hi = acc[4 + j], snd, kp;
#pragma unroll
    for (int e = 0; e < 4; ++e) { snd[e] = odd ? lo[e] : hi[e]; kp[e] = odd ? hi[e] : lo[e]; }
    *(f32x4*)(box + j * 1024 + lane * 16) = snd;
    acc[j] = kp;
  }
  bar();
  const unsigned char* mine = lds + L_HI + w * 5376;
  const unsigned char* cb = cv + (w >> 1) * CVSZ + CV_R;
#pragma unroll
  for (int j = 0; j < 4; ++j) {
    f32x4 y = acc[j] + *(const f32x4*)(mine + j * 1024 + lane * 16);
    const int X = 256 * (4 * odd + j) + 16 * n + 4 * kg;
    u32x2 rv = *(const u32x2*)(cb + (2044 - X) * 2);
    u32 y01 = pk(y.x, y.y), y23 = pk(y.z, y.w);
    u32 s01 = pk(hi16(rv.y) * sdv, lo16(rv.y) * sdv), s23 = pk(hi16(rv.x) * sdv, lo16(rv.x) * sdv);
    u32x4 o;
    o.x = pk(lo16(y01) + lo16(s01), hi16(y01) + hi16(s01)); o.y = tA;
    o.z = pk(lo16(y23) + lo16(s23), hi16(y23) + hi16(s23)); o.w = tB;
    stc(ws, W_GT + (ch0 + (w >> 1)) * 8192 + X * 4, o);
  }
}

struct Scan {
  float ar, ai, cr, ci, a20r, a20i, a40r, a40i, aqr, aqi, bqr, bqi, a320r, a320i;
};

template <bool RS, class F>
DI void gemm_bu(const unsigned char* lds, u32x4 (&Bw)[18][2], int lane, f32x4 (&acc)[5][2], float& rsr, float& rsi, F pre) {
#pragma unroll
  for (int i = 0; i < 5; ++i) { acc[i][0] = f32x4{0, 0, 0, 0}; acc[i][1] = f32x4{0, 0, 0, 0}; }
  int ab0, ab1;
  {
    const int n = lane & 15, kg = lane >> 4;
    ab0 = L_A + (n >> 2) * RB + kg * 256 + (n & 3) * 64;
    ab1 = L_A + (n >> 2) * RB + 20480 + (kg & 1) * 256 + (n & 3) * 64 + (kg >> 1) * 16;
  }
  asm volatile("" : "+v"(ab0), "+v"(ab1));
  auto rdA = [&](int ks, int i) {
    return *(const u32x4*)(lds + (ks < 16 ? ab0 + i * 4096 + (ks >> 2) * 1024 + (ks & 3) * 16 : ab1 + i * 512 + (ks & 1) * 32));
  };
  f32x4 r0 = {0, 0, 0, 0}, r1 = {0, 0, 0, 0};
  const u32x4 ones = {0x3f803f80u, 0x3f803f80u, 0x3f803f80u, 0x3f803f80u};
  u32x4 a[2][5];
#pragma unroll
  for (int i = 0; i < 5; ++i) a[0][i] = rdA(0, i);
  SB();
#pragma unroll
  for (int ks = 0; ks < 18; ++ks) {
    asm("" : "+a"(Bw[ks][0]), "+a"(Bw[ks][1]));
#pragma unroll
    for (int i = 0; i < 5; ++i) {
      if (ks + 1 < 18) a[(ks + 1) & 1][i] = rdA(ks + 1, i);
      acc[i][0] = mma(a[ks & 1][i], Bw[ks][0], acc[i][0]);
      acc[i][1] = mma(a[ks & 1][i], Bw[ks][1], acc[i][1]);
      pre(ks, i);
      SB();
    }
    if (RS) { r0 = mma(ones, Bw[ks][0], r0); r1 = mma(ones, Bw[ks][1], r1); SB(); }
  }
  if (RS) { rsr = r0.x; rsi = r1.x; }
}
DI void scan_a(const Scan& sc, const f32x4 (&acc)[5][2], rsrc_t ws, u32 eoff, int lane, u32 tA, u32 tB, float& inr, float& ini) {
  float er = 0.f, ei = 0.f;
#pragma unroll
  for (int i = 0; i < 5; ++i)
#pragma unroll
    for (int r = 0; r < 4; ++r) {
      float nr = sc.ar * er + (acc[i][0][r] - sc.ai * ei), ni = sc.ar * ei + (acc[i][1][r] + sc.ai * er);
      er = nr; ei = ni;
    }
  const int q = lane >> 4;
  float e0r, e1r, e0i, e1i, pr, pi, p0r, p1r, p0i, p1i, tr, ti;
  pair16(er, e0r, e1r); pair16(ei, e0i, e1i);
  cmul(sc.a20r, sc.a20i, e0r, e0i, pr, pi);
  pr += e1r; pi += e1i;
  pair32(pr, p0r, p1r); pair32(pi, p0i, p1i);
  cmul(sc.a40r, sc.a40i, p0r, p0i, tr, ti);
  stc(ws, q == 0 ? eoff : OOB, u32x4{__float_as_uint(tr + p1r), tA, __float_as_uint(ti + p1i), tB});
  cmul(sc.a20r, sc.a20i, p0r, p0i, tr, ti);
  inr = q == 0 ? 0.f : q == 1 ? e0r : q == 2 ? p0r : tr + e0r;
  ini = q == 0 ? 0.f : q == 1 ? e0i : q == 2 ? p0i : ti + e0i;
}
template <bool PRE>
DI void scan_b(const Scan& sc, const f32x4 (&acc)[5][2], unsigned char* lds, rsrc_t ws, u32 ebase, u32 hoff, int c, int w,
               int lane, u32 tA, u32 tB, float inr, float ini, u32x4 (&v)[7], unsigned& budget) {
  const int n = lane & 15, q = lane >> 4;
  u32 off[7]; bool need[7];
#pragma unroll
  for (int j = 0; j < 7; ++j) {
    int cp = c - 1 - q - 4 * j;
    need[j] = cp >= 0;
    off[j] = ebase + (cp >= 0 ? cp : 0) * (D * 16);
  }
  gather<7, PRE>(ws, off, need, v, tA, tB, budget);
  float hr = 0.f, hi = 0.f;
#pragma unroll
  for (int j = 6; j >= 0; --j) {
    cmul(sc.a320r, sc.a320i, hr, hi, hr, hi);
    hr += need[j] ? __uint_as_float(v[j].x) : 0.f;
    hi += need[j] ? __uint_as_float(v[j].z) : 0.f;
  }
  cmul(sc.bqr, sc.bqi, hr, hi, hr, hi);
  hr = red16(hr, hr); hi = red16(hi, hi);
  hr = red32(hr, hr); hi = red32(hi, hi);
  cmul(sc.aqr, sc.aqi, hr, hi, hr, hi);
  hr += inr; hi += ini;
  if (!PRE) { cmul(sc.cr, sc.ci, hr, hi, hr, hi); hi = -hi; }
  unsigned char* img = lds + L_HI + w * 5376;
#pragma unroll
  for (int i = 0; i < 5; ++i) {
    float o_r[4], o_i[4];
#pragma unroll
    for (int r = 0; r < 4; ++r) {
      if (PRE) {
        float nr = sc.ar * hr + (acc[i][0][r] - sc.ai * hi), ni = sc.ar * hi + (acc[i][1][r] + sc.ai * hr);
        hr = nr; hi = ni;
        o_r[r] = sc.cr * hr - sc.ci * hi;
        o_i[r] = -(sc.cr * hi) - sc.ci * hr;
      } else {
        float nr = sc.ar * hr + (acc[i][0][r] + sc.ai * hi), ni = sc.ar * hi + (acc[i][1][r] - sc.ai * hr);
        o_r[r] = hr = nr; o_i[r] = hi = ni;
      }
    }
    *(u32x2*)(img + n * 168 + 40 * q + 8 * i) = u32x2{pk(o_r[0], o_r[1]), pk(o_r[2], o_r[3])};
    *(u32x2*)(img + (16 + n) * 168 + 40 * q + 8 * i) = u32x2{pk(o_i[0], o_i[1]), pk(o_i[2], o_i[3])};
  }
  asm volatile("s_waitcnt lgkmcnt(0)" ::: "memory");
  const int tro = L_HI + w * 5376 + (8 * q + (n >> 2)) * 168 + 8 * (n & 3);
#pragma unroll
  for (int i = 0; i < 5; ++i) {
    u32x2 a = trread(lds, tro + 32 * i), b = trread(lds, tro + 4 * 168 + 32 * i);
    stc(ws, hoff + i * 1024 + lane * 16, u32x4{a.x, a.y, b.x, b.y});
  }
}
DI void dma_c4(rsrc_t r, u32 voff, u32 soff, u32 mb) {
  asm volatile("s_mov_b32 m0, %3\n\ts_nop 0\n\tbuffer_load_dwordx4 %0, %1, %2 offen sc1 lds\n\t"
               "buffer_load_dwordx4 %0, %1, %2 offen offset:1024 sc1 lds\n\tbuffer_load_dwordx4 %0, %1, %2 offen offset:2048 sc1 lds\n\t"
               "buffer_load_dwordx4 %0, %1, %2 offen offset:3072 sc1 lds" :: "v"(voff), "s"(r), "s"(soff), "s"(mb) : "memory");
}
template <int NX, class F>
DI void gemm_y(unsigned char* lds, u32 lb, rsrc_t ws, u32 hbase, u32x4 (&Cw)[9][4], int w, int g, int lane, f32x4 (&y)[5], F mid) {
  f32x4 acc[4][5];
#pragma unroll
  for (int d = 0; d < 4; ++d)
#pragma unroll
    for (int i = 0; i < 5; ++i) acc[d][i] = d ? f32x4{0, 0, 0, 0} : y[i];
  u32 ho[3];
  {
    const int n = lane & 15, kg = lane >> 4;
    const u32 hb = hbase + (w >> 1) * 18 * 5120 + (w & 1) * 512 + n * 16;
    ho[0] = hb + ((kg + g) % 9) * 10240;
    ho[1] = hb + ((4 + kg + g) % 9) * 10240;
    ho[2] = hb + ((8 + g) % 9) * 10240 + (kg >> 1) * 5120 + (kg & 1) * 256;
  }
  const u32 sa = lb + L_A + w * RB, sh = lb + L_HI + w * 5376;
  auto dmaH = [&](int j) {
    const u32 so = ((j >> 1) & 1) * 5120 + (j & 1) * 256, mb = j == 4 ? sh : sa + (j % 5) * 5120;
    dma_c4(ws, ho[j >> 2], so, mb); dma_c(ws, ho[j >> 2], so + 4096, mb + 4096);
  };
  int ra = L_A + w * RB + lane * 16, rh = L_HI + w * 5376 + lane * 16;
  asm volatile("" : "+v"(ra), "+v"(rh));
  auto rdH = [&](int j, int i) { return *(const u32x4*)(lds + (j == 4 ? rh : ra + (j % 5) * 5120) + i * 1024); };
#pragma unroll
  for (int j = 0; j < 4; ++j) { dmaH(j); mid(10 + j); }
  u32x4 hf[2][5];
  mid(-2);
  vmw<15>();
  mid(-1);
#pragma unroll
  for (int i = 0; i < 5; ++i) hf[0][i] = rdH(0, i);
  SB();
  static_for<45>([&](auto I) {
    constexpr int j = I / 5, i = I % 5;
    if (i == 0 && j < NPIN) asm("" : "+a"(Cw[j][0]));
    if (j < 8) {
      vmw<(j < 5 ? 14 : j < 8 ? 39 - I + NX : 0)>();
      hf[(j + 1) & 1][i] = rdH(j + 1, i);
    }
    acc[3][i] = mma(Cw[j][3], hf[j & 1][i], acc[3][i]);
    acc[2][i] = mma(Cw[j][2], hf[j & 1][i], acc[2][i]);
    SB();
    if constexpr (j < 5) {
      constexpr int J = j + 4;
      dma_c(ws, ho[J >> 2], ((J >> 1) & 1) * 5120 + (J & 1) * 256 + i * 1024, (J == 4 ? sh : sa + (J % 5) * 5120) + i * 1024);
    }
    if (j == 8 && i > 0) {
#pragma unroll
      for (int d = 1; d < 4; ++d) *(f32x4*)(lds + ra + ((d - 1) * 5 + i - 1) * 1024) = acc[d][i > 0 ? i - 1 : 0];
    }
    SB();
    acc[1][i] = mma(Cw[j][1], hf[j & 1][i], acc[1][i]);
    acc[0][i] = mma(Cw[j][0], hf[j & 1][i], acc[0][i]);
    SB();
    if (i == 4) mid(j);
    SB();
  });
#pragma unroll
  for (int d = 1; d < 4; ++d) *(f32x4*)(lds + ra + ((d - 1) * 5 + 4) * 1024) = acc[d][4];
  bar();
  const unsigned char* b1 = lds + L_A + ((w + 3) & 3) * RB + lane * 16;
  const unsigned char* b2 = lds + L_A + ((w + 2) & 3) * RB + 5120 + lane * 16;
  const unsigned char* b3 = lds + L_A + ((w + 1) & 3) * RB + 10240 + lane * 16;
#pragma unroll
  for (int i = 0; i < 5; ++i)
    y[i] = (acc[0][i] + *(const f32x4*)(b1 + i * 1024)) + (*(const f32x4*)(b2 + i * 1024) + *(const f32x4*)(b3 + i * 1024));
}
DI void rs_part(unsigned char* lds, const f32x4 (&z)[5], int w, int lane) {
  const int n = lane & 15, kg = lane >> 4;
#pragma unroll
  for (int i = 0; i < 5; ++i) {
    float s0 = (z[i].x + z[i].y) + (z[i].z + z[i].w);
    float s1 = (z[i].x * z[i].x + z[i].y * z[i].y) + (z[i].z * z[i].z + z[i].w * z[i].w);
    float s = red32(s0, s1);
    *(float*)(lds + L_SP + (w * 80 + 16 * i + n) * 8 + (kg >> 1) * 4) = red16(s, s);
  }
}
DI void rs_pub(unsigned char* lds, rsrc_t ws, u32 base, int tid, u32 tA, u32 tB) {
  const int r = tid < 80 ? tid : 0;
  f32x2 a = *(const f32x2*)(lds + L_SP + r * 8), b = *(const f32x2*)(lds + L_SP + (80 + r) * 8);
  f32x2 e = *(const f32x2*)(lds + L_SP + (160 + r) * 8), f = *(const f32x2*)(lds + L_SP + (240 + r) * 8);
  a = (a + b) + (e + f);
  stc(ws, tid < 80 ? base + tid * 16 : OOB, u32x4{__float_as_uint(a.x), tA, __float_as_uint(a.y), tB});
}
DI u32 rs_off(int w, int lane) {
  const int l3 = lane < 60 ? lane : 0, th = l3 / 20;
  return (240 * th + 20 * w + l3 - 20 * th) * 16;
}
template <bool PRE>
DI void rs_get(unsigned char* lds, int lout, rsrc_t ws, u32 base, int w, int lane, u32x4 (&v)[3], u32 tA, u32 tB, unsigned& budget) {
  u32 off[3]; bool need[3];
#pragma unroll
  for (int j = 0; j < 3; ++j) { off[j] = base + rs_off(w, lane) + j * 1280; need[j] = true; }
  gather<3, PRE>(ws, off, need, v, tA, tB, budget);
  float s0 = __uint_as_float(v[0].x) + __uint_as_float(v[1].x) + __uint_as_float(v[2].x);
  float s1 = __uint_as_float(v[0].z) + __uint_as_float(v[1].z) + __uint_as_float(v[2].z);
  s0 = sum3(s0, lane) * (1.f / D);
  s1 = sum3(s1, lane) * (1.f / D);
  if (lane < 20) *(f32x2*)(lds + lout + (20 * w + lane) * 8) = f32x2{__builtin_amdgcn_rsqf(fmaxf(s1 - s0 * s0, 0.f) + 1e-5f), s0};
}
DI void norm_acc(const unsigned char* lds, int lp, int kg, f32x4 (&acc)[5][2], float rsr, float rsi) {
#pragma unroll
  for (int i = 0; i < 5; ++i) {
    f32x4 pa = *(const f32x4*)(lds + lp + (20 * kg + 4 * i) * 8), pb = *(const f32x4*)(lds + lp + (20 * kg + 4 * i) * 8 + 16);
    acc[i][0].x = (acc[i][0].x - pa.y * rsr) * pa.x; acc[i][1].x = (acc[i][1].x - pa.y * rsi) * pa.x;
    acc[i][0].y = (acc[i][0].y - pa.w * rsr) * pa.z; acc[i][1].y = (acc[i][1].y - pa.w * rsi) * pa.z;
    acc[i][0].z = (acc[i][0].z - pb.y * rsr) * pb.x; acc[i][1].z = (acc[i][1].z - pb.y * rsi) * pb.x;
    acc[i][0].w = (acc[i][0].w - pb.w * rsr) * pb.z; acc[i][1].w = (acc[i][1].w - pb.w * rsi) * pb.z;
  }
}
__global__ void __launch_bounds__(256, 1) fused_kernel(Params p) {
  extern __shared__ __attribute__((aligned(16))) unsigned char lds[];
  int tid = threadIdx.x, lane = tid & 63;
  const int w = __builtin_amdgcn_readfirstlane(tid >> 6);
  const int xcd = blockIdx.x & 7, slot = blockIdx.x >> 3;
  int c, g, spare = -1;
  if (slot < 27) { c = xcd * 3 + slot / 9; g = slot % 9; }
  else {
    int e = (xcd & 3) * 5 + (slot - 27);
    if (xcd < 4) { int e2 = (xcd & 1) * 5 + (slot - 27); c = 24 + (xcd >> 1); g = e2; if (e2 >= 9) spare = 20 + (xcd >> 1); }
    else { spare = e; c = 0; g = e >> 1; }
  }
#define DUMP() do {} while (0)
  STAMP(0);
  {
    u64 pc; u32 len;
    asm volatile("s_getpc_b64 %0\n.Lpf%=:\n\ts_mov_b32 %1, .Lkend_mark-.Lpf%=" : "=s"(pc), "=s"(len));
    const u32 o = (((slot & 15) * 4 + w) * 64 + lane) * 16;
    asm volatile("s_mov_b32 m0, %2\n\ts_nop 0\n\tglobal_load_lds_dwordx4 %0, %1" :: "v"((int)o < (int)len ? o : 0u), "s"(pc & ~15ull),
                 "s"((u32)(size_t)(lds_void*)lds + L_TOT - 1024) : "memory");
  }
  const rsrc_t ws = __builtin_amdgcn_make_buffer_rsrc(p.ws, 0, W_END, 0x00020000);
  u32 ep = *(const __attribute__((address_space(4))) u32*)p.ws;
#define TAGS() asm volatile("" : "+s"(ep)); const u32 tA = ep + 1u; \
  const u32 tB = tA * 0x9E3779B1u + 0x7F4A7C15u
  unsigned budget = SPIN_MAX;
  int n = lane & 15, kg = lane >> 4;
#define PHASE() do { asm volatile("" : "+v"(tid)); lane = tid & 63; n = lane & 15; kg = lane >> 4; pch = 64 * g + 16 * w + n; } while (0)

  if (spare >= 0) {
    auto warm = [&]() {
      constexpr u32 NA = (W_ST + 3 * NC * 9 * 1280) / 128;
#pragma unroll
      for (int r = 0; r < 11; ++r) {
        const u32 L = (r * 4 + spare - 18) * 256 + tid, o = L < NA ? L * 128 : OOB;
        asm volatile("s_mov_b32 m0, %2\n\ts_nop 0\n\tbuffer_load_dword %0, %1, 0 offen sc1 lds"
                     :: "v"(o), "s"(ws), "s"((u32)(size_t)(lds_void*)lds + L_TOT - 256) : "memory");
      }
    };
    if (spare >= 18) { warm(); drain(); return; }
    const int ch0 = 64 * g + 52 + 6 * (spare & 1);
#pragma unroll
    for (int i = 0; i < 6; ++i) {
      const u16* kp = p.sk + (ch0 + i) * S + 8 * tid;
      u32x4 kx = ldg16(kp);
      u32 kn = tid < 255 ? kp[8] : 0;
      conv_stage_k(lds + i * CVSZ, kx, kn, tid);
    }
    u32 sd0 = p.sd[ch0 + (w >> 1)], sd1 = p.sd[ch0 + 2 + (w >> 1)], sd2 = p.sd[ch0 + 4 + (w >> 1)];
    TAGS();
    u32x4 kq[3][4];
    conv_gather<false>(lds, ws, ch0, tid, kq[0], tA, tB, budget);
#pragma unroll
    for (int r = 4; r < 12; ++r) kq[r >> 2][r & 3] = ldc(ws, W_KVT + ch0 * 8192 + (tid + 256 * r) * 16);
    conv_gather<true>(lds + 2 * CVSZ, ws, ch0 + 2, tid, kq[1], tA, tB, budget);
    conv_gather<true>(lds + 4 * CVSZ, ws, ch0 + 4, tid, kq[2], tA, tB, budget);
#pragma unroll 1
    for (int r = 0; r < 3; ++r) {
      const float sdv = lo16(r == 0 ? sd0 : r == 1 ? sd1 : sd2);
      bar();
      f32x4 acc[8];
#pragma unroll
      for (int j = 0; j < 8; ++j) acc[j] = f32x4{0, 0, 0, 0};
      conv_mma<0, 4>(lds, 2 * r * CVSZ, w, lane, acc, [](int, int) {});
      conv_finish(lds, lds + 2 * r * CVSZ, ws, ch0 + 2 * r, sdv, w, lane, acc, tA, tB);
    }
    asm volatile(".Lkend_mark:");
    STAMP(15);
    DUMP();
    return;
  }

  const int t0 = 80 * c;
  const u32 lb = (u32)(size_t)(lds_void*)lds;
  u32 xo[8];
  {
    const int r = (lane >> 2) & 3, jj = lane & 3;
#pragma unroll
    for (int i = 0; i < 5; ++i) {
      int t = t0 + 20 * w + 4 * i + r; t = t > S - 1 ? S - 1 : t;
      xo[i] = (t * D + 32 * kg + 8 * jj) * 2;
    }
#pragma unroll
    for (int u = 0; u < 3; ++u) {
      int t = t0 + 20 * w + 8 * u + 4 * (kg >> 1) + r; t = t > S - 1 ? S - 1 : t;
      xo[5 + u] = (t * D + 512 + 32 * (kg & 1) + 8 * jj) * 2;
    }
  }
  auto xdma = [&](int pc) {
    const u32 la = lb + L_A + w * RB + (pc >> 1) * 4096;
    if (pc & 1) { dma_g<512>(p.x, xo[pc >> 1], la + 2048); dma_g<768>(p.x, xo[pc >> 1], la + 3072); }
    else { dma_g<0>(p.x, xo[pc >> 1], la); dma_g<256>(p.x, xo[pc >> 1], la + 1024); }
    if (pc < 3) dma_g<0>(p.x, xo[5 + pc], lb + L_A + w * RB + 20480 + pc * 1024);
  };
  STAMP(26);
  u32x2 kk[8], vv[5];
  u32x4 shw[2];
  {
    const int col = 64 * g + 4 * (tid & 15), r0 = t0 + 5 * (tid >> 4);
#pragma unroll
    for (int j = 0; j < 8; ++j) { int t = r0 + j - 3; t = t < 0 ? 0 : (t > S - 1 ? S - 1 : t); kk[j] = ldg8(p.k + t * D + col); }
#pragma unroll
    for (int j = 0; j < 5; ++j) { int t = r0 + j; t = t > S - 1 ? S - 1 : t; vv[j] = ldg8(p.v + t * D + col); }
    shw[0] = ldg16(p.shk + col * 4); shw[1] = ldg16(p.shk + col * 4 + 8);
  }
  const int ch0 = 64 * g + 2 * c;
  u32x4 kx0 = ldg16(p.sk + ch0 * S + 8 * tid), kx1 = ldg16(p.sk + (ch0 + 1) * S + 8 * tid);
  u32 kn0 = p.sk[ch0 * S + 8 * tid + (tid < 255 ? 8 : 0)], kn1 = p.sk[(ch0 + 1) * S + 8 * tid + (tid < 255 ? 8 : 0)];
  int pch = 64 * g + 16 * w + n;
  u32 lre_h = p.lre[pch], lim_h = p.lim[pch], ldt_h = p.ldt[pch];
  __builtin_amdgcn_sched_barrier(0);
  u32x4 Bw[18][2];
  const int bo = (64 * g + 16 * w + (n & 12)) * D + 8 * (lane & 3);
  const u16 *br = p.bre + bo + 32 * kg, *bi = p.bim + bo + 32 * kg;
  const u16* bt = (kg < 2 ? p.bre : p.bim) + bo + 512 + 32 * (kg & 1);
  auto bld = [&](int G) {
    SB();
#pragma unroll
    for (int j = 0; j < 4; ++j) {
      if (G < 8) Bw[4 * (G >> 1) + j][G & 1] = ldg16(((G & 1) ? bi : br) + j * D + 128 * (G >> 1));
      else Bw[16 + (j >> 1)][j & 1] = ldg16(bt + j * D);
    }
    SB();
  };
#pragma unroll
  for (int G = 0; G < 5; ++G) bld(G);
  int twa = L_HI + w * 5376 + lane * 16, tra = L_HI + w * 5376 + (n & 3) * 1040 + (n >> 2) * 64;
  auto bltr = [&](int G) {
    const int i = G >> 1, m = G & 1;
    if (G < 8) ltr<16, 32, 48>(lds, twa, tra + kg * 256, Bw[4 * i][m], Bw[4 * i + 1][m], Bw[4 * i + 2][m], Bw[4 * i + 3][m]);
    else ltr<512, 32, 544>(lds, twa, tra + (kg & 1) * 256 + (kg >> 1) * 16, Bw[16][0], Bw[16][1], Bw[17][0], Bw[17][1]);
  };
  STAMP(20);
  {
    const int r0 = 5 * (tid >> 4);
    const bool first = (t0 + r0 == 0);
    asm volatile("" : "+v"(kk[0]), "+v"(kk[1]), "+v"(kk[2]), "+v"(kk[3]), "+v"(kk[4]), "+v"(kk[5]), "+v"(kk[6]), "+v"(kk[7]),
                 "+v"(vv[0]), "+v"(vv[1]), "+v"(vv[2]), "+v"(vv[3]), "+v"(vv[4]), "+v"(shw[0]), "+v"(shw[1]));
    asm volatile("" : "+v"(kx0), "+v"(kx1), "+v"(kn0), "+v"(kn1), "+v"(lre_h), "+v"(lim_h), "+v"(ldt_h));
    u16* img = (u16*)(lds + L_GI) + 4 * (tid & 15) * 84 + r0;
#pragma unroll
    for (int h = 0; h < 2; ++h) {
      f32x2 sw[4], kf[8];
      sw[0] = f32x2{lo16(shw[h].x), lo16(shw[h].z)}; sw[1] = f32x2{hi16(shw[h].x), hi16(shw[h].z)};
      sw[2] = f32x2{lo16(shw[h].y), lo16(shw[h].w)}; sw[3] = f32x2{hi16(shw[h].y), hi16(shw[h].w)};
#pragma unroll
      for (int j = 0; j < 8; ++j) {
        u32 q = kk[j][h];
        if (j < 3) q = first ? 0u : q;
        kf[j] = f32x2{lo16(q), hi16(q)};
      }
#pragma unroll
      for (int r = 0; r < 5; ++r) {
        SB();
        if (h == 0) bltr(r);
        else { xdma(2 * r); xdma(2 * r + 1); }
        SB();
        f32x2 a = kf[r] * sw[3];
#pragma unroll
        for (int j = 1; j < 4; ++j) a = __builtin_elementwise_fma(kf[r + j], sw[3 - j], a);
        a *= f32x2{lo16(vv[r][h]), hi16(vv[r][h])};
        const u32 o = pk(a.x, a.y);
        img[(2 * h) * 84 + r] = (u16)o;
        img[(2 * h + 1) * 84 + r] = (u16)(o >> 16);
      }
    }
  }
  STAMP(21);
  bld(5);
  conv_stage_k(lds + L_CV, kx0, tid < 255 ? kn0 : 0, tid);
  conv_stage_k(lds + L_CV + CVSZ, kx1, tid < 255 ? kn1 : 0, tid);
  STAMP(30);
  bld(6);
  Scan sc;
  {
    float lr = lo16(lre_h), li = lo16(lim_h), dt = rb(expf(lo16(ldt_h)));
    float e = expf(dt * lr), sn, cs;
    sincosf(dt * li, &sn, &cs);
    bld(7);
    sc.ar = e * cs; sc.ai = e * sn;
    float nr = sc.ar - 1.f, ni = sc.ai, dn = lr * lr + li * li;
    sc.cr = 2.f * ((nr * lr + ni * li) / dn); sc.ci = 2.f * ((ni * lr - nr * li) / dn);
    bld(8);
    float r2, i2, r4, i4, r5, i5, r80, i80, r160, i160, r60, i60, r240, i240;
    cmul(sc.ar, sc.ai, sc.ar, sc.ai, r2, i2); cmul(r2, i2, r2, i2, r4, i4); cmul(r4, i4, sc.ar, sc.ai, r5, i5);
    cmul(r5, i5, r5, i5, r2, i2); cmul(r2, i2, r2, i2, sc.a20r, sc.a20i);
    cmul(sc.a20r, sc.a20i, sc.a20r, sc.a20i, sc.a40r, sc.a40i);
    cmul(sc.a40r, sc.a40i, sc.a20r, sc.a20i, r60, i60);
    cmul(sc.a40r, sc.a40i, sc.a40r, sc.a40i, r80, i80);
    cmul(r80, i80, r80, i80, r160, i160); cmul(r160, i160, r80, i80, r240, i240);
    cmul(r160, i160, r160, i160, sc.a320r, sc.a320i);
    sc.aqr = kg == 0 ? 1.f : kg == 1 ? sc.a20r : kg == 2 ? sc.a40r : r60;
    sc.aqi = kg == 0 ? 0.f : kg == 1 ? sc.a20i : kg == 2 ? sc.a40i : i60;
    sc.bqr = kg == 0 ? 1.f : kg == 1 ? r80 : kg == 2 ? r160 : r240;
    sc.bqi = kg == 0 ? 0.f : kg == 1 ? i80 : kg == 2 ? i160 : i240;
  }
  STAMP(28);
  asm volatile("s_waitcnt vmcnt(16)" ::: "memory");
  {
    const u32 ones = 0x3f803f80u;
#pragma unroll
    for (int i = 0; i < 5; ++i) {
      u32x4 xv[5];
#pragma unroll
      for (int q = 0; q < 4; ++q) xv[q] = *(const u32x4*)(lds + L_A + w * RB + (i * 4 + q) * 1024 + lane * 16);
      xv[4] = *(const u32x4*)(lds + L_A + w * RB + 20480 + i * 512 + (lane & 31) * 16);
      SB();
      if (i > 0) bltr(4 + i);
      SB();
      float s1 = 0.f, s2 = 0.f;
#pragma unroll
      for (int e = 0; e < 4; ++e) { s1 = dot2(xv[4][e], ones, s1); s2 = dot2(xv[4][e], xv[4][e], s2); }
      s1 *= 0.5f; s2 *= 0.5f;
#pragma unroll
      for (int q = 0; q < 4; ++q)
#pragma unroll
        for (int e = 0; e < 4; ++e) { s1 = dot2(xv[q][e], ones, s1); s2 = dot2(xv[q][e], xv[q][e], s2); }
      float sm = red32(s1, s2);
      sm = quad_sum(red16(sm, sm));
      *(float*)(lds + L_SP + (20 * w + 4 * i + (n >> 2)) * 8 + (kg >> 1) * 4) = sm;
      SB();
    }
    asm volatile("s_waitcnt lgkmcnt(0)" ::: "memory");
    if (lane < 20) {
      f32x2 v = *(const f32x2*)(lds + L_SP + (20 * w + lane) * 8);
      float m = v.x * (1.f / D), var = fmaxf(v.y * (1.f / D) - m * m, 0.f);
      float den = rb(sqrtf(rb(rb(var) + 1e-5f)));
      *(f32x2*)(lds + L_P1 + (20 * w + lane) * 8) = f32x2{1.f / den, rb(m)};
    }
  }
#pragma unroll
  for (int ks = 0; ks < 18; ++ks) asm volatile("" : "+a"(Bw[ks][0]), "+a"(Bw[ks][1]));
  bar();
  STAMP(1);
  TAGS();
  const u64 tF = ((u64)tB << 32) | tA;
  f32x4 acc[5][2];
  float rsr, rsi;
  u32x4 kq[4];
  u32x4 Cw[9][4];
  const rsrc_t crs = __builtin_amdgcn_make_buffer_rsrc((void*)((w & 1) ? p.cim : p.cre), 0, D * D * 2, 0x00020000);
  auto cld = [&](int L) {
    const int d = L >> 3, j = L & 3, cb = ((64 * g + (n & 12)) * D + 288 * (w >> 1) + 8 * (lane & 3)) * 2 + j * D * 2;
    if (L >= 32) Cw[8][j] = __builtin_amdgcn_raw_buffer_load_b128(crs, cb + 64 * ((8 + g) % 9) + ((w + kg) & 3) * 32 * D, 0, 0);
    else Cw[L & 7][d] = __builtin_amdgcn_raw_buffer_load_b128(crs, cb + 64 * (((L & 4) + kg + g) % 9), ((w + d) & 3) * 32 * D, 0);
  };
  {
    u32x2 kvq;
    u32 kvo;
    SB();
    STAMP(23);
    gemm_bu<true>(lds, Bw, lane, acc, rsr, rsi, [&](int ks, int i) {
      if (ks < 5 && i == 0) {
        const int it = tid + 256 * ks, ch = it / 20, tq = it - 20 * ch, gq = 20 * c + tq;
        kvq = *(const u32x2*)(lds + L_GI + ch * 168 + tq * 8);
        kvo = gq < 512 ? W_KVT + (64 * g + ch) * 8192 + (511 - gq) * 16 : OOB;
      }
      if (ks < 5 && i == 4)
        stc(ws, kvo, u32x4{__builtin_amdgcn_alignbit(kvq.y, kvq.y, 16), tA, __builtin_amdgcn_alignbit(kvq.x, kvq.x, 16), tB});
      if (ks >= 6 && i == 2) cld(ks - 6);
      if (ks == 16 && i < 4) kq[i] = ldc(ws, W_KVT + ch0 * 8192 + (tid + 256 * i) * 16);
    });
  }
  STAMP(2);
  norm_acc(lds, L_P1, kg, acc, rsr, rsi);
  *(f32x2*)(lds + L_RS + tid * 8) = f32x2{rsr, rsi};
  float inr, ini;
  scan_a(sc, acc, ws, W_E + (c * D + pch) * 16, lane, tA, tB, inr, ini);
  STAMP(3);
  u32x2 dsk_h = ldg8(p.dsk + 64 * g + 16 * w + 4 * kg);
  const u16 sd_h = p.sd[ch0 + (w >> 1)];
  conv_gather<true>(lds + L_CV, ws, ch0, tid, kq, tA, tB, budget);
  bar();
  STAMP(4);
  f32x4 cacc[8];
  u32x4 ev[7];
#pragma unroll
  for (int j = 0; j < 8; ++j) cacc[j] = f32x4{0, 0, 0, 0};
  conv_mma<0, 2>(lds, L_CV, w, lane, cacc, [&](int r, int b) {
    if (b < 6) cld(12 + 6 * r + b);
    if (r == 1 && b == 2) {
#pragma unroll
      for (int j = 0; j < 7; ++j) { const int cp = c - 1 - kg - 4 * j; ev[j] = ldc(ws, W_E + pch * 16 + (cp >= 0 ? cp : 0) * (D * 16)); }
    }
  });
  STAMP(5);
  scan_b<true>(sc, acc, lds, ws, W_E + pch * 16, W_H + (c * 36 + 4 * g + w) * 5120, c, w, lane, tA, tB, inr, ini, ev, budget);
  STAMP(6);
  {
    conv_mma<2, 4>(lds, L_CV, w, lane, cacc, [&](int r, int b) {
      if (b < 6) cld(12 + 6 * r + b);
      if (r == 3 && b == 7) {
        const u32 o = W_U3 + c * 94208 + w * 23552 + (lane >> 2) * (g < 8 ? 4096 : 512) + (g < 8 ? g * 512 : 20480) + (lane & 3) * 128;
        asm volatile("s_mov_b32 m0, %2\n\ts_nop 0\n\tbuffer_load_dword %0, %1, 0 offen sc1 lds"
                     :: "v"(lane < 20 ? o : OOB), "s"(ws), "s"(lb + L_TOT - 256) : "memory");
      }
      if (r == 2 && b == 7) {
        asm volatile("s_waitcnt vmcnt(6)\n\ts_barrier" ::: "memory");
        stc2(ws, tid == 0 ? W_FLG + c * 128 + g * 8 : OOB, u32x2{tA, tB});
      }
    });
  }
  float dk0 = lo16(dsk_h.x), dk1 = hi16(dsk_h.x), dk2 = lo16(dsk_h.y), dk3 = hi16(dsk_h.y);
  f32x4 z[5];
  auto frag_off = [&](int i, int rb) {
    const int tl = 16 * i + n, q = tl / 20, rem = tl - 20 * q;
    return q * rb + (rem >> 2) * (g < 8 ? 4096 : 512) + (g < 8 ? g * 512 : 20480) + (w >> 1) * 256 + (rem & 3) * 64 + (w & 1) * 32 + kg * 8;
  };
#pragma unroll
  for (int i = 0; i < 5; ++i) {
    u32x2 xq = *(const u32x2*)(lds + L_A + frag_off(i, RB));
    f32x2 pr = *(const f32x2*)(lds + L_P1 + (16 * i + n) * 8);
    const f32x4 d = f32x4{lo16(xq.x), hi16(xq.x), lo16(xq.y), hi16(xq.y)} - pr.y;
    z[i] = d + d * pr.x * f32x4{dk0, dk1, dk2, dk3};
    asm volatile("" : "+v"(z[i]));
  }
  conv_finish(lds, lds + L_CV, ws, ch0, b2f(sd_h), w, lane, cacc, tA, tB);
  *(f32x4*)(lds + L_CV + tid * 16) = f32x4{sc.ar, sc.ai, sc.cr, sc.ci};
  *(f32x4*)(lds + L_CV + 4096 + tid * 16) = f32x4{sc.a20r, sc.a20i, sc.a40r, sc.a40i};
  *(f32x4*)(lds + L_CV + CVSZ + tid * 16) = f32x4{sc.aqr, sc.aqi, sc.bqr, sc.bqi};
  *(f32x2*)(lds + L_CV + CVSZ + 4096 + tid * 8) = f32x2{sc.a320r, sc.a320i};
  *(u32x2*)(lds + L_CV + CVSZ + 6144 + tid * 8) = dsk_h;
  STAMP(7);
  PHASE();
  twa = L_HI + w * 5376 + lane * 16; tra = L_HI + w * 5376 + (n & 3) * 1040 + (n >> 2) * 64;
  auto cltr = [&](int G) {
    if (G < 8) ltr<16, 32, 48>(lds, twa, tra + kg * 256, Cw[4 * (G & 1)][G >> 1], Cw[4 * (G & 1) + 1][G >> 1], Cw[4 * (G & 1) + 2][G >> 1], Cw[4 * (G & 1) + 3][G >> 1]);
    else ltr<256, 512, 768>(lds, twa, tra + kg * 16, Cw[8][0], Cw[8][1], Cw[8][2], Cw[8][3]);
  };
  {
    const u64 pre = poll_flags(p.ws, W_FLG + c * 128, lane);
    SB();
    cltr(0); cltr(1); cltr(2);
    SB();
    STAMP(24);
    wait_flags(p.ws, W_FLG + c * 128, tF, lane, budget, pre);
  }
  STAMP(8);
  u32 gq[5][2];
  {
    u32 off[5]; bool need[5]; u32x4 v[5];
    auto goff = [&]() {
#pragma unroll
      for (int r = 0; r < 5; ++r) {
        const int it = lane + 64 * r, ch = it / 20, tq = it - 20 * ch, q = 20 * c + tq;
        need[r] = true;
        off[r] = W_GT + (64 * g + 16 * w + ch) * 8192 + (q < 512 ? q : 511) * 16;
      }
    };
    gemm_y<0>(lds, lb, ws, W_H + c * 36 * 5120, Cw, w, g, lane, z, [&](int j) {
      if (j >= 10) cltr(j - 7);
      if (j >= 12) cltr(j - 5);
      if (j == -2) STAMP(22);
      if (j == -1) STAMP(29);
      if (j == 4) STAMP(27);
      if (j == 8) STAMP(31);
      if (j != 7) return;
      goff();
#pragma unroll
      for (int r = 0; r < 5; ++r) v[r] = ldc(ws, off[r]);
    });
    STAMP(9);
    PHASE();
    rs_part(lds, z, w, lane);
    bar();
    rs_pub(lds, ws, W_ST + (c * 9 + g) * 1280, tid, tA, tB);
    goff();
    gather<5, true>(ws, off, need, v, tA, tB, budget);
#pragma unroll
    for (int r = 0; r < 5; ++r) {
      const int it = lane + 64 * r, ch = it / 20, tq = it - 20 * ch;
      *(u32x2*)(lds + L_GI + (16 * w + ch) * 168 + tq * 8) = u32x2{v[r].x, v[r].z};
    }
    asm volatile("s_waitcnt lgkmcnt(0)" ::: "memory");
#pragma unroll
    for (int i = 0; i < 5; ++i) {
      u32x2 t = trread(lds, L_GI + (16 * w + 4 * kg + (n >> 2)) * 168 + 8 * (n & 3) + 32 * i);
      gq[i][0] = t.x; gq[i][1] = t.y;
    }
  }
  STAMP(10);
  u32x4 sv[3];
  rs_get<false>(lds, L_PAR, ws, W_ST + c * 9 * 1280, w, lane, sv, tA, tB, budget);
  bar();
  STAMP(11);
#pragma unroll
  for (int i = 0; i < 5; ++i) {
    f32x2 pr = *(const f32x2*)(lds + L_PAR + (16 * i + n) * 8);
    float gv[4] = {lo16(gq[i][0]), hi16(gq[i][0]), lo16(gq[i][1]), hi16(gq[i][1])};
#pragma unroll
    for (int e = 0; e < 4; ++e) { float t = z[i][e] - pr.y; z[i][e] = t + t * pr.x * gv[e]; }
    stc2(ws, W_U3 + c * 94208 + frag_off(i, 23552), u32x2{pk(z[i].x, z[i].y), pk(z[i].z, z[i].w)});
  }
  SB();
  rs_part(lds, z, w, lane);
  drain();
  bar();
  stc2(ws, tid == 0 ? W_FLG + (NC + c) * 128 + g * 8 : OOB, u32x2{tA, tB});
  rs_pub(lds, ws, W_ST + ((NC + c) * 9 + g) * 1280, tid, tA, tB);
  __builtin_amdgcn_s_sleep(5);
  wait_flags(p.ws, W_FLG + (NC + c) * 128, tF, lane, budget, tF + 1);
  STAMP(12);
  PHASE();
  const u32 ub = W_U3 + c * 94208 + w * 23552 + lane * 16;
  auto udma = [&](int j) {
    const int o = j < 20 ? (j % 5) * 4096 + (j / 5) * 1024 : j * 1024;
    dma_c(ws, ub, o, lb + L_A + w * RB + o);
  };
#pragma unroll
  for (int j = 0; j < 10; ++j) udma(j);
  asm volatile("s_waitcnt vmcnt(5)" ::: "memory");
  bar();
  STAMP(13);
#pragma unroll
  for (int i = 0; i < 5; ++i) asm volatile("" : "+v"(gq[i][0]), "+v"(gq[i][1]));
  gemm_bu<false>(lds, Bw, lane, acc, rsr, rsi, [&](int ks, int i) {
    if (ks == 12 && i < 3) sv[i] = ldc(ws, W_ST + (NC + c) * 9 * 1280 + rs_off(w, lane) + i * 1280);
    if (5 * ks + i < 13) udma(10 + 5 * ks + i);
    if (i == 4 && ks == 2) asm volatile("s_waitcnt vmcnt(13)\n\ts_barrier" ::: "memory");
    if (i == 4 && ks == 6) asm volatile("s_waitcnt vmcnt(8)\n\ts_barrier" ::: "memory");
    if (i == 4 && (ks == 10 || ks == 14)) asm volatile("s_waitcnt vmcnt(3)\n\ts_barrier" ::: "memory");
  });
  STAMP(25);
  PHASE();
  rs_get<true>(lds, L_P3, ws, W_ST + (NC + c) * 9 * 1280, w, lane, sv, tA, tB, budget);
  bar();
  {
    const f32x2 q = *(const f32x2*)(lds + L_RS + tid * 8);
    norm_acc(lds, L_P3, kg, acc, q.x, q.y);
  }
  {
    f32x4 a = *(const f32x4*)(lds + L_CV + tid * 16), b = *(const f32x4*)(lds + L_CV + 4096 + tid * 16);
    f32x4 e = *(const f32x4*)(lds + L_CV + CVSZ + tid * 16);
    f32x2 f = *(const f32x2*)(lds + L_CV + CVSZ + 4096 + tid * 8);
    sc.ar = a.x; sc.ai = a.y; sc.cr = a.z; sc.ci = a.w; sc.a20r = b.x; sc.a20i = b.y; sc.a40r = b.z; sc.a40i = b.w;
    sc.aqr = e.x; sc.aqi = e.y; sc.bqr = e.z; sc.bqi = e.w; sc.a320r = f.x; sc.a320i = f.y;
  }
  scan_a(sc, acc, ws, W_E + ((NC + c) * D + pch) * 16, lane, tA, tB, inr, ini);
  STAMP(14);
  SB();
  dsk_h = *(const u32x2*)(lds + L_CV + CVSZ + 6144 + tid * 8);
#pragma unroll
  for (int i = 0; i < 5; ++i) {
    f32x2 pr = *(const f32x2*)(lds + L_P3 + (16 * i + n) * 8);
    const f32x4 t = z[i] - pr.y;
    z[i] = t + t * pr.x * f32x4{lo16(dsk_h.x), hi16(dsk_h.x), lo16(dsk_h.y), hi16(dsk_h.y)};
#pragma unroll
    for (int r = 0; r < 4; ++r) {
      const float a = acc[i][0][r], b = acc[i][1][r];
      acc[i][0][r] = sc.cr * a - sc.ci * b; acc[i][1][r] = -(sc.cr * b) - sc.ci * a;
    }
    asm volatile("" : "+v"(acc[i][0]), "+v"(acc[i][1]), "+v"(z[i]));
  }
  SB();
  scan_b<false>(sc, acc, lds, ws, W_E + (NC * D + pch) * 16, W_H + ((NC + c) * 36 + 4 * g + w) * 5120, c, w, lane, tA, tB, inr, ini, ev, budget);
  drain();
  bar();
  stc2(ws, tid == 0 ? W_FLG + (2 * NC + c) * 128 + g * 8 : OOB, u32x2{tA, tB});
  STAMP(15);
  __builtin_amdgcn_s_sleep(5);
  wait_flags(p.ws, W_FLG + (2 * NC + c) * 128, tF, lane, budget, tF + 1);
  STAMP(16);
  {
    gemm_y<0>(lds, lb, ws, W_H + (NC + c) * 36 * 5120, Cw, w, g, lane, z, [](int) {});
    rs_part(lds, z, w, lane);
  }
  bar();
  STAMP(17);
  rs_pub(lds, ws, W_ST + ((2 * NC + c) * 9 + g) * 1280, tid, tA, tB);
  f32x4 zc[5], gf[5];
#pragma unroll
  for (int i = 0; i < 5; ++i) {
    zc[i] = z[i] + (*(const float*)(lds + L_P1 + (16 * i + n) * 8 + 4) + *(const float*)(lds + L_PAR + (16 * i + n) * 8 + 4) +
                    *(const float*)(lds + L_P3 + (16 * i + n) * 8 + 4));
    gf[i] = f32x4{lo16(gq[i][0]), hi16(gq[i][0]), lo16(gq[i][1]), hi16(gq[i][1])};
    asm volatile("" : "+v"(zc[i]), "+v"(gf[i]));
  }
  rs_get<false>(lds, L_P2, ws, W_ST + (2 * NC + c) * 9 * 1280, w, lane, sv, tA, tB, budget);
  bar();
  STAMP(18);
  const rsrc_t ors = __builtin_amdgcn_make_buffer_rsrc(p.out, 0, S * D * 4, 0x00020000);
#pragma unroll
  for (int i = 0; i < 5; ++i) {
    f32x2 pr = *(const f32x2*)(lds + L_P2 + (16 * i + n) * 8);
    const f32x4 o = zc[i] + (z[i] - pr.y) * pr.x * gf[i];
    const int t = t0 + 16 * i + n;
    __builtin_amdgcn_raw_buffer_store_b128(__builtin_bit_cast(u32x4, o), ors, (t * D + 64 * g + 16 * w + 4 * kg) * 4, 0, 19);
  }
  if (c == NC - 1 && g == 0 && tid == 0) __hip_atomic_store((gu32*)p.ws, tA, __ATOMIC_RELAXED, __HIP_MEMORY_SCOPE_AGENT);
  STAMP(19);
  DUMP();
}

extern "C" void kernel_launch(void* const* d_in, const int* in_sizes, int n_in, void* d_out, int out_size, void* d_ws,
                              size_t ws_size, hipStream_t stream) {
  Params p;
  p.x = (const u16*)d_in[0]; p.lre = (const u16*)d_in[1]; p.lim = (const u16*)d_in[2];
  p.bre = (const u16*)d_in[3]; p.bim = (const u16*)d_in[4]; p.cre = (const u16*)d_in[5]; p.cim = (const u16*)d_in[6];
  p.ldt = (const u16*)d_in[7]; p.dsk = (const u16*)d_in[8]; p.k = (const u16*)d_in[10]; p.v = (const u16*)d_in[11];
  p.shk = (const u16*)d_in[12]; p.sk = (const u16*)d_in[13]; p.sd = (const u16*)d_in[14];
  p.out = (float*)d_out; p.ws = (unsigned char*)d_ws;
  (void)hipFuncSetAttribute((const void*)fused_kernel, hipFuncAttributeMaxDynamicSharedMemorySize, L_TOT);
  hipLaunchKernelGGL(fused_kernel, dim3(256), dim3(256), L_TOT, stream, p);
}
```
